```python
import torch

def _softmax(x, axis=-1):
    return torch.softmax(x, dim=axis)
def _gelu(x):
    return torch.nn.functional.gelu(x, approximate="tanh")
def _silu(x):
    return torch.nn.functional.silu(x)
def _sigmoid(x):
    return torch.sigmoid(x)
def _layernorm(x, axis=-1, eps=1e-5):
    m = x.mean(dim=axis, keepdim=True)
    v = x.var(dim=axis, keepdim=True, unbiased=False)
    return (x - m) / torch.sqrt(v + eps)
def _scatter_add(tgt, idx, src, axis=0):
    return tgt.clone().index_add_(axis, idx, src)
def _segment_sum(src, seg, n):
    out = torch.zeros((n,) + src.shape[1:], dtype=src.dtype)
    return out.index_add_(0, seg, src)
def _pad(x, pw):
    flat = tuple(v for pair in reversed(pw) for v in pair)
    return torch.nn.functional.pad(x, flat)

kernel_name = 'sequential_preLN__sliding_local_dilated__dense_activation_squared_relu__prompt'

def setup_inputs():

    _w = (lambda a: (lambda t: t.to(torch.bfloat16) if t.is_floating_point() else t)(torch.as_tensor(a)))
    def _A_setup():
        B, S, D = 1, 2048, 640
        return {
            "x": _w((lambda *s: torch.randn(*s, dtype=torch.bfloat16) * 0.05)(B, S, D)),
            "wqkv": _w((lambda *s: torch.randn(*s, dtype=torch.bfloat16) * 0.05)(D, 3 * D)),
            "wo": _w((lambda *s: torch.randn(*s, dtype=torch.bfloat16) * 0.05)(D, D)),
        }
    _A_in = _A_setup()

    def _B_setup():
        B, S, D, F = 1, 2048, 640, 1536
        return {
            "x": _w((lambda *s: torch.randn(*s, dtype=torch.bfloat16) * 0.05)(B, S, D)),
            "w1": _w((lambda *s: torch.randn(*s, dtype=torch.bfloat16) * 0.05)(D, F)),
            "b1": _w((lambda *s: torch.randn(*s, dtype=torch.bfloat16) * 0.4)(F)),
            "w2": _w((lambda *s: torch.randn(*s, dtype=torch.bfloat16) * 0.05)(F, D)),
            "b2": _w((lambda *s: torch.randn(*s, dtype=torch.bfloat16) * 1.6)(D)),
        }
    _B_in = _B_setup()

    return {
        "A_x": _A_in["x"],
        "A_wqkv": _A_in["wqkv"],
        "A_wo": _A_in["wo"],
        "B_x": _B_in["x"],
        "B_w1": _B_in["w1"],
        "B_b1": _B_in["b1"],
        "B_w2": _B_in["w2"],
        "B_b2": _B_in["b2"],
    }

def reference(A_x, A_wqkv, A_wo, B_x, B_w1, B_b1, B_w2, B_b2):
    def _A_ref(x, wqkv, wo):
        B, S, D = x.shape
        H = 32; Dh = D // H
        W, dil = 8, 2
        qkv = (lambda x, *d: x.permute(*d))((x @ wqkv).reshape(B, S, 3, H, Dh), 2, 0, 3, 1, 4)
        q, k, v = qkv[0], qkv[1], qkv[2]
        scale = Dh ** -0.5
        pos = torch.arange(S)
        off = torch.arange(W) * dil
        idx = pos[:, None] - off[None, :]
        mask = idx >= 0
        idx_c = torch.where(mask, idx, 0).reshape(-1)
        kg = k[:, :, idx_c, :].reshape(B, H, S, W, Dh)
        vg = v[:, :, idx_c, :].reshape(B, H, S, W, Dh)
        logits = torch.einsum("bhsd,bhswd->bhsw", q, kg) * scale
        logits = torch.where(mask[None, None, :, :], logits, -1e9)
        attn = _softmax(logits, axis=-1)
        o = torch.einsum("bhsw,bhswd->bhsd", attn, vg)
        return (lambda x, *d: x.permute(*d))(o, 0, 2, 1, 3).reshape(B, S, D) @ wo

    def _B_ref(x, w1, b1, w2, b2):
        h = torch.einsum("bsd,df->bsf", x, w1) + b1
        zero = torch.zeros_like(h)
        r = torch.where(h > zero, h, zero)
        h = r * r
        out = torch.einsum("bsf,fd->bsd", h, w2) + b2
        return out

    x = A_x
    h = x + _A_ref(_layernorm(x, axis=-1), A_wqkv, A_wo)
    h2 = h + _B_ref(_layernorm(h, axis=-1), B_w1, B_b1, B_w2, B_b2)
    return h2
```

```cpp
#include <hip/hip_runtime.h>
#include <hip/hip_bf16.h>
#include <stdint.h>

#define GAS __attribute__((address_space(1)))
#define LAS __attribute__((address_space(3)))
#define DEVI __device__ __forceinline__
typedef unsigned v4u __attribute__((ext_vector_type(4)));
typedef unsigned v2u __attribute__((ext_vector_type(2)));
typedef float f32x4 __attribute__((ext_vector_type(4)));
typedef short bf16x8 __attribute__((ext_vector_type(8)));
typedef short s16x4 __attribute__((ext_vector_type(4)));
typedef __bf16 bf16x2 __attribute__((ext_vector_type(2)));
typedef unsigned long long u64;
typedef GAS u64 gu64;
#define RLX_AGENT __ATOMIC_RELAXED, __HIP_MEMORY_SCOPE_AGENT

#define NLAUNCH 2
#define DIAG 0

constexpr int T_ = 2048, D_ = 640, F_ = 1536;
constexpr unsigned WS_NONCE = 0;
constexpr unsigned WS_F0Q = 4096;
constexpr unsigned WS_F0O = 8192;
constexpr unsigned WS_F0A = 12288;
constexpr unsigned WS_F0B = 16384;
constexpr unsigned WS_F0X = 20480;
constexpr unsigned WS_F1 = 24576;
constexpr unsigned WS_F2 = 28672;
constexpr unsigned WS_F3 = 32768;
constexpr unsigned WS_DIAG = 36864;
constexpr unsigned WS_XN = 131072;
constexpr unsigned WS_WQ = WS_XN + 128 * 20480;
constexpr unsigned WS_WO = WS_WQ + 16 * 20 * 8 * 1024;
constexpr unsigned WS_W1 = WS_WO + 8 * 20 * 5 * 1024;
constexpr unsigned WS_W2 = WS_W1 + 16 * 20 * 6 * 1024;
constexpr unsigned WS_O = WS_W2 + 8 * 48 * 5 * 1024;
constexpr unsigned WS_H = WS_O + 128 * 20480;
constexpr unsigned WS_H1 = WS_H + 128 * 20480;
constexpr unsigned WS_ST = WS_H1 + 128 * 49152;
constexpr unsigned WS_END = WS_ST + 128 * 2048;
constexpr unsigned LDS_BYTES = 163840;
constexpr unsigned L_STASH = 153600;
constexpr unsigned L_P0_LN = 65536;
constexpr unsigned L_P1_HALO = 65536;
constexpr unsigned L_P1_EX = 125952;
constexpr unsigned L_RED = 102400;
constexpr unsigned L_P3_B1 = 122880;
constexpr unsigned L_P4_B2 = 122880;

struct Params {
  const unsigned short* x; const unsigned short* wqkv; const unsigned short* wo; const unsigned short* w1; const unsigned short* b1;
  const unsigned short* w2; const unsigned short* b2; unsigned short* out; unsigned char* ws;
};

typedef __amdgpu_buffer_rsrc_t rsrc_t;
DEVI rsrc_t mk_rsrc(const void* p, unsigned bytes) { return __builtin_amdgcn_make_buffer_rsrc((void*)p, 0, bytes, 0x00020000); }
DEVI v4u mk_srd(const void* p, unsigned bytes) {
  u64 a = (u64)p; v4u r; r.x = (unsigned)a; r.y = (unsigned)(a >> 32) & 0xffffu; r.z = bytes; r.w = 0x00020000u; return r;
}
template <int AUX> DEVI void dma16(rsrc_t rs, LAS char* lds, unsigned voff, unsigned soff) {
  __builtin_amdgcn_raw_ptr_buffer_load_lds(rs, (LAS void*)lds, 16, voff, soff, 0, AUX);
}
DEVI void aload(v4u& d, v4u srd, unsigned voff, unsigned soff) {
  asm volatile("buffer_load_dwordx4 %0, %1, %2, %3 offen sc1" : "=v"(d) : "v"(voff), "s"(srd), "s"(soff) : "memory");
}
DEVI void st16(rsrc_t rs, v4u v, unsigned voff, unsigned soff) { __builtin_amdgcn_raw_buffer_store_b128(v, rs, voff, soff, 16); }
#define WAITV(n) asm volatile("s_waitcnt vmcnt(%0)" ::"i"(n) : "memory")
#define WAITV1(n, a) asm volatile("s_waitcnt vmcnt(%1)" : "+v"(a) : "i"(n) : "memory")
#define WAITV2(n, a, b) asm volatile("s_waitcnt vmcnt(%2)" : "+v"(a), "+v"(b) : "i"(n) : "memory")
#define WAITV4(n, a, b, c, d) asm volatile("s_waitcnt vmcnt(%4)" : "+v"(a), "+v"(b), "+v"(c), "+v"(d) : "i"(n) : "memory")
#define WAITL0() asm volatile("s_waitcnt lgkmcnt(0)" ::: "memory")
#define BAR() __builtin_amdgcn_s_barrier()
#define SCHED0() __builtin_amdgcn_sched_barrier(0)
DEVI void lbar() { WAITL0(); BAR(); asm volatile("" ::: "memory"); }

DEVI unsigned pk2(float a, float b) { bf16x2 p; p[0] = (__bf16)a; p[1] = (__bf16)b; return __builtin_bit_cast(unsigned, p); }
DEVI float bflo(unsigned u) { return __uint_as_float(u << 16); }
DEVI float bfhi(unsigned u) { return __uint_as_float(u & 0xffff0000u); }
DEVI bf16x8 asbf(v4u v) { return __builtin_bit_cast(bf16x8, v); }
DEVI f32x4 mfma(v4u a, v4u b, f32x4 c) { return __builtin_amdgcn_mfma_f32_16x16x32_bf16(asbf(a), asbf(b), c, 0, 0, 0); }
DEVI v4u ldsr16(LAS char* p) { return *(LAS v4u*)p; }
DEVI float xrow_max(float v) {
  unsigned u = __float_as_uint(v);
  auto a = __builtin_amdgcn_permlane32_swap(u, u, false, false); v = fmaxf(__uint_as_float(a[0]), __uint_as_float(a[1]));
  u = __float_as_uint(v);
  auto b = __builtin_amdgcn_permlane16_swap(u, u, false, false); return fmaxf(__uint_as_float(b[0]), __uint_as_float(b[1]));
}
DEVI float xrow_sum(float v) {
  unsigned u = __float_as_uint(v);
  auto a = __builtin_amdgcn_permlane32_swap(u, u, false, false); v = __uint_as_float(a[0]) + __uint_as_float(a[1]);
  u = __float_as_uint(v);
  auto b = __builtin_amdgcn_permlane16_swap(u, u, false, false); return __uint_as_float(b[0]) + __uint_as_float(b[1]);
}
DEVI float row16_sum(float v) {
  v += __uint_as_float(__builtin_amdgcn_update_dpp(0u, __float_as_uint(v), 0x128, 0xf, 0xf, false));
  v += __uint_as_float(__builtin_amdgcn_update_dpp(0u, __float_as_uint(v), 0x124, 0xf, 0xf, false));
  v += __uint_as_float(__builtin_amdgcn_update_dpp(0u, __float_as_uint(v), 0x122, 0xf, 0xf, false));
  v += __uint_as_float(__builtin_amdgcn_update_dpp(0u, __float_as_uint(v), 0x121, 0xf, 0xf, false));
  return v;
}

DEVI void publish(unsigned char* ws, unsigned off, u64 tag) { __hip_atomic_store((gu64*)(ws + off), tag, RLX_AGENT); }
DEVI void wait_flags(unsigned char* ws, unsigned laneoff, bool active, u64 tag) {
  for (unsigned sp = 0; sp < (1u << 18); ++sp) {
    u64 v = tag;
    if (active) v = __hip_atomic_load((gu64*)(ws + laneoff), RLX_AGENT);
    if (__all(v == tag)) break;
    __builtin_amdgcn_s_sleep(1);
  }
  __builtin_amdgcn_fence(__ATOMIC_ACQUIRE, "wavefront");
  asm volatile("" ::: "memory");
}

#define STAMP(id) do {} while (0)

template <int KIND>
DEVI void retile_unit(const Params& p, rsrc_t ws, LAS char* st, int lane, int a, int ks, u64 tag, bool flags) {
  constexpr int NCH = KIND == 0 ? 15 : (KIND == 2 ? 12 : 10);
  constexpr int NI = (32 * NCH + 63) / 64;
  constexpr int NT = KIND == 0 ? 8 : (KIND == 2 ? 6 : 5);
  constexpr unsigned RS = KIND == 0 ? 3840u : (KIND == 2 ? 3072u : 1280u);
  const void* src = KIND == 0 ? (const void*)p.wqkv : KIND == 1 ? (const void*)p.wo : KIND == 2 ? (const void*)p.w1 : (const void*)p.w2;
  constexpr unsigned SB = KIND == 0 ? 640u * 3840u : KIND == 1 ? 640u * 1280u : KIND == 2 ? 640u * 3072u : 1536u * 1280u;
  rsrc_t rs = mk_rsrc(src, SB);
#pragma unroll
  for (int n = 0; n < NI; ++n) {
    int q = n * 64 + lane; if (q > 32 * NCH - 1) q = 32 * NCH - 1;
    int rho = q / NCH, ch = q - rho * NCH;
    int row; unsigned col;
    if (KIND == 0) { row = 32 * ks + rho; int sec = ch / 5; col = sec * 1280 + 80 * a + 16 * (ch - sec * 5); }
    else if (KIND == 1) {
      int g = rho >> 3, t = (rho >> 2) & 1, e = rho & 3;
      row = ks < 16 ? (2 * ks + t) * 20 + 4 * g + e : (2 * (4 * (ks - 16) + g) + t) * 20 + 16 + e;
      col = 160 * a + 16 * ch;
    } else if (KIND == 2) { row = 32 * ks + rho; col = 192 * a + 16 * ch; }
    else { int g = rho >> 3, t = (rho >> 2) & 1, e = rho & 3; row = 32 * ks + 16 * t + 4 * g + e; col = 160 * a + 16 * ch; }
    dma16<0>(rs, st + n * 1024, (unsigned)row * RS + col, 0);
  }
  STAMP(17);
  BAR();
  WAITV(0);
  STAMP(18);
  unsigned dst;
  if (KIND == 0) dst = WS_WQ + (unsigned)(a * 20 + ks) * 8192;
  else if (KIND == 1) dst = WS_WO + (unsigned)(a * 20 + ks) * 5120;
  else if (KIND == 2) dst = WS_W1 + (unsigned)(a * 20 + ks) * 6144;
  else { int kh = ks / 24, s = ks - kh * 24; dst = WS_W2 + (unsigned)a * 245760 + (s >> 2) * 40960 + kh * 20480 + (s & 3) * 5120; }
  const int g = lane >> 4, q4 = (lane & 15) >> 2, p4 = lane & 3;
  constexpr int RB = NCH * 16;
  LAS char* base = st + (8 * g + q4) * RB;
#pragma unroll
  for (int t = 0; t < NT; ++t) {
    int cb;
    if (KIND == 0) {
      if (t == 0) cb = 80 + 8 * p4; else if (t == 1) cb = 120 + 8 * p4;
      else if (t == 2) cb = (p4 < 2 ? 80 : 0) + 32 + 40 * (p4 & 1);
      else if (t == 3) cb = 8 * p4; else if (t == 4) cb = 40 + 8 * p4;
      else if (t == 5) cb = 160 + 8 * p4; else if (t == 6) cb = 200 + 8 * p4;
      else cb = 160 + 32 + 40 * (p4 & 1);
    } else cb = 32 * t + 8 * p4;
    s16x4 lo = __builtin_amdgcn_ds_read_tr16_b64_v4i16((LAS s16x4*)(base + cb));
    s16x4 hi = __builtin_amdgcn_ds_read_tr16_b64_v4i16((LAS s16x4*)(base + 4 * RB + cb));
    v2u l2 = __builtin_bit_cast(v2u, lo), h2 = __builtin_bit_cast(v2u, hi);
    v4u v = {l2.x, l2.y, h2.x, h2.y};
    if (KIND == 0 && t == 7) { if ((lane & 15) >= 8) v = v4u{0u, 0u, 0u, 0u}; }
    st16(ws, v, lane * 16, dst + t * 1024);
  }
  STAMP(19);
  if (flags) {
    WAITV(0);
    STAMP(20);
    unsigned fo = KIND == 0 ? WS_F0Q + (a * 32 + ks) * 8 : KIND == 1 ? WS_F0O + (a * 32 + ks) * 8 : KIND == 2 ? WS_F0A + (a * 32 + ks) * 8 : WS_F0B + (a * 64 + ks) * 8;
    if (lane == 0) publish(p.ws, fo, tag);
  }
}

DEVI void ln_unit(const Params& p, rsrc_t ws, LAS char* lds, int lane, int w, int tb, u64 tag, bool flags) {
  const int t = lane >> 4, i = lane & 15, c = 4 * w + t;
  const GAS v4u* src = (const GAS v4u*)((const GAS char*)p.x + (size_t)(tb * 16 + c) * 1280) + i;
  v4u r[5];
#pragma unroll
  for (int n = 0; n < 5; ++n) r[n] = src[16 * n];
  STAMP(17);
  float v[40];
#pragma unroll
  for (int n = 0; n < 5; ++n)
#pragma unroll
    for (int e = 0; e < 4; ++e) { v[n * 8 + 2 * e] = bflo(r[n][e]); v[n * 8 + 2 * e + 1] = bfhi(r[n][e]); }
  float s = 0.f;
#pragma unroll
  for (int e = 0; e < 40; ++e) s += v[e];
  const float mean = row16_sum(s) * (1.f / 640.f);
  float q = 0.f;
#pragma unroll
  for (int e = 0; e < 40; ++e) { v[e] -= mean; q += v[e] * v[e]; }
  const float rstd = __builtin_amdgcn_rsqf(row16_sum(q) * (1.f / 640.f) + 1e-5f);
  LAS char* st = lds + L_P0_LN;
#pragma unroll
  for (int n = 0; n < 5; ++n) {
    v4u o;
#pragma unroll
    for (int e = 0; e < 4; ++e) o[e] = pk2(v[n * 8 + 2 * e] * rstd, v[n * 8 + 2 * e + 1] * rstd);
    *(LAS v4u*)(st + (i + 16 * n) * 272 + c * 16) = o;
  }
  STAMP(18);
  lbar();
#pragma unroll
  for (int n = 0; n < 5; ++n) {
    const int ks = w * 5 + n;
    v4u o = ldsr16(st + (4 * ks + (lane >> 4)) * 272 + (lane & 15) * 16);
    st16(ws, o, lane * 16, WS_XN + (unsigned)tb * 20480 + ks * 1024);
  }
  STAMP(19);
  if (flags) { WAITV(0); STAMP(20); if (lane == 0) publish(p.ws, WS_F0X + (tb * 4 + w) * 8, tag); }
}

DEVI void phase0(const Params& p, LAS char* lds, int lane, int w, u64 tag, bool flags) {
  rsrc_t ws = mk_rsrc(p.ws, WS_END);
  const int cu = blockIdx.x;
  LAS char* st = lds + w * 8192;
  if (cu < 128 && w < 4) { ln_unit(p, ws, lds, lane, w, cu, tag, flags); return; }
  int slot;
  if (cu < 128) slot = cu * 4 + (w - 4);
  else {
    int u = w * 128 + (cu - 128);
    if (u < 320) { retile_unit<0>(p, ws, st, lane, u & 15, u >> 4, tag, flags); return; }
    slot = 512 + (u - 320);
  }
  if (slot < 160) retile_unit<1>(p, ws, st, lane, slot & 7, slot >> 3, tag, flags);
  else if (slot < 480) { int u = slot - 160; retile_unit<2>(p, ws, st, lane, u & 15, u >> 4, tag, flags); }
  else if (slot < 864) { int u = slot - 480; retile_unit<3>(p, ws, st, lane, u & 7, u >> 3, tag, flags); }
  else BAR();
}

DEVI void phase1(const Params& p, LAS char* lds, int lane, int w, u64 tag, bool wait0) {
  const int x8 = blockIdx.x & 7, r = blockIdx.x >> 3;
  const int ti = 4 * (x8 >> 1) + (r >> 3), pr = 8 * (x8 & 1) + (r & 7);
  rsrc_t ws = mk_rsrc(p.ws, WS_END);
  const v4u srd = mk_srd(p.ws, WS_END);
  const unsigned l16 = lane * 16;
  const int blk = 8 * ti + w, hblk = ti > 0 ? 8 * ti - 1 : 0;
  if (wait0) {
    unsigned fo; bool act = lane < 56;
    if (lane < 36) { int b = (ti > 0 ? 8 * ti - 1 : 0) + (lane >> 2); if (b > 127) b = 127; if (ti == 0 && lane >= 32) b = 7; fo = WS_F0X + (b * 4 + (lane & 3)) * 8; }
    else fo = WS_F0Q + (pr * 32 + (lane - 36)) * 8;
    wait_flags(p.ws, fo, act, tag);
  }
  STAMP(2);
  const unsigned wq = WS_WQ + (unsigned)pr * 163840, xa = WS_XN + (unsigned)blk * 20480, xh = WS_XN + (unsigned)hblk * 20480;
  v4u a[20];
#define P1_ISSUE(s) do { \
    dma16<16>(ws, lds + ((s) & 3) * 16384 + (2 * w) * 1024, l16, wq + (s) * 16384 + (2 * w) * 1024); \
    dma16<16>(ws, lds + ((s) & 3) * 16384 + (2 * w + 1) * 1024, l16, wq + (s) * 16384 + (2 * w + 1) * 1024); \
    aload(a[2 * (s)], srd, l16, xa + (2 * (s)) * 1024); aload(a[2 * (s) + 1], srd, l16, xa + (2 * (s) + 1) * 1024); \
    if ((s) < 3) { int f = 8 * (s) + w; dma16<16>(ws, lds + L_P1_HALO + f * 1024, l16, xh + (f < 20 ? f : 19) * 1024); } } while (0)
  P1_ISSUE(0); P1_ISSUE(1);
  f32x4 acc[8], acch = {0.f, 0.f, 0.f, 0.f};
#pragma unroll
  for (int t = 0; t < 8; ++t) acc[t] = f32x4{0.f, 0.f, 0.f, 0.f};
  const bool hnormal = w < 3 || w >= 6;
  const int ht = w < 3 ? w : (w < 6 ? w + 2 : 0);
  v4u B[2][8], H1[2], H2[2];
#define P1_READ(ks) do { \
    const unsigned ro = (((ks) >> 1) & 3) * 16384 + ((ks) & 1) * 8192; \
    _Pragma("unroll") for (int t = 0; t < 8; ++t) B[(ks) & 1][t] = ldsr16(lds + ro + t * 1024 + l16); \
    const unsigned oW = ro + ht * 1024, oX = L_P1_HALO + (ks) * 1024; \
    H1[(ks) & 1] = ldsr16(lds + (hnormal ? oW : oX) + l16); H2[(ks) & 1] = ldsr16(lds + (hnormal ? oX : oW) + l16); } while (0)
  WAITV2(5, a[0], a[1]); BAR(); asm volatile("" ::: "memory");
  P1_READ(0);
  P1_ISSUE(2);
  SCHED0();
#pragma unroll
  for (int ks = 0; ks < 20; ++ks) {
    if (ks & 1) {
      const int j = (ks + 1) >> 1;
      if (j <= 9) {
        if (j == 1) asm volatile("s_waitcnt vmcnt(5) lgkmcnt(0)" : "+v"(a[2]), "+v"(a[3])::"memory");
        else if (j <= 8) asm volatile("s_waitcnt vmcnt(4) lgkmcnt(0)" : "+v"(a[2 * j]), "+v"(a[2 * j + 1])::"memory");
        else asm volatile("s_waitcnt vmcnt(0) lgkmcnt(0)" : "+v"(a[2 * j]), "+v"(a[2 * j + 1])::"memory");
        BAR(); asm volatile("" ::: "memory");
        if (j + 2 <= 9) P1_ISSUE(j + 2);
      }
    }
    if (ks + 1 < 20) P1_READ(ks + 1);
#pragma unroll
    for (int t = 0; t < 5; ++t) acc[t] = mfma(B[ks & 1][t], a[ks], acc[t]);
#pragma unroll
    for (int t = 5; t < 8; ++t) acc[t] = mfma(a[ks], B[ks & 1][t], acc[t]);
    acch = mfma(H1[ks & 1], H2[ks & 1], acch);
#pragma unroll
    for (int t = 0; t < 9; ++t) { __builtin_amdgcn_sched_group_barrier(0x100, 1, 0); __builtin_amdgcn_sched_group_barrier(0x8, 1, 0); }
    SCHED0();
  }
#undef P1_ISSUE
#undef P1_READ
  STAMP(3);
  v2u pk[8];
#pragma unroll
  for (int t = 0; t < 8; ++t) pk[t] = v2u{pk2(acc[t][0], acc[t][1]), pk2(acc[t][2], acc[t][3])};
  LAS char* ex = lds + L_P1_EX + lane * 8;
  {
    LAS char* mine = ex + (w + 1) * 3072;
    *(LAS v2u*)(mine) = pk[0]; *(LAS v2u*)(mine + 512) = pk[1]; *(LAS v2u*)(mine + 1024) = pk[2];
    *(LAS v2u*)(mine + 1536) = pk[5]; *(LAS v2u*)(mine + 2048) = pk[6]; *(LAS v2u*)(mine + 2560) = pk[7];
    if (w < 6) *(LAS v2u*)(ex + w * 512) = v2u{pk2(acch[0], acch[1]), pk2(acch[2], acch[3])};
  }
  lbar();
  v2u pv[6];
#pragma unroll
  for (int e = 0; e < 6; ++e) pv[e] = *(LAS v2u*)(ex + w * 3072 + e * 512);
  const int g = lane >> 4, c = lane & 15, odd = c & 1, ce = c - odd;
  const bool havePrev = (ti > 0) || (w > 0);
  const bool v1a = havePrev && (4 * g >= ce + 2), v1b = havePrev && (4 * g >= ce), v2a = ce >= 4 * g, v2b = ce - 2 >= 4 * g;
  v2u rq;
  { auto s0 = __builtin_amdgcn_permlane32_swap(pk[2].x, pk[2].x, false, false); auto s1 = __builtin_amdgcn_permlane32_swap(pk[2].y, pk[2].y, false, false);
    rq = v2u{s0[1], s1[1]}; }
  const float c2 = 0.22360679774997896f * 1.4426950408889634f;
  f32x4 OA[2], OR[2];
#pragma unroll
  for (int hh = 0; hh < 2; ++hh) {
    const bool mine = g == hh;
    v4u Q = {pk[3 + hh].x, pk[3 + hh].y, mine ? rq.x : 0u, mine ? rq.y : 0u};
    v4u Kp = {pv[hh].x, pv[hh].y, pv[2].x, pv[2].y}, Ko = {pk[hh].x, pk[hh].y, pk[2].x, pk[2].y};
    const f32x4 z = {0.f, 0.f, 0.f, 0.f};
    f32x4 S1 = mfma(Kp, Q, z), S2 = mfma(Ko, Q, z);
    const float NEG = -3.0e38f;
    float s1a = odd ? S1[1] : S1[0], s1b = odd ? S1[3] : S1[2], s2a = odd ? S2[1] : S2[0], s2b = odd ? S2[3] : S2[2];
    s1a = v1a ? s1a : NEG; s1b = v1b ? s1b : NEG; s2a = v2a ? s2a : NEG; s2b = v2b ? s2b : NEG;
    const float m = xrow_max(fmaxf(fmaxf(s1a, s1b), fmaxf(s2a, s2b)));
    float p1a = v1a ? __builtin_amdgcn_exp2f((s1a - m) * c2) : 0.f, p1b = v1b ? __builtin_amdgcn_exp2f((s1b - m) * c2) : 0.f;
    float p2a = v2a ? __builtin_amdgcn_exp2f((s2a - m) * c2) : 0.f, p2b = v2b ? __builtin_amdgcn_exp2f((s2b - m) * c2) : 0.f;
    const float inv = __builtin_amdgcn_rcpf(xrow_sum((p1a + p1b) + (p2a + p2b)));
    const unsigned sh = 16 * odd;
    v4u P = {pk2(p1a * inv, 0.f) << sh, pk2(p1b * inv, 0.f) << sh, pk2(p2a * inv, 0.f) << sh, pk2(p2b * inv, 0.f) << sh};
    v4u VA = {pv[3 + hh].x, pv[3 + hh].y, pk[5 + hh].x, pk[5 + hh].y}, VR = {pv[5].x, pv[5].y, pk[7].x, pk[7].y};
    OA[hh] = mfma(VA, P, z); OR[hh] = mfma(VR, P, z);
  }
  {
    v4u om = {pk2(OA[0][0], OA[0][1]), pk2(OA[0][2], OA[0][3]), pk2(OA[1][0], OA[1][1]), pk2(OA[1][2], OA[1][3])};
    st16(ws, om, l16, WS_O + (unsigned)blk * 20480 + pr * 1024);
    unsigned r0x = pk2(OR[0][0], OR[0][1]), r0y = pk2(OR[0][2], OR[0][3]), r1x = pk2(OR[1][0], OR[1][1]), r1y = pk2(OR[1][2], OR[1][3]);
    auto t0 = __builtin_amdgcn_permlane16_swap(r1x, r1x, false, false); auto t1 = __builtin_amdgcn_permlane16_swap(r1y, r1y, false, false);
    v4u orr = {r0x, r0y, t0[1], t1[1]};
    if (lane < 16) st16(ws, orr, l16, WS_O + (unsigned)blk * 20480 + (16 + (pr >> 2)) * 1024 + (pr & 3) * 256);
  }
  STAMP(4);
}

DEVI void park_partials(LAS char* lds, unsigned red, const f32x4 (&acc)[5], int lane, int w) {
  const int b = w & 3, kh = w >> 2, g = lane >> 4, c = lane & 15;
  LAS char* d = lds + red + kh * 21504 + (16 * b + c) * 336 + g * 16;
#pragma unroll
  for (int mt = 0; mt < 5; ++mt) *(LAS f32x4*)(d + mt * 64) = acc[mt];
}

DEVI void phase2_prefetch(const Params& p, LAS char* lds, int lane, int w, u64 tag, bool wait0) {
  const int x8 = blockIdx.x & 7, r = blockIdx.x >> 3;
  const int ti = 2 * x8 + (r >> 4), hb = (r >> 3) & 1, j = r & 7;
  rsrc_t ws = mk_rsrc(p.ws, WS_END);
  if (wait0) wait_flags(p.ws, WS_F0O + (j * 32 + lane) * 8, lane < 20, tag);
  {
    rsrc_t xs = mk_rsrc(p.x, T_ * 1280);
    for (int n = w; n < 10; n += 8) {
      int q = n * 64 + lane, row = q / 10, ch = q - row * 10;
      dma16<0>(xs, lds + L_STASH + n * 1024, (unsigned)(128 * ti + 64 * hb + row) * 1280 + 160 * j + 16 * ch, 0);
    }
  }
  for (int f = w; f < 100; f += 8) dma16<16>(ws, lds + f * 1024, lane * 16, WS_WO + (unsigned)j * 102400 + f * 1024);
}
DEVI void phase2(const Params& p, LAS char* lds, int lane, int w, u64 tag, bool wait1) {
  const int x8 = blockIdx.x & 7, r = blockIdx.x >> 3;
  const int ti = 2 * x8 + (r >> 4), hb = (r >> 3) & 1, j = r & 7;
  rsrc_t ws = mk_rsrc(p.ws, WS_END);
  const v4u srd = mk_srd(p.ws, WS_END);
  const unsigned l16 = lane * 16;
  const int b = w & 3, kh = w >> 2, blk = 8 * ti + 4 * hb + b;
  if (wait1) wait_flags(p.ws, WS_F1 + (ti * 16 + lane) * 8, lane < 16, tag);
  STAMP(6);
  v4u a[10];
#pragma unroll
  for (int s = 0; s < 10; ++s) aload(a[s], srd, l16, WS_O + (unsigned)blk * 20480 + (10 * kh + s) * 1024);
  WAITV(10); BAR(); asm volatile("" ::: "memory");
  f32x4 acc[5];
#pragma unroll
  for (int t = 0; t < 5; ++t) acc[t] = f32x4{0.f, 0.f, 0.f, 0.f};
  v4u B[2][5];
  LAS char* bb = lds + kh * 51200 + l16;
#pragma unroll
  for (int t = 0; t < 5; ++t) B[0][t] = ldsr16(bb + t * 1024);
  SCHED0();
#pragma unroll
  for (int s = 0; s < 10; ++s) {
    asm volatile("s_waitcnt vmcnt(%1)" : "+v"(a[s]) : "i"(9 - s) : "memory");
    if (s + 1 < 10) {
#pragma unroll
      for (int t = 0; t < 5; ++t) B[(s + 1) & 1][t] = ldsr16(bb + (s + 1) * 5120 + t * 1024);
    }
#pragma unroll
    for (int t = 0; t < 5; ++t) acc[t] = mfma(B[s & 1][t], a[s], acc[t]);
#pragma unroll
    for (int t = 0; t < 5; ++t) { __builtin_amdgcn_sched_group_barrier(0x100, 1, 0); __builtin_amdgcn_sched_group_barrier(0x8, 1, 0); }
    SCHED0();
  }
  STAMP(7);
  park_partials(lds, L_RED, acc, lane, w);
  lbar();
  const int hf = kh, g4 = lane >> 4, c = lane & 15, row = 16 * b + c;
  float ssum = 0.f, ssq = 0.f;
#pragma unroll
  for (int rd = 0; rd < 2; ++rd) {
    const int G = rd == 0 ? 4 * hf + g4 : 8 + hf;
    if (rd == 0 || g4 == 0) {
      LAS char* s0 = lds + L_RED + row * 336 + G * 32;
      f32x4 u0 = *(LAS f32x4*)s0, u1 = *(LAS f32x4*)(s0 + 16), w0 = *(LAS f32x4*)(s0 + 21504), w1 = *(LAS f32x4*)(s0 + 21504 + 16);
      LAS char* sx = lds + L_STASH + row * 160 + G * 16;
      v4u xv = *(LAS v4u*)sx;
      float h[8];
      h[0] = u0[0] + w0[0] + bflo(xv[0]); h[1] = u0[1] + w0[1] + bfhi(xv[0]); h[2] = u0[2] + w0[2] + bflo(xv[1]); h[3] = u0[3] + w0[3] + bfhi(xv[1]);
      h[4] = u1[0] + w1[0] + bflo(xv[2]); h[5] = u1[1] + w1[1] + bfhi(xv[2]); h[6] = u1[2] + w1[2] + bflo(xv[3]); h[7] = u1[3] + w1[3] + bfhi(xv[3]);
      v4u hv = {pk2(h[0], h[1]), pk2(h[2], h[3]), pk2(h[4], h[5]), pk2(h[6], h[7])};
      *(LAS v4u*)sx = hv;
      st16(ws, hv, c * 16 + (10 * j + G) * 256, WS_H + (unsigned)blk * 20480);
#pragma unroll
      for (int e = 0; e < 4; ++e) { float lo = bflo(hv[e]), hi = bfhi(hv[e]); ssum += lo + hi; ssq += lo * lo + hi * hi; }
    }
  }
  ssum = xrow_sum(ssum); ssq = xrow_sum(ssq);
  {
    const int idx = 2 * j + hf;
    const int e = idx & 3;
    if (lane < 16) {
      v2u sv = {__float_as_uint(ssum), __float_as_uint(ssq)};
      __builtin_amdgcn_raw_buffer_store_b64(sv, ws, c * 16 + (e & 1) * 8, WS_ST + (unsigned)blk * 2048 + (e >> 1) * 1024 + (idx >> 2) * 256, 16);
    }
  }
  STAMP(8);
}

DEVI void phase3_prefetch(const Params& p, LAS char* lds, int lane, int w, u64 tag, bool wait0) {
  const int x8 = blockIdx.x & 7, r = blockIdx.x >> 3;
  const int jj = 8 * (x8 & 1) + (r & 7);
  rsrc_t ws = mk_rsrc(p.ws, WS_END);
  if (wait0) wait_flags(p.ws, WS_F0A + (jj * 32 + lane) * 8, lane < 20, tag);
  if (w == 0) { rsrc_t bs = mk_rsrc(p.b1, F_ * 2); dma16<0>(bs, lds + L_P3_B1, lane < 12 ? 192 * jj + lane * 16 : 0x7fffffffu, 0); }
#pragma unroll
  for (int n = 0; n < 15; ++n) { const int f = n * 8 + w; dma16<16>(ws, lds + f * 1024, lane * 16, WS_W1 + (unsigned)jj * 122880 + f * 1024); }
}
DEVI void phase3(const Params& p, LAS char* lds, int lane, int w, u64 tag, bool wait2) {
  const int x8 = blockIdx.x & 7, r = blockIdx.x >> 3;
  const int ti = 4 * (x8 >> 1) + (r >> 3), jj = 8 * (x8 & 1) + (r & 7);
  rsrc_t ws = mk_rsrc(p.ws, WS_END);
  const v4u srd = mk_srd(p.ws, WS_END);
  const unsigned l16 = lane * 16;
  const int blk = 8 * ti + w;
  WAITV(0); BAR(); asm volatile("" ::: "memory");
  if (w < 6) {
    f32x4 cs = {0.f, 0.f, 0.f, 0.f};
    const v4u ones = {0x3f803f80u, 0x3f803f80u, 0x3f803f80u, 0x3f803f80u};
#pragma unroll
    for (int ks = 0; ks < 20; ++ks) cs = mfma(ldsr16(lds + (ks * 6 + w) * 1024 + l16), ones, cs);
    if ((lane & 15) == 0) *(LAS f32x4*)(lds + L_P3_B1 + 1024 + (w * 16 + (lane >> 4) * 4) * 4) = cs;
  }
  if (wait2) wait_flags(p.ws, WS_F2 + (ti * 16 + lane) * 8, lane < 16, tag);
  STAMP(10);
  v4u a[20], st0, st1;
#pragma unroll
  for (int s = 0; s < 20; ++s) aload(a[s], srd, l16, WS_H + (unsigned)blk * 20480 + s * 1024);
  aload(st0, srd, l16, WS_ST + (unsigned)blk * 2048); aload(st1, srd, l16, WS_ST + (unsigned)blk * 2048 + 1024);
  f32x4 acc[6];
#pragma unroll
  for (int t = 0; t < 6; ++t) acc[t] = f32x4{0.f, 0.f, 0.f, 0.f};
  v4u B[2][6];
  LAS char* bb = lds + l16;
#pragma unroll
  for (int t = 0; t < 6; ++t) B[0][t] = ldsr16(bb + t * 1024);
  SCHED0();
#pragma unroll
  for (int s = 0; s < 20; ++s) {
    asm volatile("s_waitcnt vmcnt(%1)" : "+v"(a[s]) : "i"(21 - s) : "memory");
    if (s + 1 < 20) {
#pragma unroll
      for (int t = 0; t < 6; ++t) B[(s + 1) & 1][t] = ldsr16(bb + (s + 1) * 6144 + t * 1024);
    }
#pragma unroll
    for (int t = 0; t < 6; ++t) acc[t] = mfma(B[s & 1][t], a[s], acc[t]);
#pragma unroll
    for (int t = 0; t < 6; ++t) { __builtin_amdgcn_sched_group_barrier(0x100, 1, 0); __builtin_amdgcn_sched_group_barrier(0x8, 1, 0); }
    SCHED0();
  }
  STAMP(11);
  WAITV2(0, st0, st1);
  lbar();
  float s1 = (__uint_as_float(st0[0]) + __uint_as_float(st0[2])) + (__uint_as_float(st1[0]) + __uint_as_float(st1[2]));
  float s2 = (__uint_as_float(st0[1]) + __uint_as_float(st0[3])) + (__uint_as_float(st1[1]) + __uint_as_float(st1[3]));
  s1 = xrow_sum(s1); s2 = xrow_sum(s2);
  const float mean = s1 * (1.f / 640.f);
  const float var = fmaxf(s2 * (1.f / 640.f) - mean * mean, 0.f);
  const float rstd = __builtin_amdgcn_rsqf(var + 1e-5f), mr = -mean * rstd;
  const int g = lane >> 4;
  unsigned o[12];
#pragma unroll
  for (int t = 0; t < 6; ++t) {
    f32x4 cs = *(LAS f32x4*)(lds + L_P3_B1 + 1024 + (t * 16 + g * 4) * 4);
    v2u bb1 = *(LAS v2u*)(lds + L_P3_B1 + (t * 16 + g * 4) * 2);
    float y0 = fmaxf(fmaf(rstd, acc[t][0], fmaf(mr, cs[0], bflo(bb1.x))), 0.f), y1 = fmaxf(fmaf(rstd, acc[t][1], fmaf(mr, cs[1], bfhi(bb1.x))), 0.f);
    float y2 = fmaxf(fmaf(rstd, acc[t][2], fmaf(mr, cs[2], bflo(bb1.y))), 0.f), y3 = fmaxf(fmaf(rstd, acc[t][3], fmaf(mr, cs[3], bfhi(bb1.y))), 0.f);
    o[2 * t] = pk2(y0 * y0, y1 * y1); o[2 * t + 1] = pk2(y2 * y2, y3 * y3);
  }
#pragma unroll
  for (int q = 0; q < 3; ++q) st16(ws, v4u{o[4 * q], o[4 * q + 1], o[4 * q + 2], o[4 * q + 3]}, l16, WS_H1 + (unsigned)blk * 49152 + (3 * jj + q) * 1024);
  STAMP(12);
}

DEVI void phase4_prefetch(const Params& p, LAS char* lds, int lane, int w, u64 tag, bool wait0) {
  const int r = blockIdx.x >> 3, j = r & 7;
  rsrc_t ws = mk_rsrc(p.ws, WS_END);
  if (wait0) wait_flags(p.ws, WS_F0B + (j * 64 + lane) * 8, lane < 48, tag);
  if (w == 0) { rsrc_t bs = mk_rsrc(p.b2, D_ * 2); dma16<0>(bs, lds + L_P4_B2, lane < 10 ? 160 * j + lane * 16 : 0x7fffffffu, 0); }
#pragma unroll
  for (int n = 0; n < 15; ++n) { const int f = n * 8 + w; dma16<16>(ws, lds + f * 1024, lane * 16, WS_W2 + (unsigned)j * 245760 + f * 1024); }
}
DEVI void phase4(const Params& p, LAS char* lds, int lane, int w, u64 tag, bool wait3, bool have_stash) {
  const int x8 = blockIdx.x & 7, r = blockIdx.x >> 3;
  const int ti = 2 * x8 + (r >> 4), hb = (r >> 3) & 1, j = r & 7;
  rsrc_t ws = mk_rsrc(p.ws, WS_END);
  const v4u srd = mk_srd(p.ws, WS_END);
  const unsigned l16 = lane * 16;
  const int b = w & 3, kh = w >> 2, blk = 8 * ti + 4 * hb + b;
  if (wait3) wait_flags(p.ws, WS_F3 + (ti * 16 + lane) * 8, lane < 16, tag);
  STAMP(14);
  const unsigned ha = WS_H1 + (unsigned)blk * 49152 + kh * 24 * 1024, wb = WS_W2 + (unsigned)j * 245760;
  v4u a[24];
#pragma unroll
  for (int s = 0; s < 8; ++s) aload(a[s], srd, l16, ha + s * 1024);
  f32x4 acc[5];
#pragma unroll
  for (int t = 0; t < 5; ++t) acc[t] = f32x4{0.f, 0.f, 0.f, 0.f};
  v4u B[2][5];
  LAS char* bb = lds + kh * 20480 + l16;
#define P4_READ(s) do { _Pragma("unroll") for (int t = 0; t < 5; ++t) B[(s) & 1][t] = ldsr16(bb + (((s) >> 2) % 3) * 40960 + ((s) & 3) * 5120 + t * 1024); } while (0)
  WAITV4(4, a[0], a[1], a[2], a[3]); BAR(); asm volatile("" ::: "memory");
  P4_READ(0);
  SCHED0();
#pragma unroll
  for (int s = 0; s < 24; ++s) {
    const int stg = s >> 2;
    if ((s & 3) == 3 && stg + 1 < 6) {
      const int j1 = stg + 1, q = 4 * j1;
      if (j1 == 1) asm volatile("s_waitcnt vmcnt(3) lgkmcnt(0)" : "+v"(a[q]), "+v"(a[q + 1]), "+v"(a[q + 2]), "+v"(a[q + 3])::"memory");
      else if (j1 <= 4) asm volatile("s_waitcnt vmcnt(8) lgkmcnt(0)" : "+v"(a[q]), "+v"(a[q + 1]), "+v"(a[q + 2]), "+v"(a[q + 3])::"memory");
      else asm volatile("s_waitcnt vmcnt(0) lgkmcnt(0)" : "+v"(a[q]), "+v"(a[q + 1]), "+v"(a[q + 2]), "+v"(a[q + 3])::"memory");
      BAR(); asm volatile("" ::: "memory");
    }
    if (s + 8 < 24) aload(a[s + 8], srd, l16, ha + (s + 8) * 1024);
    if ((s & 3) == 3 && stg + 3 < 6) {
#pragma unroll
      for (int n = 0; n < 5; ++n) { const int f = n * 8 + w; dma16<16>(ws, lds + (stg % 3) * 40960 + f * 1024, l16, wb + (stg + 3) * 40960 + f * 1024); }
    }
    if (s + 1 < 24) P4_READ(s + 1);
#pragma unroll
    for (int t = 0; t < 5; ++t) acc[t] = mfma(B[s & 1][t], a[s], acc[t]);
#pragma unroll
    for (int t = 0; t < 5; ++t) { __builtin_amdgcn_sched_group_barrier(0x100, 1, 0); __builtin_amdgcn_sched_group_barrier(0x8, 1, 0); }
    SCHED0();
  }
#undef P4_READ
  STAMP(15);
  lbar();
  park_partials(lds, 0, acc, lane, w);
  lbar();
  for (int q = w * 64 + lane; q < 640; q += 512) {
    const int row = q / 10, G = q - row * 10;
    LAS char* s0 = lds + row * 336 + G * 32;
    f32x4 u0 = *(LAS f32x4*)s0, u1 = *(LAS f32x4*)(s0 + 16), w0 = *(LAS f32x4*)(s0 + 21504), w1 = *(LAS f32x4*)(s0 + 21504 + 16);
    v4u hv;
    const int tok = 128 * ti + 64 * hb + row;
    if (have_stash) hv = *(LAS v4u*)(lds + L_STASH + row * 160 + G * 16);
    else hv = __builtin_amdgcn_raw_buffer_load_b128(ws, (unsigned)(tok >> 4) * 20480 + (tok & 15) * 16 + (10 * j + G) * 256, WS_H, 0);
    v4u bv = *(LAS v4u*)(lds + L_P4_B2 + G * 16);
    float y[8];
    y[0] = u0[0] + w0[0] + bflo(bv[0]); y[1] = u0[1] + w0[1] + bfhi(bv[0]); y[2] = u0[2] + w0[2] + bflo(bv[1]); y[3] = u0[3] + w0[3] + bfhi(bv[1]);
    y[4] = u1[0] + w1[0] + bflo(bv[2]); y[5] = u1[1] + w1[1] + bfhi(bv[2]); y[6] = u1[2] + w1[2] + bflo(bv[3]); y[7] = u1[3] + w1[3] + bfhi(bv[3]);
    y[0] += bflo(hv[0]); y[1] += bfhi(hv[0]); y[2] += bflo(hv[1]); y[3] += bfhi(hv[1]); y[4] += bflo(hv[2]); y[5] += bfhi(hv[2]); y[6] += bflo(hv[3]); y[7] += bfhi(hv[3]);
    v4u ov = {pk2(y[0], y[1]), pk2(y[2], y[3]), pk2(y[4], y[5]), pk2(y[6], y[7])};
    *(GAS v4u*)((GAS char*)p.out + (size_t)tok * 1280 + 160 * j + 16 * G) = ov;
  }
  STAMP(16);
}

DEVI void wg_publish(const Params& p, unsigned off, u64 tag) {
  WAITV(0); lbar();
  if (threadIdx.x == 0) publish(p.ws, off, tag);
}

template <int PB, int PE>
__global__ __launch_bounds__(512, 2) __attribute__((amdgpu_waves_per_eu(2, 2))) void fused_block(Params p) {
  extern __shared__ __attribute__((aligned(16))) char smem[];
  LAS char* lds = (LAS char*)smem;
  const int lane = threadIdx.x & 63, w = __builtin_amdgcn_readfirstlane(threadIdx.x >> 6);
  const int x8 = blockIdx.x & 7, r = blockIdx.x >> 3;
  u64 tag = 1;
  constexpr bool ALL = (PB == 0 && PE == 5);
  if (ALL) {
    const u64 n = __hip_atomic_load((gu64*)(p.ws + WS_NONCE), RLX_AGENT);
    const unsigned lo = __builtin_amdgcn_readfirstlane((unsigned)n), hi = __builtin_amdgcn_readfirstlane((unsigned)(n >> 32));
    tag = (((u64)hi << 32) | lo) * 6364136223846793005ull + 1442695040888963407ull;
  }
  STAMP(0);
  if (PB <= 0 && 0 < PE) {
    phase0(p, lds, lane, w, tag, ALL);
    if (!ALL && PE == 1 && NLAUNCH == 2) {
      if (blockIdx.x == 0) for (int i = threadIdx.x; i < 3 * 512; i += 512) ((GAS u64*)(p.ws + WS_F1))[i] = 0;
    }
    STAMP(1);
    if (PE > 1) lbar();
  }
  constexpr bool IN1 = PB <= 1 && 2 < PE + 0, IN2 = PB <= 2 && 3 < PE + 0, IN3 = PB <= 3 && 4 < PE + 0;
  if (PB <= 1 && 1 < PE) {
    phase1(p, lds, lane, w, tag, ALL);
    if (IN1) {
      const int ti = 4 * (x8 >> 1) + (r >> 3), pr = 8 * (x8 & 1) + (r & 7);
      wg_publish(p, WS_F1 + (ti * 16 + pr) * 8, tag);
    }
    STAMP(5);
  }
  if (PB <= 2 && 2 < PE) {
    phase2_prefetch(p, lds, lane, w, tag, ALL);
    phase2(p, lds, lane, w, tag, IN1);
    if (IN2) {
      const int ti = 2 * x8 + (r >> 4);
      wg_publish(p, WS_F2 + (ti * 16 + (r & 15)) * 8, tag);
    }
    STAMP(9);
  }
  if (PB <= 3 && 3 < PE) {
    phase3_prefetch(p, lds, lane, w, tag, ALL);
    phase3(p, lds, lane, w, tag, IN2);
    if (IN3) {
      const int ti = 4 * (x8 >> 1) + (r >> 3), jj = 8 * (x8 & 1) + (r & 7);
      wg_publish(p, WS_F3 + (ti * 16 + jj) * 8, tag);
    }
    STAMP(13);
  }
  if (PB <= 4 && 4 < PE) {
    phase4_prefetch(p, lds, lane, w, tag, ALL);
    if (ALL && blockIdx.x == 0 && w == 7) {
      for (int q = 0; q < 4; ++q) wait_flags(p.ws, WS_F1 + (q * 64 + lane) * 8, true, tag);
      if (lane == 0) publish(p.ws, WS_NONCE, tag);
    }
    phase4(p, lds, lane, w, tag, IN3, PB <= 2);
  }
}

extern "C" void kernel_launch(void* const* d_in, const int* in_sizes, int n_in, void* d_out, int out_size, void* d_ws, size_t ws_size, hipStream_t stream) {
  Params p;
  p.x = (const unsigned short*)d_in[0]; p.wqkv = (const unsigned short*)d_in[1]; p.wo = (const unsigned short*)d_in[2];
  p.w1 = (const unsigned short*)d_in[4]; p.b1 = (const unsigned short*)d_in[5]; p.w2 = (const unsigned short*)d_in[6]; p.b2 = (const unsigned short*)d_in[7];
  p.out = (unsigned short*)d_out; p.ws = (unsigned char*)d_ws;
#define LAUNCH(a, b) do { hipFuncSetAttribute((const void*)fused_block<a, b>, hipFuncAttributeMaxDynamicSharedMemorySize, LDS_BYTES); \
    hipLaunchKernelGGL((fused_block<a, b>), dim3(256), dim3(512), LDS_BYTES, stream, p); } while (0)
  LAUNCH(0, 1); LAUNCH(1, 5);
}
```

```cpp
#include <hip/hip_runtime.h>
#include <hip/hip_bf16.h>
#include <stdint.h>

#define GAS __attribute__((address_space(1)))
#define LAS __attribute__((address_space(3)))
#define DEVI __device__ __forceinline__
typedef unsigned v4u __attribute__((ext_vector_type(4)));
typedef unsigned v2u __attribute__((ext_vector_type(2)));
typedef float f32x4 __attribute__((ext_vector_type(4)));
typedef short bf16x8 __attribute__((ext_vector_type(8)));
typedef short s16x4 __attribute__((ext_vector_type(4)));
typedef __bf16 bf16x2 __attribute__((ext_vector_type(2)));
typedef unsigned long long u64;
typedef GAS u64 gu64;
#define RLX_AGENT __ATOMIC_RELAXED, __HIP_MEMORY_SCOPE_AGENT

#define NLAUNCH 1
#define OUT_SC1 0
#define WS_AT_END 1
#define TOUCH 0
#define DIAG 0

constexpr int T_ = 2048, D_ = 640, F_ = 1536;
constexpr unsigned WS_NONCE = 0;
constexpr unsigned WS_F0Q = 40960;
constexpr unsigned WS_F0O = 8192;
constexpr unsigned WS_F0A = 12288;
constexpr unsigned WS_F0B = 16384;
constexpr unsigned WS_F0X = 20480;
constexpr unsigned WS_F1 = 24576;
constexpr unsigned WS_F2 = 28672;
constexpr unsigned WS_F3 = 32768;
constexpr unsigned WS_XN = 131072;
constexpr unsigned WS_WQ = WS_XN + 128 * 20480;
constexpr unsigned WS_WO = WS_WQ + 16 * 20 * 8 * 1024;
constexpr unsigned WS_W1 = WS_WO + 8 * 20 * 5 * 1024;
constexpr unsigned WS_W2 = WS_W1 + 16 * 20 * 6 * 1024;
constexpr unsigned WS_O = WS_W2 + 8 * 48 * 5 * 1024;
constexpr unsigned WS_H = WS_O + 128 * 20480;
constexpr unsigned WS_H1 = WS_H + 128 * 20480;
constexpr unsigned WS_ST = WS_H1 + 128 * 49152;
constexpr unsigned WS_END = WS_ST + 128 * 2048;
constexpr unsigned LDS_BYTES = 163840;
constexpr unsigned L_STASH = 153600;
constexpr unsigned L_P0_LN = 65536;
constexpr unsigned L_TAG = 123904 + 1024;
#define SHADOW_SLEEP 24
constexpr unsigned L_P1_HALO = 65536;
constexpr unsigned L_P1_EX = 125952;
constexpr unsigned L_RED = 102400;
constexpr unsigned L_P3_B1 = 122880;
constexpr unsigned L_P4_B2 = 122880;

struct Params {
  const unsigned short* x; const unsigned short* wqkv; const unsigned short* wo; const unsigned short* w1; const unsigned short* b1;
  const unsigned short* w2; const unsigned short* b2; unsigned short* out; unsigned char* ws;
};

typedef __amdgpu_buffer_rsrc_t rsrc_t;
DEVI rsrc_t mk_rsrc(const void* p, unsigned bytes) { return __builtin_amdgcn_make_buffer_rsrc((void*)p, 0, bytes, 0x00020000); }
DEVI v4u mk_srd(const void* p, unsigned bytes) {
  u64 a = (u64)p; v4u r; r.x = (unsigned)a; r.y = (unsigned)(a >> 32) & 0xffffu; r.z = bytes; r.w = 0x00020000u; return r;
}
template <int AUX> DEVI void dma16(rsrc_t rs, LAS char* lds, unsigned voff, unsigned soff) {
  __builtin_amdgcn_raw_ptr_buffer_load_lds(rs, (LAS void*)lds, 16, voff, soff, 0, AUX);
}
DEVI void aload(v4u& d, v4u srd, unsigned voff, unsigned soff) {
  asm volatile("buffer_load_dwordx4 %0, %1, %2, %3 offen sc1" : "=v"(d) : "v"(voff), "s"(srd), "s"(soff) : "memory");
}
DEVI void st16(rsrc_t rs, v4u v, unsigned voff, unsigned soff) { __builtin_amdgcn_raw_buffer_store_b128(v, rs, voff + soff, 0, 16); }
#define WAITV(n) asm volatile("s_waitcnt vmcnt(%0)" ::"i"(n) : "memory")
#define WAITV1(n, a) asm volatile("s_waitcnt vmcnt(%1)" : "+v"(a) : "i"(n) : "memory")
#define WAITV2(n, a, b) asm volatile("s_waitcnt vmcnt(%2)" : "+v"(a), "+v"(b) : "i"(n) : "memory")
#define WAITV4(n, a, b, c, d) asm volatile("s_waitcnt vmcnt(%4)" : "+v"(a), "+v"(b), "+v"(c), "+v"(d) : "i"(n) : "memory")
#define WAITL0() asm volatile("s_waitcnt lgkmcnt(0)" ::: "memory")
#define BAR() __builtin_amdgcn_s_barrier()
#define SCHED0() __builtin_amdgcn_sched_barrier(0)
DEVI void lbar() { WAITL0(); BAR(); asm volatile("" ::: "memory"); }

DEVI unsigned pk2(float a, float b) { bf16x2 p; p[0] = (__bf16)a; p[1] = (__bf16)b; return __builtin_bit_cast(unsigned, p); }
DEVI float bflo(unsigned u) { return __uint_as_float(u << 16); }
DEVI float bfhi(unsigned u) { return __uint_as_float(u & 0xffff0000u); }
DEVI bf16x8 asbf(v4u v) { return __builtin_bit_cast(bf16x8, v); }
DEVI f32x4 mfma(v4u a, v4u b, f32x4 c) { return __builtin_amdgcn_mfma_f32_16x16x32_bf16(asbf(a), asbf(b), c, 0, 0, 0); }
DEVI v4u ldsr16(LAS char* p) { return *(LAS v4u*)p; }
DEVI float xrow_max(float v) {
  unsigned u = __float_as_uint(v);
  auto a = __builtin_amdgcn_permlane32_swap(u, u, false, false); v = fmaxf(__uint_as_float(a[0]), __uint_as_float(a[1]));
  u = __float_as_uint(v);
  auto b = __builtin_amdgcn_permlane16_swap(u, u, false, false); return fmaxf(__uint_as_float(b[0]), __uint_as_float(b[1]));
}
DEVI float xrow_sum(float v) {
  unsigned u = __float_as_uint(v);
  auto a = __builtin_amdgcn_permlane32_swap(u, u, false, false); v = __uint_as_float(a[0]) + __uint_as_float(a[1]);
  u = __float_as_uint(v);
  auto b = __builtin_amdgcn_permlane16_swap(u, u, false, false); return __uint_as_float(b[0]) + __uint_as_float(b[1]);
}
DEVI float row16_sum(float v) {
  v += __uint_as_float(__builtin_amdgcn_update_dpp(0u, __float_as_uint(v), 0x128, 0xf, 0xf, false));
  v += __uint_as_float(__builtin_amdgcn_update_dpp(0u, __float_as_uint(v), 0x124, 0xf, 0xf, false));
  v += __uint_as_float(__builtin_amdgcn_update_dpp(0u, __float_as_uint(v), 0x122, 0xf, 0xf, false));
  v += __uint_as_float(__builtin_amdgcn_update_dpp(0u, __float_as_uint(v), 0x121, 0xf, 0xf, false));
  return v;
}

DEVI u64 rfl64(u64 v) { return ((u64)__builtin_amdgcn_readfirstlane((unsigned)(v >> 32)) << 32) | (unsigned)__builtin_amdgcn_readfirstlane((unsigned)v); }
DEVI void publish(unsigned char* ws, unsigned off, u64 tag) { __hip_atomic_store((gu64*)(ws + off), tag, RLX_AGENT); }
DEVI void wait_flags(unsigned char* ws, unsigned laneoff, bool active, u64 tag) {
  for (unsigned sp = 0; sp < (1u << 18); ++sp) {
    u64 v = tag;
    if (active) v = __hip_atomic_load((gu64*)(ws + laneoff), RLX_AGENT);
    if (__all(v == tag)) break;
    __builtin_amdgcn_s_sleep(1);
  }
  __builtin_amdgcn_fence(__ATOMIC_ACQUIRE, "wavefront");
  asm volatile("" ::: "memory");
}

DEVI void wait_flags2(unsigned char* ws, unsigned off1, bool act1, unsigned off2, bool act2, u64 tag) {
  for (unsigned sp = 0; sp < (1u << 18); ++sp) {
    u64 v1 = tag, v2 = tag;
    if (act1) v1 = __hip_atomic_load((gu64*)(ws + off1), RLX_AGENT);
    if (act2) v2 = __hip_atomic_load((gu64*)(ws + off2), RLX_AGENT);
    if (__all(v1 == tag && v2 == tag)) break;
    __builtin_amdgcn_s_sleep(1);
  }
  __builtin_amdgcn_fence(__ATOMIC_ACQUIRE, "wavefront");
  asm volatile("" ::: "memory");
}

#define STAMP(id) do {} while (0)

template <int KIND>
DEVI unsigned retile_unit(const Params& p, rsrc_t ws, LAS char* st, int lane, int a, int ks) {
  constexpr int NCH = KIND == 2 ? 12 : 10;
  constexpr int NI = 32 * NCH / 64;
  constexpr int NT = KIND == 2 ? 6 : 5;
  constexpr unsigned RS = KIND == 2 ? 3072u : 1280u;
  const void* src = KIND == 1 ? (const void*)p.wo : KIND == 2 ? (const void*)p.w1 : (const void*)p.w2;
  constexpr unsigned SB = KIND == 1 ? 640u * 1280u : KIND == 2 ? 640u * 3072u : 1536u * 1280u;
  rsrc_t rs = mk_rsrc(src, SB);
#pragma unroll
  for (int n = 0; n < NI; ++n) {
    int q = n * 64 + lane;
    int rho = q / NCH, ch = q - rho * NCH;
    int row; unsigned col;
    const int g = rho >> 3, t = (rho >> 2) & 1, e = rho & 3;
    if (KIND == 1) { row = ks < 16 ? (2 * ks + t) * 20 + 4 * g + e : (2 * (4 * (ks - 16) + g) + t) * 20 + 16 + e; col = 160 * a + 16 * ch; }
    else if (KIND == 2) { row = 32 * ks + rho; col = 192 * a + 16 * ch; }
    else { row = 32 * ks + 16 * t + 4 * g + e; col = 160 * a + 16 * ch; }
    dma16<0>(rs, st + n * 1024, (unsigned)row * RS + col, 0);
  }
  WAITV(0);
  STAMP(18);
  unsigned dst;
  if (KIND == 1) dst = WS_WO + (unsigned)(a * 20 + ks) * 5120;
  else if (KIND == 2) dst = WS_W1 + (unsigned)(a * 20 + ks) * 6144;
  else { int kh = ks / 24, s = ks - kh * 24; dst = WS_W2 + (unsigned)a * 245760 + (s >> 2) * 40960 + kh * 20480 + (s & 3) * 5120; }
  const int g = lane >> 4, q4 = (lane & 15) >> 2, p4 = lane & 3;
  constexpr int RB = NCH * 16;
  LAS char* base = st + (8 * g + q4) * RB + 8 * p4;
#pragma unroll
  for (int t = 0; t < NT; ++t) {
    s16x4 lo = __builtin_amdgcn_ds_read_tr16_b64_v4i16((LAS s16x4*)(base + 32 * t));
    s16x4 hi = __builtin_amdgcn_ds_read_tr16_b64_v4i16((LAS s16x4*)(base + 4 * RB + 32 * t));
    v2u l2 = __builtin_bit_cast(v2u, lo), h2 = __builtin_bit_cast(v2u, hi);
    st16(ws, v4u{l2.x, l2.y, h2.x, h2.y}, lane * 16, dst + t * 1024);
  }
  STAMP(19);
  return KIND == 1 ? WS_F0O + (a * 32 + ks) * 8 : KIND == 2 ? WS_F0A + (a * 32 + ks) * 8 : WS_F0B + (a * 64 + ks) * 8;
}

DEVI void wqkv_unit(const Params& p, rsrc_t ws, LAS char* lds, int lane, int w, int u, u64 nonce, u64& tag, bool flags) {
  const int ks = u >> 2, hf = (u >> 1) & 1, rh = u & 1;
  rsrc_t rs = mk_rsrc(p.wqkv, 640u * 3840u);
#pragma unroll
  for (int m = 0; m < 4; ++m) {
    const int n = w + 8 * m;
    if (n < 30) {
      const int q = n * 64 + lane, row = q / 120, ch = q - row * 120, sec = ch / 40;
      dma16<0>(rs, lds + n * 1024, (unsigned)(32 * ks + 16 * rh + row) * 3840u + sec * 1280 + 640 * hf + 16 * (ch - sec * 40), 0);
    }
  }
  STAMP(17);
  WAITV(0);
  if (flags && w == 0) { asm volatile("" : "+v"(nonce)); if (lane == 0) *(LAS u64*)(lds + L_TAG) = nonce * 6364136223846793005ull + 1442695040888963407ull; }
  lbar();
  STAMP(18);
  if (flags) tag = rfl64(*(LAS u64*)(lds + L_TAG));
  const int gl = (lane >> 4) & 1, th = lane >> 5, q4 = (lane & 15) >> 2, p4 = lane & 3;
  LAS char* base = lds + (8 * gl + q4) * 1920 + 80 * w;
  const unsigned dst = WS_WQ + (unsigned)((8 * hf + w) * 20 + ks) * 8192;
  const unsigned vo = th * 1024 + (2 * rh + gl) * 256 + (lane & 15) * 16;
#pragma unroll
  for (int m = 0; m < 4; ++m) {
    int cb;
    if (m == 0) cb = 640 + 40 * th + 8 * p4;
    else if (m == 1) cb = th == 0 ? (p4 < 2 ? 640 : 0) + 32 + 40 * (p4 & 1) : 8 * p4;
    else if (m == 2) cb = th == 0 ? 40 + 8 * p4 : 1280 + 8 * p4;
    else cb = th == 0 ? 1280 + 40 + 8 * p4 : 1280 + 32 + 40 * (p4 & 1);
    s16x4 lo = __builtin_amdgcn_ds_read_tr16_b64_v4i16((LAS s16x4*)(base + cb));
    s16x4 hi = __builtin_amdgcn_ds_read_tr16_b64_v4i16((LAS s16x4*)(base + 4 * 1920 + cb));
    v2u l2 = __builtin_bit_cast(v2u, lo), h2 = __builtin_bit_cast(v2u, hi);
    v4u v = {l2.x, l2.y, h2.x, h2.y};
    if (m == 3 && th == 1 && (lane & 15) >= 8) v = v4u{0u, 0u, 0u, 0u};
    st16(ws, v, vo, dst + m * 2048);
  }
  STAMP(19);
  if (flags) { WAITV(0); STAMP(20); if (lane == 0) publish(p.ws, WS_F0Q + ((8 * hf + w) * 64 + ks * 2 + rh) * 8, tag); }
}

DEVI void ln_unit(const Params& p, rsrc_t ws, LAS char* lds, int lane, int w, int tb, u64 nonce, u64& tag, bool flags) {
  const int t = lane >> 4, i = lane & 15, c = 4 * w + t;
  const GAS v4u* src = (const GAS v4u*)((const GAS char*)p.x + (size_t)(tb * 16 + c) * 1280) + i;
  v4u r[5];
#pragma unroll
  for (int n = 0; n < 5; ++n) r[n] = src[16 * n];
  STAMP(17);
  float v[40];
#pragma unroll
  for (int n = 0; n < 5; ++n)
#pragma unroll
    for (int e = 0; e < 4; ++e) { v[n * 8 + 2 * e] = bflo(r[n][e]); v[n * 8 + 2 * e + 1] = bfhi(r[n][e]); }
  float s4[4] = {0.f, 0.f, 0.f, 0.f};
#pragma unroll
  for (int e = 0; e < 40; ++e) s4[e & 3] += v[e];
  const float mean = row16_sum((s4[0] + s4[1]) + (s4[2] + s4[3])) * (1.f / 640.f);
  float q4[4] = {0.f, 0.f, 0.f, 0.f};
#pragma unroll
  for (int e = 0; e < 40; ++e) { v[e] -= mean; q4[e & 3] = fmaf(v[e], v[e], q4[e & 3]); }
  const float rstd = __builtin_amdgcn_rsqf(row16_sum((q4[0] + q4[1]) + (q4[2] + q4[3])) * (1.f / 640.f) + 1e-5f);
  LAS char* st = lds + L_P0_LN;
#pragma unroll
  for (int n = 0; n < 5; ++n) {
    v4u o;
#pragma unroll
    for (int e = 0; e < 4; ++e) o[e] = pk2(v[n * 8 + 2 * e] * rstd, v[n * 8 + 2 * e + 1] * rstd);
    *(LAS v4u*)(st + (i + 16 * n) * 272 + c * 16) = o;
  }
  if (flags && w == 0) { if (lane == 0) *(LAS u64*)(lds + L_TAG) = nonce * 6364136223846793005ull + 1442695040888963407ull; }
  STAMP(18);
  lbar();
  if (flags) tag = rfl64(*(LAS u64*)(lds + L_TAG));
#pragma unroll
  for (int n = 0; n < 5; ++n) {
    const int ks = w * 5 + n;
    v4u o = ldsr16(st + (4 * ks + (lane >> 4)) * 272 + (lane & 15) * 16);
    st16(ws, o, lane * 16, WS_XN + (unsigned)tb * 20480 + ks * 1024);
  }
  STAMP(19);
  if (flags) { WAITV(0); STAMP(20); if (lane == 0) publish(p.ws, WS_F0X + (tb * 4 + w) * 8, tag); }
}

DEVI unsigned phase0(const Params& p, LAS char* lds, int lane, int w, u64& tag, bool flags) {
  rsrc_t ws = mk_rsrc(p.ws, WS_END);
  const int cu = blockIdx.x;
  u64 nonce = 0;
  if (flags && w == 0) nonce = __hip_atomic_load((gu64*)(p.ws + WS_NONCE), RLX_AGENT);
  if (cu < 128 && w < 4) { ln_unit(p, ws, lds, lane, w, cu, nonce, tag, flags); return 0; }
  if (cu >= 128 && cu < 208) { wqkv_unit(p, ws, lds, lane, w, cu - 128, nonce, tag, flags); return 0; }
  int slot;
  if (cu < 128) slot = cu * 4 + (w - 4);
  else {
    slot = 512 + (cu - 208) * 8 + w;
    if (flags && w == 0 && lane == 0) *(LAS u64*)(lds + L_TAG) = nonce * 6364136223846793005ull + 1442695040888963407ull;
  }
  lbar();
  if (flags) tag = rfl64(*(LAS u64*)(lds + L_TAG));
  if (cu >= 208) __builtin_amdgcn_s_sleep(SHADOW_SLEEP);
  STAMP(17);
  LAS char* st = lds + (cu < 128 ? (w - 4) : w) * 8192;
  if (slot < 160) return retile_unit<1>(p, ws, st, lane, slot & 7, slot >> 3);
  if (slot < 480) { int u = slot - 160; return retile_unit<2>(p, ws, st, lane, u & 15, u >> 4); }
  if (slot < 864) { int u = slot - 480; return retile_unit<3>(p, ws, st, lane, u & 7, u >> 3); }
  return 0;
}

DEVI void phase1(const Params& p, LAS char* lds, int lane, int w, u64 tag, bool wait0, unsigned pending) {
  const int x8 = blockIdx.x & 7, r = blockIdx.x >> 3;
  const int ti = 4 * (x8 >> 1) + (r >> 3), pr = 8 * (x8 & 1) + (r & 7);
  rsrc_t ws = mk_rsrc(p.ws, WS_END);
  const v4u srd = mk_srd(p.ws, WS_END);
  const unsigned l16 = lane * 16;
  const int blk = 8 * ti + w, hblk = ti > 0 ? 8 * ti - 1 : 0;
  if (wait0) {
    if (pending) { WAITV(0); if (lane == 0) publish(p.ws, pending, tag); }
    if (w == 7) {
      int bq = (ti > 0 ? 8 * ti - 1 : 0) + (lane >> 2); if (ti == 0 && lane >= 32) bq = 7;
      wait_flags2(p.ws, WS_F0X + (bq * 4 + (lane & 3)) * 8, lane < 36, WS_F0Q + (pr * 64 + lane) * 8, lane < 40, tag);
    }
    lbar();
  }
  STAMP(2);
  const unsigned wq = WS_WQ + (unsigned)pr * 163840, xa = WS_XN + (unsigned)blk * 20480, xh = WS_XN + (unsigned)hblk * 20480;
  v4u a[20];
#define P1_B(s, e) dma16<16>(ws, lds + ((s) & 3) * 16384 + (2 * w + (e)) * 1024, l16, wq + (s) * 16384 + (2 * w + (e)) * 1024)
#define P1_A(k) aload(a[k], srd, l16, xa + (k) * 1024)
#define P1_H(s) do { int f = 8 * (s) + w; dma16<16>(ws, lds + L_P1_HALO + f * 1024, l16, xh + (f < 20 ? f : 19) * 1024); } while (0)
  P1_B(0, 0); P1_B(0, 1); P1_A(0); P1_H(0); P1_A(1);
  P1_B(1, 0); P1_B(1, 1); P1_A(2); P1_H(1); P1_A(3);
  f32x4 acc[8], acch = {0.f, 0.f, 0.f, 0.f};
#pragma unroll
  for (int t = 0; t < 8; ++t) acc[t] = f32x4{0.f, 0.f, 0.f, 0.f};
  const bool hnormal = w < 3 || w >= 6;
  const int ht = w < 3 ? w : (w < 6 ? w + 2 : 0);
  v4u B[2][8], H1[2], H2[2];
#define P1_RO(ks) ((((ks) >> 1) & 3) * 16384 + ((ks) & 1) * 8192)
#define P1_READ(ks, t0, t1) do { _Pragma("unroll") for (int t = (t0); t < (t1); ++t) B[(ks) & 1][t] = ldsr16(lds + P1_RO(ks) + t * 1024 + l16); } while (0)
#define P1_READH(ks) do { const unsigned oW = P1_RO(ks) + ht * 1024, oX = L_P1_HALO + (ks) * 1024; \
    H1[(ks) & 1] = ldsr16(lds + (hnormal ? oW : oX) + l16); H2[(ks) & 1] = ldsr16(lds + (hnormal ? oX : oW) + l16); } while (0)
  WAITV2(5, a[0], a[1]); BAR(); asm volatile("" ::: "memory");
  P1_READ(0, 0, 8); P1_READH(0);
  SCHED0();
#pragma unroll
  for (int ks = 0; ks < 20; ++ks) {
    if (ks & 1) {
      const int j = (ks + 1) >> 1;
      if (j <= 9) {
        if (j == 1) asm volatile("s_waitcnt vmcnt(5) lgkmcnt(0)" : "+v"(a[2]), "+v"(a[3])::"memory");
        else if (j <= 8) asm volatile("s_waitcnt vmcnt(4) lgkmcnt(0)" : "+v"(a[2 * j]), "+v"(a[2 * j + 1])::"memory");
        else asm volatile("s_waitcnt vmcnt(0) lgkmcnt(0)" : "+v"(a[2 * j]), "+v"(a[2 * j + 1])::"memory");
        BAR(); asm volatile("" ::: "memory");
      }
    }
    const int sn = ks == 0 ? 2 : ((ks + 1) >> 1) + 2;
    if (ks + 1 < 20) P1_READ(ks + 1, 0, 5);
#pragma unroll
    for (int t = 0; t < 5; ++t) acc[t] = mfma(B[ks & 1][t], a[ks], acc[t]);
#pragma unroll
    for (int t = 0; t < 5; ++t) { __builtin_amdgcn_sched_group_barrier(0x100, 1, 0); __builtin_amdgcn_sched_group_barrier(0x8, 1, 0); }
    SCHED0();
    if (sn <= 9) {
      if (ks == 0) { P1_B(2, 0); P1_B(2, 1); P1_H(2); }
      else if (ks & 1) P1_B(sn, 0);
      else P1_A(2 * sn);
    }
    SCHED0();
    if (ks + 1 < 20) { P1_READ(ks + 1, 5, 8); P1_READH(ks + 1); }
#pragma unroll
    for (int t = 5; t < 8; ++t) acc[t] = mfma(a[ks], B[ks & 1][t], acc[t]);
    acch = mfma(H1[ks & 1], H2[ks & 1], acch);
#pragma unroll
    for (int t = 0; t < 4; ++t) { __builtin_amdgcn_sched_group_barrier(0x100, 1, 0); __builtin_amdgcn_sched_group_barrier(0x8, 1, 0); }
    SCHED0();
    if (sn <= 9) {
      if (ks == 0) { P1_A(4); P1_A(5); }
      else if (ks & 1) P1_B(sn, 1);
      else P1_A(2 * sn + 1);
    }
    SCHED0();
  }
#undef P1_B
#undef P1_A
#undef P1_H
#undef P1_RO
#undef P1_READ
#undef P1_READH
  STAMP(3);
  v2u pk[8];
#pragma unroll
  for (int t = 0; t < 8; ++t) pk[t] = v2u{pk2(acc[t][0], acc[t][1]), pk2(acc[t][2], acc[t][3])};
  LAS char* ex = lds + L_P1_EX + lane * 8;
  {
    LAS char* mine = ex + (w + 1) * 3072;
    *(LAS v2u*)(mine) = pk[0]; *(LAS v2u*)(mine + 512) = pk[1]; *(LAS v2u*)(mine + 1024) = pk[2];
    *(LAS v2u*)(mine + 1536) = pk[5]; *(LAS v2u*)(mine + 2048) = pk[6]; *(LAS v2u*)(mine + 2560) = pk[7];
    if (w < 6) *(LAS v2u*)(ex + w * 512) = v2u{pk2(acch[0], acch[1]), pk2(acch[2], acch[3])};
  }
  lbar();
  v2u pv[6];
#pragma unroll
  for (int e = 0; e < 6; ++e) pv[e] = *(LAS v2u*)(ex + w * 3072 + e * 512);
  const int g = lane >> 4, c = lane & 15, odd = c & 1, ce = c - odd;
  const bool havePrev = (ti > 0) || (w > 0);
  const bool v1a = havePrev && (4 * g >= ce + 2), v1b = havePrev && (4 * g >= ce), v2a = ce >= 4 * g, v2b = ce - 2 >= 4 * g;
  v2u rq;
  { auto s0 = __builtin_amdgcn_permlane32_swap(pk[2].x, pk[2].x, false, false); auto s1 = __builtin_amdgcn_permlane32_swap(pk[2].y, pk[2].y, false, false);
    rq = v2u{s0[1], s1[1]}; }
  const float c2 = 0.22360679774997896f * 1.4426950408889634f;
  f32x4 OA[2], OR[2];
#pragma unroll
  for (int hh = 0; hh < 2; ++hh) {
    const bool mine = g == hh;
    v4u Q = {pk[3 + hh].x, pk[3 + hh].y, mine ? rq.x : 0u, mine ? rq.y : 0u};
    v4u Kp = {pv[hh].x, pv[hh].y, pv[2].x, pv[2].y}, Ko = {pk[hh].x, pk[hh].y, pk[2].x, pk[2].y};
    const f32x4 z = {0.f, 0.f, 0.f, 0.f};
    f32x4 S1 = mfma(Kp, Q, z), S2 = mfma(Ko, Q, z);
    const float NEG = -3.0e38f;
    float s1a = odd ? S1[1] : S1[0], s1b = odd ? S1[3] : S1[2], s2a = odd ? S2[1] : S2[0], s2b = odd ? S2[3] : S2[2];
    s1a = v1a ? s1a : NEG; s1b = v1b ? s1b : NEG; s2a = v2a ? s2a : NEG; s2b = v2b ? s2b : NEG;
    const float m = xrow_max(fmaxf(fmaxf(s1a, s1b), fmaxf(s2a, s2b)));
    float p1a = v1a ? __builtin_amdgcn_exp2f((s1a - m) * c2) : 0.f, p1b = v1b ? __builtin_amdgcn_exp2f((s1b - m) * c2) : 0.f;
    float p2a = v2a ? __builtin_amdgcn_exp2f((s2a - m) * c2) : 0.f, p2b = v2b ? __builtin_amdgcn_exp2f((s2b - m) * c2) : 0.f;
    const float inv = __builtin_amdgcn_rcpf(xrow_sum((p1a + p1b) + (p2a + p2b)));
    const unsigned sh = 16 * odd;
    v4u P = {pk2(p1a * inv, 0.f) << sh, pk2(p1b * inv, 0.f) << sh, pk2(p2a * inv, 0.f) << sh, pk2(p2b * inv, 0.f) << sh};
    v4u VA = {pv[3 + hh].x, pv[3 + hh].y, pk[5 + hh].x, pk[5 + hh].y}, VR = {pv[5].x, pv[5].y, pk[7].x, pk[7].y};
    OA[hh] = mfma(VA, P, z); OR[hh] = mfma(VR, P, z);
  }
  {
    v4u om = {pk2(OA[0][0], OA[0][1]), pk2(OA[0][2], OA[0][3]), pk2(OA[1][0], OA[1][1]), pk2(OA[1][2], OA[1][3])};
    st16(ws, om, l16, WS_O + (unsigned)blk * 20480 + pr * 1024);
    unsigned r0x = pk2(OR[0][0], OR[0][1]), r0y = pk2(OR[0][2], OR[0][3]), r1x = pk2(OR[1][0], OR[1][1]), r1y = pk2(OR[1][2], OR[1][3]);
    auto t0 = __builtin_amdgcn_permlane16_swap(r1x, r1x, false, false); auto t1 = __builtin_amdgcn_permlane16_swap(r1y, r1y, false, false);
    v4u orr = {r0x, r0y, t0[1], t1[1]};
    if (lane < 16) st16(ws, orr, l16, WS_O + (unsigned)blk * 20480 + (16 + (pr >> 2)) * 1024 + (pr & 3) * 256);
  }
  STAMP(4);
}

DEVI void park_partials(LAS char* lds, unsigned red, const f32x4 (&acc)[5], int lane, int w) {
  const int b = w & 3, kh = w >> 2, g = lane >> 4, c = lane & 15;
  LAS char* d = lds + red + kh * 21504 + (16 * b + c) * 336 + g * 16;
#pragma unroll
  for (int mt = 0; mt < 5; ++mt) *(LAS f32x4*)(d + mt * 64) = acc[mt];
}

DEVI void phase2_prefetch(const Params& p, LAS char* lds, int lane, int w, u64 tag, bool wait0) {
  const int x8 = blockIdx.x & 7, r = blockIdx.x >> 3;
  const int ti = 2 * x8 + (r >> 4), hb = (r >> 3) & 1, j = r & 7;
  rsrc_t ws = mk_rsrc(p.ws, WS_END);
  if (w == 7) return;
  if (wait0) wait_flags(p.ws, WS_F0O + (j * 32 + lane) * 8, lane < 20, tag);
  for (int f = w; f < 100; f += 7) dma16<16>(ws, lds + f * 1024, lane * 16, WS_WO + (unsigned)j * 102400 + f * 1024);
  {
    rsrc_t xs = mk_rsrc(p.x, T_ * 1280);
    for (int n = w; n < 10; n += 7) {
      int q = n * 64 + lane, row = q / 10, ch = q - row * 10;
      dma16<0>(xs, lds + L_STASH + n * 1024, (unsigned)(128 * ti + 64 * hb + row) * 1280 + 160 * j + 16 * ch, 0);
    }
  }
}
DEVI void phase2(const Params& p, LAS char* lds, int lane, int w, u64 tag, bool wait1) {
  const int x8 = blockIdx.x & 7, r = blockIdx.x >> 3;
  const int ti = 2 * x8 + (r >> 4), hb = (r >> 3) & 1, j = r & 7;
  rsrc_t ws = mk_rsrc(p.ws, WS_END);
  const v4u srd = mk_srd(p.ws, WS_END);
  const unsigned l16 = lane * 16;
  const int b = w & 3, kh = w >> 2, blk = 8 * ti + 4 * hb + b;
  if (wait1 && w == 7) wait_flags(p.ws, WS_F1 + (ti * 16 + lane) * 8, lane < 16, tag);
  WAITV(0); BAR(); asm volatile("" ::: "memory");
  STAMP(6);
  v4u a[10];
  const unsigned oa = WS_O + (unsigned)blk * 20480 + 10 * kh * 1024;
#pragma unroll
  for (int s = 0; s < 5; ++s) aload(a[s], srd, l16, oa + s * 1024);
  f32x4 acc[5];
#pragma unroll
  for (int t = 0; t < 5; ++t) acc[t] = f32x4{0.f, 0.f, 0.f, 0.f};
  v4u B[2][5];
  LAS char* bb = lds + kh * 51200 + l16;
#pragma unroll
  for (int t = 0; t < 5; ++t) B[0][t] = ldsr16(bb + t * 1024);
  SCHED0();
#pragma unroll
  for (int s = 0; s < 10; ++s) {
    asm volatile("s_waitcnt vmcnt(%1)" : "+v"(a[s]) : "i"(s + 4 < 10 ? 4 : 9 - s) : "memory");
    if (s + 5 < 10) aload(a[s + 5], srd, l16, oa + (s + 5) * 1024);
    if (s + 1 < 10) {
#pragma unroll
      for (int t = 0; t < 5; ++t) B[(s + 1) & 1][t] = ldsr16(bb + (s + 1) * 5120 + t * 1024);
    }
#pragma unroll
    for (int t = 0; t < 5; ++t) acc[t] = mfma(B[s & 1][t], a[s], acc[t]);
#pragma unroll
    for (int t = 0; t < 5; ++t) { __builtin_amdgcn_sched_group_barrier(0x100, 1, 0); __builtin_amdgcn_sched_group_barrier(0x8, 1, 0); }
    SCHED0();
  }
  STAMP(7);
  park_partials(lds, L_RED, acc, lane, w);
  lbar();
  const int hf = kh, g4 = lane >> 4, c = lane & 15, row = 16 * b + c;
  float ssum = 0.f, ssq = 0.f;
#pragma unroll
  for (int rd = 0; rd < 2; ++rd) {
    const int G = rd == 0 ? 4 * hf + g4 : 8 + hf;
    if (rd == 0 || g4 == 0) {
      LAS char* s0 = lds + L_RED + row * 336 + G * 32;
      f32x4 u0 = *(LAS f32x4*)s0, u1 = *(LAS f32x4*)(s0 + 16), w0 = *(LAS f32x4*)(s0 + 21504), w1 = *(LAS f32x4*)(s0 + 21504 + 16);
      LAS char* sx = lds + L_STASH + row * 160 + G * 16;
      v4u xv = *(LAS v4u*)sx;
      float h[8];
      h[0] = u0[0] + w0[0] + bflo(xv[0]); h[1] = u0[1] + w0[1] + bfhi(xv[0]); h[2] = u0[2] + w0[2] + bflo(xv[1]); h[3] = u0[3] + w0[3] + bfhi(xv[1]);
      h[4] = u1[0] + w1[0] + bflo(xv[2]); h[5] = u1[1] + w1[1] + bfhi(xv[2]); h[6] = u1[2] + w1[2] + bflo(xv[3]); h[7] = u1[3] + w1[3] + bfhi(xv[3]);
      v4u hv = {pk2(h[0], h[1]), pk2(h[2], h[3]), pk2(h[4], h[5]), pk2(h[6], h[7])};
      *(LAS v4u*)sx = hv;
      st16(ws, hv, c * 16 + (10 * j + G) * 256, WS_H + (unsigned)blk * 20480);
#pragma unroll
      for (int e = 0; e < 4; ++e) { float lo = bflo(hv[e]), hi = bfhi(hv[e]); ssum += lo + hi; ssq += lo * lo + hi * hi; }
    }
  }
  ssum = xrow_sum(ssum); ssq = xrow_sum(ssq);
  {
    const int idx = 2 * j + hf;
    const int e = idx & 3;
    if (lane < 16) {
      v2u sv = {__float_as_uint(ssum), __float_as_uint(ssq)};
      __builtin_amdgcn_raw_buffer_store_b64(sv, ws, c * 16 + (e & 1) * 8, WS_ST + (unsigned)blk * 2048 + (e >> 1) * 1024 + (idx >> 2) * 256, 16);
    }
  }
  STAMP(8);
}

DEVI void phase3_prefetch(const Params& p, LAS char* lds, int lane, int w, u64 tag, bool wait0) {
  const int x8 = blockIdx.x & 7, r = blockIdx.x >> 3;
  const int jj = 8 * (x8 & 1) + (r & 7);
  rsrc_t ws = mk_rsrc(p.ws, WS_END);
  if (w == 7) return;
  if (wait0) wait_flags(p.ws, WS_F0A + (jj * 32 + lane) * 8, lane < 20, tag);
  if (w == 0) { rsrc_t bs = mk_rsrc(p.b1, F_ * 2); dma16<0>(bs, lds + L_P3_B1, lane < 12 ? 192 * jj + lane * 16 : 0x7fffffffu, 0); }
  for (int f = w; f < 120; f += 7) dma16<16>(ws, lds + f * 1024, lane * 16, WS_W1 + (unsigned)jj * 122880 + f * 1024);
}
DEVI void phase3(const Params& p, LAS char* lds, int lane, int w, u64 tag, bool wait2) {
  const int x8 = blockIdx.x & 7, r = blockIdx.x >> 3;
  const int ti = 4 * (x8 >> 1) + (r >> 3), jj = 8 * (x8 & 1) + (r & 7);
  rsrc_t ws = mk_rsrc(p.ws, WS_END);
  const v4u srd = mk_srd(p.ws, WS_END);
  const unsigned l16 = lane * 16;
  const int blk = 8 * ti + w;
  if (wait2 && w == 7) wait_flags(p.ws, WS_F2 + (ti * 16 + lane) * 8, lane < 16, tag);
  WAITV(0); BAR(); asm volatile("" ::: "memory");
  STAMP(10);
  v4u a[20], st0, st1;
  const unsigned ha = WS_H + (unsigned)blk * 20480;
#pragma unroll
  for (int s = 0; s < 6; ++s) aload(a[s], srd, l16, ha + s * 1024);
  if (w < 6) {
    f32x4 cs = {0.f, 0.f, 0.f, 0.f};
    const v4u ones = {0x3f803f80u, 0x3f803f80u, 0x3f803f80u, 0x3f803f80u};
#pragma unroll
    for (int ks = 0; ks < 20; ++ks) cs = mfma(ldsr16(lds + (ks * 6 + w) * 1024 + l16), ones, cs);
    if ((lane & 15) == 0) *(LAS f32x4*)(lds + L_P3_B1 + 1024 + (w * 16 + (lane >> 4) * 4) * 4) = cs;
  }
  f32x4 acc[6];
#pragma unroll
  for (int t = 0; t < 6; ++t) acc[t] = f32x4{0.f, 0.f, 0.f, 0.f};
  v4u B[2][6];
  LAS char* bb = lds + l16;
#pragma unroll
  for (int t = 0; t < 6; ++t) B[0][t] = ldsr16(bb + t * 1024);
  SCHED0();
#pragma unroll
  for (int s = 0; s < 20; ++s) {
    asm volatile("s_waitcnt vmcnt(%1)" : "+v"(a[s]) : "i"(s + 5 < 22 ? 5 : 21 - s) : "memory");
    if (s + 6 < 20) aload(a[s + 6], srd, l16, ha + (s + 6) * 1024);
    else if (s + 6 == 20) aload(st0, srd, l16, WS_ST + (unsigned)blk * 2048);
    else if (s + 6 == 21) aload(st1, srd, l16, WS_ST + (unsigned)blk * 2048 + 1024);
    if (s + 1 < 20) {
#pragma unroll
      for (int t = 0; t < 6; ++t) B[(s + 1) & 1][t] = ldsr16(bb + (s + 1) * 6144 + t * 1024);
    }
#pragma unroll
    for (int t = 0; t < 6; ++t) acc[t] = mfma(B[s & 1][t], a[s], acc[t]);
#pragma unroll
    for (int t = 0; t < 6; ++t) { __builtin_amdgcn_sched_group_barrier(0x100, 1, 0); __builtin_amdgcn_sched_group_barrier(0x8, 1, 0); }
    SCHED0();
  }
  STAMP(11);
  WAITV2(0, st0, st1);
  lbar();
  float s1 = (__uint_as_float(st0[0]) + __uint_as_float(st0[2])) + (__uint_as_float(st1[0]) + __uint_as_float(st1[2]));
  float s2 = (__uint_as_float(st0[1]) + __uint_as_float(st0[3])) + (__uint_as_float(st1[1]) + __uint_as_float(st1[3]));
  s1 = xrow_sum(s1); s2 = xrow_sum(s2);
  const float mean = s1 * (1.f / 640.f);
  const float var = fmaxf(s2 * (1.f / 640.f) - mean * mean, 0.f);
  const float rstd = __builtin_amdgcn_rsqf(var + 1e-5f), mr = -mean * rstd;
  const int g = lane >> 4;
  unsigned o[12];
#pragma unroll
  for (int t = 0; t < 6; ++t) {
    f32x4 cs = *(LAS f32x4*)(lds + L_P3_B1 + 1024 + (t * 16 + g * 4) * 4);
    v2u bb1 = *(LAS v2u*)(lds + L_P3_B1 + (t * 16 + g * 4) * 2);
    float y0 = fmaxf(fmaf(rstd, acc[t][0], fmaf(mr, cs[0], bflo(bb1.x))), 0.f), y1 = fmaxf(fmaf(rstd, acc[t][1], fmaf(mr, cs[1], bfhi(bb1.x))), 0.f);
    float y2 = fmaxf(fmaf(rstd, acc[t][2], fmaf(mr, cs[2], bflo(bb1.y))), 0.f), y3 = fmaxf(fmaf(rstd, acc[t][3], fmaf(mr, cs[3], bfhi(bb1.y))), 0.f);
    o[2 * t] = pk2(y0 * y0, y1 * y1); o[2 * t + 1] = pk2(y2 * y2, y3 * y3);
  }
#pragma unroll
  for (int q = 0; q < 3; ++q) st16(ws, v4u{o[4 * q], o[4 * q + 1], o[4 * q + 2], o[4 * q + 3]}, l16, WS_H1 + (unsigned)blk * 49152 + (3 * jj + q) * 1024);
  STAMP(12);
}

DEVI void phase4_prefetch(const Params& p, LAS char* lds, int lane, int w, u64 tag, bool wait0) {
  const int r = blockIdx.x >> 3, j = r & 7;
  rsrc_t ws = mk_rsrc(p.ws, WS_END);
  if (w == 7) return;
  if (wait0) wait_flags(p.ws, WS_F0B + (j * 64 + lane) * 8, lane < 48, tag);
  if (w == 0) { rsrc_t bs = mk_rsrc(p.b2, D_ * 2); dma16<0>(bs, lds + L_P4_B2, lane < 10 ? 160 * j + lane * 16 : 0x7fffffffu, 0); }
  for (int f = w; f < 120; f += 7) dma16<16>(ws, lds + f * 1024, lane * 16, WS_W2 + (unsigned)j * 245760 + f * 1024);
}
constexpr int P4D = 6;
constexpr int p4_nb(int r) { return (r == 3 || r == 7 || r == 11) ? 2 : ((r >= 4 && r <= 14) ? 1 : 0); }
constexpr int p4_na(int r) { return r + P4D < 24 ? 1 : 0; }
constexpr int p4_sum(int r0, int r1) { int n = 0; for (int r = r0; r < r1; ++r) n += p4_na(r) + p4_nb(r); return n; }
constexpr int p4_younger(int s) { return s < P4D ? (P4D - 1 - s) + p4_sum(0, s) : p4_nb(s - P4D) + p4_sum(s - P4D + 1, s); }
DEVI void phase4(const Params& p, LAS char* lds, int lane, int w, u64 tag, bool wait3, bool have_stash) {
  const int x8 = blockIdx.x & 7, r = blockIdx.x >> 3;
  const int ti = 2 * x8 + (r >> 4), hb = (r >> 3) & 1, j = r & 7;
  rsrc_t ws = mk_rsrc(p.ws, WS_END);
  const v4u srd = mk_srd(p.ws, WS_END);
  const unsigned l16 = lane * 16;
  const int b = w & 3, kh = w >> 2, blk = 8 * ti + 4 * hb + b;
  if (wait3 && w == 7) wait_flags(p.ws, WS_F3 + (ti * 16 + lane) * 8, lane < 16, tag);
  WAITV(0); BAR(); asm volatile("" ::: "memory");
  STAMP(14);
  const unsigned ha = WS_H1 + (unsigned)blk * 49152 + kh * 24 * 1024, wb = WS_W2 + (unsigned)j * 245760;
  v4u a[24];
#pragma unroll
  for (int s = 0; s < P4D; ++s) aload(a[s], srd, l16, ha + s * 1024);
  f32x4 acc[5];
#pragma unroll
  for (int t = 0; t < 5; ++t) acc[t] = f32x4{0.f, 0.f, 0.f, 0.f};
  v4u B[2][5];
  LAS char* bb = lds + kh * 20480 + l16;
#define P4_READ(s) do { _Pragma("unroll") for (int t = 0; t < 5; ++t) B[(s) & 1][t] = ldsr16(bb + (((s) >> 2) % 3) * 40960 + ((s) & 3) * 5120 + t * 1024); } while (0)
#define P4_B(stage, n) dma16<16>(ws, lds + ((stage) % 3) * 40960 + ((n) * 8 + w) * 1024, l16, wb + (stage) * 40960 + ((n) * 8 + w) * 1024)
  P4_READ(0);
  SCHED0();
#pragma unroll
  for (int s = 0; s < 24; ++s) {
    if ((s & 3) == 3 && s < 23) {
      if (s == 11 || s == 15) asm volatile("s_waitcnt vmcnt(9) lgkmcnt(0)" ::: "memory");
      else if (s == 19) asm volatile("s_waitcnt vmcnt(3) lgkmcnt(0)" ::: "memory");
      else asm volatile("s_waitcnt lgkmcnt(0)" ::: "memory");
      BAR(); asm volatile("" ::: "memory");
    }
    asm volatile("s_waitcnt vmcnt(%1)" : "+v"(a[s]) : "i"(p4_younger(s)) : "memory");
    if (s + P4D < 24) aload(a[s + P4D], srd, l16, ha + (s + P4D) * 1024);
    if (s + 1 < 24) P4_READ(s + 1);
#pragma unroll
    for (int t = 0; t < 5; ++t) acc[t] = mfma(B[s & 1][t], a[s], acc[t]);
#pragma unroll
    for (int t = 0; t < 5; ++t) { __builtin_amdgcn_sched_group_barrier(0x100, 1, 0); __builtin_amdgcn_sched_group_barrier(0x8, 1, 0); }
    SCHED0();
    if (p4_nb(s) > 0) {
      const int stage = 3 + (s - 3) / 4, first = ((s - 3) & 3) == 0 ? 0 : ((s - 3) & 3) + 1;
      P4_B(stage, first);
      if (p4_nb(s) == 2) P4_B(stage, first + 1);
      SCHED0();
    }
  }
#undef P4_READ
#undef P4_B
  STAMP(15);
  lbar();
  park_partials(lds, 0, acc, lane, w);
  lbar();
  for (int q = w * 64 + lane; q < 640; q += 512) {
    const int row = q / 10, G = q - row * 10;
    LAS char* s0 = lds + row * 336 + G * 32;
    f32x4 u0 = *(LAS f32x4*)s0, u1 = *(LAS f32x4*)(s0 + 16), w0 = *(LAS f32x4*)(s0 + 21504), w1 = *(LAS f32x4*)(s0 + 21504 + 16);
    v4u hv;
    const int tok = 128 * ti + 64 * hb + row;
    if (have_stash) hv = *(LAS v4u*)(lds + L_STASH + row * 160 + G * 16);
    else hv = __builtin_amdgcn_raw_buffer_load_b128(ws, (unsigned)(tok >> 4) * 20480 + (tok & 15) * 16 + (10 * j + G) * 256, WS_H, 0);
    v4u bv = *(LAS v4u*)(lds + L_P4_B2 + G * 16);
    float y[8];
    y[0] = u0[0] + w0[0] + bflo(bv[0]); y[1] = u0[1] + w0[1] + bfhi(bv[0]); y[2] = u0[2] + w0[2] + bflo(bv[1]); y[3] = u0[3] + w0[3] + bfhi(bv[1]);
    y[4] = u1[0] + w1[0] + bflo(bv[2]); y[5] = u1[1] + w1[1] + bfhi(bv[2]); y[6] = u1[2] + w1[2] + bflo(bv[3]); y[7] = u1[3] + w1[3] + bfhi(bv[3]);
    y[0] += bflo(hv[0]); y[1] += bfhi(hv[0]); y[2] += bflo(hv[1]); y[3] += bfhi(hv[1]); y[4] += bflo(hv[2]); y[5] += bfhi(hv[2]); y[6] += bflo(hv[3]); y[7] += bfhi(hv[3]);
    v4u ov = {pk2(y[0], y[1]), pk2(y[2], y[3]), pk2(y[4], y[5]), pk2(y[6], y[7])};
    *(GAS v4u*)((GAS char*)p.out + (size_t)tok * 1280 + 160 * j + 16 * G) = ov;
  }
  STAMP(16);
}

DEVI void wg_publish(const Params& p, unsigned off, u64 tag) {
  WAITV(0); lbar();
  if (threadIdx.x == 448) publish(p.ws, off, tag);
}

template <int PB, int PE>
__global__ __launch_bounds__(512, 2) __attribute__((amdgpu_waves_per_eu(2, 2))) void fused_block(Params p) {
  extern __shared__ __attribute__((aligned(16))) char smem[];
  LAS char* lds = (LAS char*)smem;
  const int lane = threadIdx.x & 63, w = __builtin_amdgcn_readfirstlane(threadIdx.x >> 6);
  const int x8 = blockIdx.x & 7, r = blockIdx.x >> 3;
  u64 tag = 1;
  unsigned pending = 0;
  STAMP(0);
  constexpr bool ALL = (PB == 0 && PE == 5);
  if (PB <= 0 && 0 < PE) {
    pending = phase0(p, lds, lane, w, tag, ALL);
    if (!ALL && PE == 1 && NLAUNCH == 2) {
      if (blockIdx.x == 0) for (int i = threadIdx.x; i < 3 * 512; i += 512) ((GAS u64*)(p.ws + WS_F1))[i] = 0;
    }
    STAMP(1);
  }
  constexpr bool IN1 = PB <= 1 && 2 < PE + 0, IN2 = PB <= 2 && 3 < PE + 0, IN3 = PB <= 3 && 4 < PE + 0;
  if (PB <= 1 && 1 < PE) {
    phase1(p, lds, lane, w, tag, ALL, pending);
    if (IN1) {
      const int ti = 4 * (x8 >> 1) + (r >> 3), pr = 8 * (x8 & 1) + (r & 7);
      wg_publish(p, WS_F1 + (ti * 16 + pr) * 8, tag);
    }
    STAMP(5);
  }
  if (PB <= 2 && 2 < PE) {
    phase2_prefetch(p, lds, lane, w, tag, ALL);
    phase2(p, lds, lane, w, tag, IN1);
    if (IN2) {
      const int ti = 2 * x8 + (r >> 4);
      wg_publish(p, WS_F2 + (ti * 16 + (r & 15)) * 8, tag);
    }
    STAMP(9);
  }
  if (PB <= 3 && 3 < PE) {
    phase3_prefetch(p, lds, lane, w, tag, ALL);
    phase3(p, lds, lane, w, tag, IN2);
    if (IN3) {
      const int ti = 4 * (x8 >> 1) + (r >> 3), jj = 8 * (x8 & 1) + (r & 7);
      wg_publish(p, WS_F3 + (ti * 16 + jj) * 8, tag);
    }
    STAMP(13);
  }
  if (PB <= 4 && 4 < PE) {
    phase4_prefetch(p, lds, lane, w, tag, ALL);
    if (ALL && blockIdx.x == 0 && w == 7) {
      for (int q = 0; q < 4; ++q) wait_flags(p.ws, WS_F1 + (q * 64 + lane) * 8, true, tag);
      if (lane == 0) publish(p.ws, WS_NONCE, tag);
    }
    phase4(p, lds, lane, w, tag, IN3, PB <= 2);
  }
}

extern "C" void kernel_launch(void* const* d_in, const int* in_sizes, int n_in, void* d_out, int out_size, void* d_ws, size_t ws_size, hipStream_t stream) {
  Params p;
  p.x = (const unsigned short*)d_in[0]; p.wqkv = (const unsigned short*)d_in[1]; p.wo = (const unsigned short*)d_in[2];
  p.w1 = (const unsigned short*)d_in[4]; p.b1 = (const unsigned short*)d_in[5]; p.w2 = (const unsigned short*)d_in[6]; p.b2 = (const unsigned short*)d_in[7];
  p.out = (unsigned short*)d_out; p.ws = (unsigned char*)d_ws;
  p.ws = (unsigned char*)d_ws + ((ws_size - WS_END - (1u << 21)) & ~(size_t)((1u << 21) - 1));
#define LAUNCH(a, b) do { hipFuncSetAttribute((const void*)fused_block<a, b>, hipFuncAttributeMaxDynamicSharedMemorySize, LDS_BYTES); \
    hipLaunchKernelGGL((fused_block<a, b>), dim3(256), dim3(512), LDS_BYTES, stream, p); } while (0)
  LAUNCH(0, 5);
}
```

```cpp
#include <hip/hip_runtime.h>
#include <hip/hip_bf16.h>
#include <stdint.h>

#define GAS __attribute__((address_space(1)))
#define LAS __attribute__((address_space(3)))
#define DEVI __device__ __forceinline__
typedef unsigned v4u __attribute__((ext_vector_type(4)));
typedef unsigned v2u __attribute__((ext_vector_type(2)));
typedef float f32x4 __attribute__((ext_vector_type(4)));
typedef short bf16x8 __attribute__((ext_vector_type(8)));
typedef short s16x4 __attribute__((ext_vector_type(4)));
typedef __bf16 bf16x2 __attribute__((ext_vector_type(2)));
typedef unsigned long long u64;
typedef GAS u64 gu64;
#define RLX_AGENT __ATOMIC_RELAXED, __HIP_MEMORY_SCOPE_AGENT

#define NLAUNCH 1
#define OUT_SC1 0
#define WS_AT_END 0
#define TOUCH 0
#define P1_SAME 0
#define DIAG 0

constexpr int T_ = 2048, D_ = 640, F_ = 1536;
constexpr unsigned WS_NONCE = 0;
constexpr unsigned WS_F0Q = 40960;
constexpr unsigned WS_F0O = 8192;
constexpr unsigned WS_F0A = 12288;
constexpr unsigned WS_F0B = 16384;
constexpr unsigned WS_F0X = 20480;
constexpr unsigned WS_F1 = 24576;
constexpr unsigned WS_F2 = 28672;
constexpr unsigned WS_F3 = 32768;
constexpr unsigned WS_XN = 131072;
constexpr unsigned WS_WQ = WS_XN + 128 * 20480;
constexpr unsigned WS_WO = WS_WQ + 16 * 20 * 8 * 1024;
constexpr unsigned WS_W1 = WS_WO + 8 * 20 * 5 * 1024;
constexpr unsigned WS_W2 = WS_W1 + 16 * 20 * 6 * 1024;
constexpr unsigned WS_O = WS_W2 + 8 * 48 * 5 * 1024;
constexpr unsigned WS_H = WS_O + 128 * 20480;
constexpr unsigned WS_H1 = WS_H + 128 * 20480;
constexpr unsigned WS_ST = WS_H1 + 128 * 49152;
constexpr unsigned WS_END = WS_ST + 128 * 2048;
constexpr unsigned LDS_BYTES = 163840;
constexpr unsigned L_STASH = 153600;
constexpr unsigned L_P0_LN = 65536;
constexpr unsigned L_TAG = 123904 + 1024;
#define SHADOW_SLEEP 24
constexpr unsigned P1_STAGE = 34816;
constexpr unsigned L_P1_EX = 102400;
constexpr unsigned P1_RS = 264;
constexpr unsigned L_EXH = 124928;
constexpr unsigned L_P3_B1 = 122880;
constexpr unsigned L_P4_B2 = 122880;

struct Params {
  const unsigned short* x; const unsigned short* wqkv; const unsigned short* wo; const unsigned short* w1; const unsigned short* b1;
  const unsigned short* w2; const unsigned short* b2; unsigned short* out; unsigned char* ws;
};

typedef __amdgpu_buffer_rsrc_t rsrc_t;
DEVI rsrc_t mk_rsrc(const void* p, unsigned bytes) { return __builtin_amdgcn_make_buffer_rsrc((void*)p, 0, bytes, 0x00020000); }
DEVI v4u mk_srd(const void* p, unsigned bytes) {
  u64 a = (u64)p; v4u r; r.x = (unsigned)a; r.y = (unsigned)(a >> 32) & 0xffffu; r.z = bytes; r.w = 0x00020000u; return r;
}
template <int AUX> DEVI void dma16(rsrc_t rs, LAS char* lds, unsigned voff, unsigned soff) {
  __builtin_amdgcn_raw_ptr_buffer_load_lds(rs, (LAS void*)lds, 16, voff, soff, 0, AUX);
}
DEVI void aload(v4u& d, v4u srd, unsigned voff, unsigned soff) {
  asm volatile("buffer_load_dwordx4 %0, %1, %2, %3 offen sc1" : "=v"(d) : "v"(voff), "s"(srd), "s"(soff) : "memory");
}
DEVI void st16(rsrc_t rs, v4u v, unsigned voff, unsigned soff) { __builtin_amdgcn_raw_buffer_store_b128(v, rs, voff + soff, 0, 16); }
#define WAITV(n) asm volatile("s_waitcnt vmcnt(%0)" ::"i"(n) : "memory")
#define WAITV1(n, a) asm volatile("s_waitcnt vmcnt(%1)" : "+v"(a) : "i"(n) : "memory")
#define WAITV2(n, a, b) asm volatile("s_waitcnt vmcnt(%2)" : "+v"(a), "+v"(b) : "i"(n) : "memory")
#define WAITV4(n, a, b, c, d) asm volatile("s_waitcnt vmcnt(%4)" : "+v"(a), "+v"(b), "+v"(c), "+v"(d) : "i"(n) : "memory")
#define WAITL0() asm volatile("s_waitcnt lgkmcnt(0)" ::: "memory")
#define BAR() __builtin_amdgcn_s_barrier()
#define SCHED0() __builtin_amdgcn_sched_barrier(0)
DEVI void lbar() { WAITL0(); BAR(); asm volatile("" ::: "memory"); }

DEVI unsigned pk2(float a, float b) { bf16x2 p; p[0] = (__bf16)a; p[1] = (__bf16)b; return __builtin_bit_cast(unsigned, p); }
DEVI float bflo(unsigned u) { return __uint_as_float(u << 16); }
DEVI float bfhi(unsigned u) { return __uint_as_float(u & 0xffff0000u); }
DEVI bf16x8 asbf(v4u v) { return __builtin_bit_cast(bf16x8, v); }
DEVI f32x4 mfma(v4u a, v4u b, f32x4 c) { return __builtin_amdgcn_mfma_f32_16x16x32_bf16(asbf(a), asbf(b), c, 0, 0, 0); }
DEVI v4u ldsr16(LAS char* p) { return *(LAS v4u*)p; }
DEVI float fsel(bool c, float a, float b) { asm("" : "+v"(a)); asm("" : "+v"(b)); return c ? a : b; }
DEVI float xrow_max(float v) {
  unsigned u = __float_as_uint(v);
  auto a = __builtin_amdgcn_permlane32_swap(u, u, false, false); v = fmaxf(__uint_as_float(a[0]), __uint_as_float(a[1]));
  u = __float_as_uint(v);
  auto b = __builtin_amdgcn_permlane16_swap(u, u, false, false); return fmaxf(__uint_as_float(b[0]), __uint_as_float(b[1]));
}
DEVI float xrow_sum(float v) {
  unsigned u = __float_as_uint(v);
  auto a = __builtin_amdgcn_permlane32_swap(u, u, false, false); v = __uint_as_float(a[0]) + __uint_as_float(a[1]);
  u = __float_as_uint(v);
  auto b = __builtin_amdgcn_permlane16_swap(u, u, false, false); return __uint_as_float(b[0]) + __uint_as_float(b[1]);
}
DEVI float row16_sum(float v) {
  v += __uint_as_float(__builtin_amdgcn_update_dpp(0u, __float_as_uint(v), 0x128, 0xf, 0xf, false));
  v += __uint_as_float(__builtin_amdgcn_update_dpp(0u, __float_as_uint(v), 0x124, 0xf, 0xf, false));
  v += __uint_as_float(__builtin_amdgcn_update_dpp(0u, __float_as_uint(v), 0x122, 0xf, 0xf, false));
  v += __uint_as_float(__builtin_amdgcn_update_dpp(0u, __float_as_uint(v), 0x121, 0xf, 0xf, false));
  return v;
}

DEVI u64 rfl64(u64 v) { return ((u64)__builtin_amdgcn_readfirstlane((unsigned)(v >> 32)) << 32) | (unsigned)__builtin_amdgcn_readfirstlane((unsigned)v); }
DEVI void publish(unsigned char* ws, unsigned off, u64 tag) { __hip_atomic_store((gu64*)(ws + off), tag, RLX_AGENT); }
DEVI void wait_flags(unsigned char* ws, unsigned laneoff, bool active, u64 tag) {
  for (unsigned sp = 0; sp < (1u << 18); ++sp) {
    u64 v = tag;
    if (active) v = __hip_atomic_load((gu64*)(ws + laneoff), RLX_AGENT);
    if (__all(v == tag)) break;
    __builtin_amdgcn_s_sleep(1);
  }
  __builtin_amdgcn_fence(__ATOMIC_ACQUIRE, "wavefront");
  asm volatile("" ::: "memory");
}

DEVI void wait_flags2(unsigned char* ws, unsigned off1, bool act1, unsigned off2, bool act2, u64 tag) {
  for (unsigned sp = 0; sp < (1u << 18); ++sp) {
    u64 v1 = tag, v2 = tag;
    if (act1) v1 = __hip_atomic_load((gu64*)(ws + off1), RLX_AGENT);
    if (act2) v2 = __hip_atomic_load((gu64*)(ws + off2), RLX_AGENT);
    if (__all(v1 == tag && v2 == tag)) break;
    __builtin_amdgcn_s_sleep(1);
  }
  __builtin_amdgcn_fence(__ATOMIC_ACQUIRE, "wavefront");
  asm volatile("" ::: "memory");
}

#define STAMP_ARG
#define STAMP_PASS
#define STAMP(id) do {} while (0)

template <int KIND>
DEVI unsigned retile_unit(const Params& p, rsrc_t ws, LAS char* st, int lane, int a, int ks STAMP_ARG) {
  constexpr int NCH = KIND == 2 ? 12 : 10;
  constexpr int NI = 32 * NCH / 64;
  constexpr int NT = KIND == 2 ? 6 : 5;
  constexpr unsigned RS = KIND == 2 ? 3072u : 1280u;
  const void* src = KIND == 1 ? (const void*)p.wo : KIND == 2 ? (const void*)p.w1 : (const void*)p.w2;
  constexpr unsigned SB = KIND == 1 ? 640u * 1280u : KIND == 2 ? 640u * 3072u : 1536u * 1280u;
  rsrc_t rs = mk_rsrc(src, SB);
#pragma unroll
  for (int n = 0; n < NI; ++n) {
    int q = n * 64 + lane;
    int rho = q / NCH, ch = q - rho * NCH;
    int row; unsigned col;
    const int g = rho >> 3, t = (rho >> 2) & 1, e = rho & 3;
    if (KIND == 1) { row = ks < 16 ? (2 * ks + t) * 20 + 4 * g + e : (2 * (4 * (ks - 16) + g) + t) * 20 + 16 + e; col = 160 * a + 16 * ch; }
    else if (KIND == 2) { row = 32 * ks + rho; col = 192 * a + 16 * ch; }
    else { row = 32 * ks + 16 * t + 4 * g + e; col = 160 * a + 16 * ch; }
    dma16<0>(rs, st + n * 1024, (unsigned)row * RS + col, 0);
  }
  WAITV(0);
  STAMP(18);
  unsigned dst;
  if (KIND == 1) dst = WS_WO + (unsigned)(a * 20 + ks) * 5120;
  else if (KIND == 2) dst = WS_W1 + (unsigned)(a * 20 + ks) * 6144;
  else { int kh = ks / 24, s = ks - kh * 24; dst = WS_W2 + (unsigned)a * 245760 + (s >> 2) * 40960 + kh * 20480 + (s & 3) * 5120; }
  const int g = lane >> 4, q4 = (lane & 15) >> 2, p4 = lane & 3;
  constexpr int RB = NCH * 16;
  LAS char* base = st + (8 * g + q4) * RB + 8 * p4;
#pragma unroll
  for (int t = 0; t < NT; ++t) {
    s16x4 lo = __builtin_amdgcn_ds_read_tr16_b64_v4i16((LAS s16x4*)(base + 32 * t));
    s16x4 hi = __builtin_amdgcn_ds_read_tr16_b64_v4i16((LAS s16x4*)(base + 4 * RB + 32 * t));
    v2u l2 = __builtin_bit_cast(v2u, lo), h2 = __builtin_bit_cast(v2u, hi);
    st16(ws, v4u{l2.x, l2.y, h2.x, h2.y}, lane * 16, dst + t * 1024);
  }
  STAMP(19);
  return KIND == 1 ? WS_F0O + (a * 32 + ks) * 8 : KIND == 2 ? WS_F0A + (a * 32 + ks) * 8 : WS_F0B + (a * 64 + ks) * 8;
}

DEVI void wqkv_unit(const Params& p, rsrc_t ws, LAS char* lds, int lane, int w, int u, u64 nonce, u64& tag, bool flags STAMP_ARG) {
  const int ks = u >> 2, hf = (u >> 1) & 1, rh = u & 1;
  rsrc_t rs = mk_rsrc(p.wqkv, 640u * 3840u);
#pragma unroll
  for (int m = 0; m < 4; ++m) {
    const int n = w + 8 * m;
    if (n < 30) {
      const int q = n * 64 + lane, row = q / 120, ch = q - row * 120, sec = ch / 40;
      dma16<0>(rs, lds + n * 1024, (unsigned)(32 * ks + 16 * rh + row) * 3840u + sec * 1280 + 640 * hf + 16 * (ch - sec * 40), 0);
    }
  }
  STAMP(17);
  WAITV(0);
  if (flags && w == 0) { asm volatile("" : "+v"(nonce)); if (lane == 0) *(LAS u64*)(lds + L_TAG) = nonce * 6364136223846793005ull + 1442695040888963407ull; }
  lbar();
  STAMP(18);
  if (flags) tag = rfl64(*(LAS u64*)(lds + L_TAG));
  const int gl = (lane >> 4) & 1, th = lane >> 5, q4 = (lane & 15) >> 2, p4 = lane & 3;
  LAS char* base = lds + (8 * gl + q4) * 1920 + 80 * w;
  const unsigned dst = WS_WQ + (unsigned)((8 * hf + w) * 20 + ks) * 8192;
  const unsigned vo = th * 1024 + (2 * rh + gl) * 256 + (lane & 15) * 16;
#pragma unroll
  for (int m = 0; m < 4; ++m) {
    int cb;
    if (m == 0) cb = 640 + 40 * th + 8 * p4;
    else if (m == 1) cb = th == 0 ? (p4 < 2 ? 640 : 0) + 32 + 40 * (p4 & 1) : 8 * p4;
    else if (m == 2) cb = th == 0 ? 40 + 8 * p4 : 1280 + 8 * p4;
    else cb = th == 0 ? 1280 + 40 + 8 * p4 : 1280 + 32 + 40 * (p4 & 1);
    s16x4 lo = __builtin_amdgcn_ds_read_tr16_b64_v4i16((LAS s16x4*)(base + cb));
    s16x4 hi = __builtin_amdgcn_ds_read_tr16_b64_v4i16((LAS s16x4*)(base + 4 * 1920 + cb));
    v2u l2 = __builtin_bit_cast(v2u, lo), h2 = __builtin_bit_cast(v2u, hi);
    v4u v = {l2.x, l2.y, h2.x, h2.y};
    if (m == 3 && th == 1 && (lane & 15) >= 8) v = v4u{0u, 0u, 0u, 0u};
    st16(ws, v, vo, dst + m * 2048);
  }
  STAMP(19);
  if (flags) { WAITV(0); STAMP(20); if (lane == 0) publish(p.ws, WS_F0Q + ((8 * hf + w) * 64 + ks * 2 + rh) * 8, tag); }
}

DEVI void ln_unit(const Params& p, rsrc_t ws, LAS char* lds, int lane, int w, int tb, u64 nonce, u64& tag, bool flags STAMP_ARG) {
  const int t = lane >> 4, i = lane & 15, c = 4 * w + t;
  const GAS v4u* src = (const GAS v4u*)((const GAS char*)p.x + (size_t)(tb * 16 + c) * 1280) + i;
  v4u r[5];
#pragma unroll
  for (int n = 0; n < 5; ++n) r[n] = src[16 * n];
  STAMP(17);
  float v[40];
#pragma unroll
  for (int n = 0; n < 5; ++n)
#pragma unroll
    for (int e = 0; e < 4; ++e) { v[n * 8 + 2 * e] = bflo(r[n][e]); v[n * 8 + 2 * e + 1] = bfhi(r[n][e]); }
  float s4[4] = {0.f, 0.f, 0.f, 0.f};
#pragma unroll
  for (int e = 0; e < 40; ++e) s4[e & 3] += v[e];
  const float mean = row16_sum((s4[0] + s4[1]) + (s4[2] + s4[3])) * (1.f / 640.f);
  float q4[4] = {0.f, 0.f, 0.f, 0.f};
#pragma unroll
  for (int e = 0; e < 40; ++e) { v[e] -= mean; q4[e & 3] = fmaf(v[e], v[e], q4[e & 3]); }
  const float rstd = __builtin_amdgcn_rsqf(row16_sum((q4[0] + q4[1]) + (q4[2] + q4[3])) * (1.f / 640.f) + 1e-5f);
  LAS char* st = lds + L_P0_LN;
#pragma unroll
  for (int n = 0; n < 5; ++n) {
    v4u o;
#pragma unroll
    for (int e = 0; e < 4; ++e) o[e] = pk2(v[n * 8 + 2 * e] * rstd, v[n * 8 + 2 * e + 1] * rstd);
    *(LAS v4u*)(st + (i + 16 * n) * 272 + c * 16) = o;
  }
  if (flags && w == 0) { if (lane == 0) *(LAS u64*)(lds + L_TAG) = nonce * 6364136223846793005ull + 1442695040888963407ull; }
  STAMP(18);
  lbar();
  if (flags) tag = rfl64(*(LAS u64*)(lds + L_TAG));
#pragma unroll
  for (int n = 0; n < 5; ++n) {
    const int ks = w * 5 + n;
    v4u o = ldsr16(st + (4 * ks + (lane >> 4)) * 272 + (lane & 15) * 16);
    st16(ws, o, lane * 16, WS_XN + (unsigned)tb * 20480 + ks * 1024);
  }
  STAMP(19);
  if (flags) { WAITV(0); STAMP(20); if (lane == 0) publish(p.ws, WS_F0X + (tb * 4 + w) * 8, tag); }
}

DEVI unsigned phase0(const Params& p, LAS char* lds, int lane, int w, u64& tag, bool flags STAMP_ARG) {
  rsrc_t ws = mk_rsrc(p.ws, WS_END);
  const int cu = blockIdx.x;
  u64 nonce = 0;
  if (flags && w == 0) nonce = __hip_atomic_load((gu64*)(p.ws + WS_NONCE), RLX_AGENT);
  if (cu < 128 && w < 4) { ln_unit(p, ws, lds, lane, w, cu, nonce, tag, flags STAMP_PASS); return 0; }
  if (cu >= 128 && cu < 208) { wqkv_unit(p, ws, lds, lane, w, cu - 128, nonce, tag, flags STAMP_PASS); return 0; }
  int slot;
  if (cu < 128) slot = cu * 4 + (w - 4);
  else {
    slot = 512 + (cu - 208) * 8 + w;
    if (flags && w == 0 && lane == 0) *(LAS u64*)(lds + L_TAG) = nonce * 6364136223846793005ull + 1442695040888963407ull;
  }
  lbar();
  if (flags) tag = rfl64(*(LAS u64*)(lds + L_TAG));
  if (cu >= 208) __builtin_amdgcn_s_sleep(SHADOW_SLEEP);
  STAMP(17);
  LAS char* st = lds + (cu < 128 ? (w - 4) : w) * 8192;
  if (slot < 160) return retile_unit<1>(p, ws, st, lane, slot & 7, slot >> 3 STAMP_PASS);
  if (slot < 480) { int u = slot - 160; return retile_unit<2>(p, ws, st, lane, u & 15, u >> 4 STAMP_PASS); }
  if (slot < 864) { int u = slot - 480; return retile_unit<3>(p, ws, st, lane, u & 7, u >> 3 STAMP_PASS); }
  return 0;
}

template <int NB>
DEVI void p1_loop(LAS char* lds, rsrc_t ws, const unsigned (&sb)[5], const unsigned (&si)[5], bool five, unsigned l16, int w,
                  unsigned t0o, unsigned t1o, unsigned xo, f32x4 (&acc)[2][5], v4u srd, unsigned fo1, unsigned fo2, v2u& f1, v2u& f2) {
#define P1_OP(s, i) dma16<16>(ws, lds + ((s) & 3) * P1_STAGE + (w + 8 * (i)) * 1024, l16, sb[i] + (s) * si[i])
  v4u W[2][2], X[2][NB];
#define P1_RO(ks) ((((ks) >> 1) & 3) * P1_STAGE + ((ks) & 1) * 17408)
#define P1_READ(ks) do { W[(ks) & 1][0] = ldsr16(lds + P1_RO(ks) + t0o); W[(ks) & 1][1] = ldsr16(lds + P1_RO(ks) + t1o); \
    _Pragma("unroll") for (int b = 0; b < NB; ++b) X[(ks) & 1][b] = ldsr16(lds + P1_RO(ks) + xo + b * 1024); } while (0)
#pragma unroll
  for (int s = 0; s < 3; ++s) { if (five) P1_OP(s, 4); P1_OP(s, 0); P1_OP(s, 1); P1_OP(s, 2); P1_OP(s, 3); }
  WAITV(8); BAR(); asm volatile("" ::: "memory");
  P1_READ(0);
  SCHED0();
#pragma unroll
  for (int ks = 0; ks < 20; ++ks) {
    if (ks & 1) {
      const int j = (ks + 1) >> 1;
      if (j <= 9) {
        if (j <= 7) asm volatile("s_waitcnt vmcnt(8) lgkmcnt(0)" ::: "memory");
        else if (j == 8) asm volatile("s_waitcnt vmcnt(4) lgkmcnt(0)" ::: "memory");
        else asm volatile("s_waitcnt vmcnt(0) lgkmcnt(0)" ::: "memory");
        BAR(); asm volatile("" ::: "memory");
      }
    }
    const int sn = ks == 0 ? 3 : ((ks + 1) >> 1) + 3;
    if (ks == 11) {
      asm volatile("buffer_load_dwordx2 %0, %1, %2, 0 offen sc1" : "=v"(f1) : "v"(fo1), "s"(srd) : "memory");
      asm volatile("buffer_load_dwordx2 %0, %1, %2, 0 offen sc1" : "=v"(f2) : "v"(fo2), "s"(srd) : "memory");
    }
    if (sn <= 9) {
      if (ks == 0) { if (five) P1_OP(3, 4); P1_OP(3, 0); P1_OP(3, 1); }
      else if (ks & 1) { if (five) P1_OP(sn, 4); P1_OP(sn, 0); }
      else P1_OP(sn, 2);
    }
    SCHED0();
    if (ks + 1 < 20) P1_READ(ks + 1);
#pragma unroll
    for (int b = 0; b < NB; ++b) { acc[0][b] = mfma(W[ks & 1][0], X[ks & 1][b], acc[0][b]); acc[1][b] = mfma(W[ks & 1][1], X[ks & 1][b], acc[1][b]); }
    __builtin_amdgcn_sched_group_barrier(0x100, 2, 0);
#pragma unroll
    for (int b = 0; b < NB; ++b) { __builtin_amdgcn_sched_group_barrier(0x8, 2, 0); __builtin_amdgcn_sched_group_barrier(0x100, 1, 0); }
    SCHED0();
    if (sn <= 9) {
      if (ks == 0) { P1_OP(3, 2); P1_OP(3, 3); }
      else if (ks & 1) P1_OP(sn, 1);
      else P1_OP(sn, 3);
    }
    SCHED0();
  }
#undef P1_OP
#undef P1_RO
#undef P1_READ
}

DEVI void phase1(const Params& p, LAS char* lds, int lane, int w, u64 tag, bool wait0, unsigned pending STAMP_ARG) {
  const int x8 = blockIdx.x & 7, r = blockIdx.x >> 3;
  const int ti = 4 * (x8 >> 1) + (r >> 3), pr = 8 * (x8 & 1) + (r & 7);
  rsrc_t ws = mk_rsrc(p.ws, WS_END);
  const v4u srd = mk_srd(p.ws, WS_END);
  const unsigned l16 = lane * 16;
  if (wait0) {
    if (pending) { WAITV(0); if (lane == 0) publish(p.ws, pending, tag); }
    if (w == 7) {
      int bq = (ti > 0 ? 8 * ti - 1 : 0) + (lane >> 2); if (ti == 0 && lane >= 32) bq = 7;
      wait_flags2(p.ws, WS_F0X + (bq * 4 + (lane & 3)) * 8, lane < 36, WS_F0Q + (pr * 64 + lane) * 8, lane < 40, tag);
    }
    lbar();
  }
  STAMP(2);
  unsigned sb[5], si[5];
#pragma unroll
  for (int i = 0; i < 5; ++i) {
    const int n = (w + 8 * i) < 34 ? (w + 8 * i) : 33, e = n >= 17 ? 1 : 0, f = n - 17 * e;
    int blk = 8 * ti - 1 + f; if (blk < 0) blk = 0;
    sb[i] = f < 9 ? WS_XN + (unsigned)blk * 20480 + e * 1024 : WS_WQ + (unsigned)pr * 163840 + (e * 8 + (f - 9)) * 1024;
    si[i] = f < 9 ? 2048u : 16384u;
  }
  const int np = w & 3, rg = w >> 2;
  const int T0 = np == 0 ? 0 : np == 1 ? 2 : np == 2 ? 5 : 3, T1 = np == 0 ? 1 : np == 1 ? 7 : np == 2 ? 6 : 4;
  f32x4 acc[2][5];
#pragma unroll
  for (int e = 0; e < 2; ++e)
#pragma unroll
    for (int b = 0; b < 5; ++b) acc[e][b] = f32x4{0.f, 0.f, 0.f, 0.f};
  const int j2 = r & 7, j3 = 8 * (x8 & 1) + (r & 7);
  const unsigned fo1 = lane < 20 ? WS_F0O + (j2 * 32 + lane) * 8 : WS_F0A + (j3 * 32 + (lane < 40 ? lane - 20 : 0)) * 8;
  const unsigned fo2 = WS_F0B + (j2 * 64 + (lane < 48 ? lane : 0)) * 8;
  v2u f1, f2;
  const unsigned t0o = (9 + T0) * 1024 + l16, t1o = (9 + T1) * 1024 + l16;
  if (rg == 0) p1_loop<5>(lds, ws, sb, si, w < 2, l16, w, t0o, t1o, l16, acc, srd, fo1, fo2, f1, f2);
  else p1_loop<4>(lds, ws, sb, si, false, l16, w, t0o, t1o, 5 * 1024 + l16, acc, srd, fo1, fo2, f1, f2);
  STAMP(3);
  const int g = lane >> 4, c = lane & 15;
  {
    LAS char* d = lds + L_P1_EX + (rg * 80 + c) * P1_RS + g * 8;
#pragma unroll
    for (int b = 0; b < 5; ++b) {
      if (b < 4 || rg == 0) {
        *(LAS v2u*)(d + b * 16 * P1_RS + T0 * 32) = v2u{pk2(acc[0][b][0], acc[0][b][1]), pk2(acc[0][b][2], acc[0][b][3])};
        *(LAS v2u*)(d + b * 16 * P1_RS + T1 * 32) = v2u{pk2(acc[1][b][0], acc[1][b][1]), pk2(acc[1][b][2], acc[1][b][3])};
      }
    }
  }
  if (wait0) {
    asm volatile("" : "+v"(f1), "+v"(f2));
    const bool ok = (((u64)f1.y << 32) | f1.x) == tag && (((u64)f2.y << 32) | f2.x) == tag;
    if (!__all(ok)) wait_flags2(p.ws, fo1, true, fo2, true, tag);
  }
  STAMP(22);
  lbar();
  STAMP(23);
  const int blk = 8 * ti + w;
  LAS char* own = lds + L_P1_EX + ((w + 1) * 16 + c) * P1_RS + g * 8;
  LAS char* trp = lds + L_P1_EX + ((w + 1) * 16 + 4 * g + (c >> 2)) * P1_RS + (c & 3) * 8;
  v2u ka[2], kp[2], qa[2], qr[2], va[2], vp[2];
  const v2u kr = *(LAS v2u*)(own + 64), krp = *(LAS v2u*)(own + 64 - 16 * P1_RS);
#pragma unroll
  for (int hh = 0; hh < 2; ++hh) {
    ka[hh] = *(LAS v2u*)(own + hh * 32); kp[hh] = *(LAS v2u*)(own + hh * 32 - 16 * P1_RS);
    qa[hh] = *(LAS v2u*)(own + (3 + hh) * 32);
    qr[hh] = *(LAS v2u*)(own - g * 8 + 64 + 16 + hh * 8);
    va[hh] = __builtin_bit_cast(v2u, __builtin_amdgcn_ds_read_tr16_b64_v4i16((LAS s16x4*)(trp + (5 + hh) * 32)));
    vp[hh] = __builtin_bit_cast(v2u, __builtin_amdgcn_ds_read_tr16_b64_v4i16((LAS s16x4*)(trp + (5 + hh) * 32 - 16 * P1_RS)));
  }
  const v2u vr = __builtin_bit_cast(v2u, __builtin_amdgcn_ds_read_tr16_b64_v4i16((LAS s16x4*)(trp + 7 * 32)));
  const v2u vrp = __builtin_bit_cast(v2u, __builtin_amdgcn_ds_read_tr16_b64_v4i16((LAS s16x4*)(trp + 7 * 32 - 16 * P1_RS)));
  STAMP(24);
  SCHED0();
  const int odd = c & 1, ce = c - odd;
  const bool havePrev = (ti > 0) || (w > 0);
  const bool v1a = havePrev && (4 * g >= ce + 2), v1b = havePrev && (4 * g >= ce), v2a = ce >= 4 * g, v2b = ce - 2 >= 4 * g;
  const float c2 = 0.22360679774997896f * 1.4426950408889634f, NEG = -3.0e38f;
  const f32x4 z = {0.f, 0.f, 0.f, 0.f};
  f32x4 S1[2], S2[2];
#pragma unroll
  for (int hh = 0; hh < 2; ++hh) {
    const bool mine = g == hh;
    const v4u Q = {qa[hh].x, qa[hh].y, mine ? qr[hh].x : 0u, mine ? qr[hh].y : 0u};
    S1[hh] = mfma(v4u{kp[hh].x, kp[hh].y, krp.x, krp.y}, Q, z);
    S2[hh] = mfma(v4u{ka[hh].x, ka[hh].y, kr.x, kr.y}, Q, z);
  }
  float s1a[2], s1b[2], s2a[2], s2b[2], m[2], inv[2];
#pragma unroll
  for (int hh = 0; hh < 2; ++hh) {
    s1a[hh] = fsel(v1a, fsel(odd, S1[hh][1], S1[hh][0]), NEG); s1b[hh] = fsel(v1b, fsel(odd, S1[hh][3], S1[hh][2]), NEG);
    s2a[hh] = fsel(v2a, fsel(odd, S2[hh][1], S2[hh][0]), NEG); s2b[hh] = fsel(v2b, fsel(odd, S2[hh][3], S2[hh][2]), NEG);
    m[hh] = fmaxf(fmaxf(s1a[hh], s1b[hh]), fmaxf(s2a[hh], s2b[hh]));
  }
  {
    auto a0 = __builtin_amdgcn_permlane32_swap(__float_as_uint(m[0]), __float_as_uint(m[0]), false, false);
    auto a1 = __builtin_amdgcn_permlane32_swap(__float_as_uint(m[1]), __float_as_uint(m[1]), false, false);
    m[0] = fmaxf(__uint_as_float(a0[0]), __uint_as_float(a0[1])); m[1] = fmaxf(__uint_as_float(a1[0]), __uint_as_float(a1[1]));
    auto b0 = __builtin_amdgcn_permlane16_swap(__float_as_uint(m[0]), __float_as_uint(m[0]), false, false);
    auto b1 = __builtin_amdgcn_permlane16_swap(__float_as_uint(m[1]), __float_as_uint(m[1]), false, false);
    m[0] = fmaxf(__uint_as_float(b0[0]), __uint_as_float(b0[1])); m[1] = fmaxf(__uint_as_float(b1[0]), __uint_as_float(b1[1]));
  }
#pragma unroll
  for (int hh = 0; hh < 2; ++hh) {
    s1a[hh] = __builtin_amdgcn_exp2f((s1a[hh] - m[hh]) * c2); s1b[hh] = __builtin_amdgcn_exp2f((s1b[hh] - m[hh]) * c2);
    s2a[hh] = __builtin_amdgcn_exp2f((s2a[hh] - m[hh]) * c2); s2b[hh] = __builtin_amdgcn_exp2f((s2b[hh] - m[hh]) * c2);
    inv[hh] = (s1a[hh] + s1b[hh]) + (s2a[hh] + s2b[hh]);
  }
  {
    auto a0 = __builtin_amdgcn_permlane32_swap(__float_as_uint(inv[0]), __float_as_uint(inv[0]), false, false);
    auto a1 = __builtin_amdgcn_permlane32_swap(__float_as_uint(inv[1]), __float_as_uint(inv[1]), false, false);
    inv[0] = __uint_as_float(a0[0]) + __uint_as_float(a0[1]); inv[1] = __uint_as_float(a1[0]) + __uint_as_float(a1[1]);
    auto b0 = __builtin_amdgcn_permlane16_swap(__float_as_uint(inv[0]), __float_as_uint(inv[0]), false, false);
    auto b1 = __builtin_amdgcn_permlane16_swap(__float_as_uint(inv[1]), __float_as_uint(inv[1]), false, false);
    inv[0] = __builtin_amdgcn_rcpf(__uint_as_float(b0[0]) + __uint_as_float(b0[1])); inv[1] = __builtin_amdgcn_rcpf(__uint_as_float(b1[0]) + __uint_as_float(b1[1]));
  }
  STAMP(25);
  f32x4 OA[2], OR[2];
  const unsigned sh = 16 * odd;
#pragma unroll
  for (int hh = 0; hh < 2; ++hh) {
    const v4u P = {pk2(s1a[hh] * inv[hh], 0.f) << sh, pk2(s1b[hh] * inv[hh], 0.f) << sh, pk2(s2a[hh] * inv[hh], 0.f) << sh, pk2(s2b[hh] * inv[hh], 0.f) << sh};
    OA[hh] = mfma(v4u{vp[hh].x, vp[hh].y, va[hh].x, va[hh].y}, P, z);
    OR[hh] = mfma(v4u{vrp.x, vrp.y, vr.x, vr.y}, P, z);
  }
  {
    v4u om = {pk2(OA[0][0], OA[0][1]), pk2(OA[0][2], OA[0][3]), pk2(OA[1][0], OA[1][1]), pk2(OA[1][2], OA[1][3])};
    st16(ws, om, l16, WS_O + (unsigned)blk * 20480 + pr * 1024);
    unsigned r0x = pk2(OR[0][0], OR[0][1]), r0y = pk2(OR[0][2], OR[0][3]), r1x = pk2(OR[1][0], OR[1][1]), r1y = pk2(OR[1][2], OR[1][3]);
    auto t0 = __builtin_amdgcn_permlane16_swap(r1x, r1x, false, false); auto t1 = __builtin_amdgcn_permlane16_swap(r1y, r1y, false, false);
    v4u orr = {r0x, r0y, t0[1], t1[1]};
    if (lane < 16) st16(ws, orr, l16, WS_O + (unsigned)blk * 20480 + (16 + (pr >> 2)) * 1024 + (pr & 3) * 256);
  }
  STAMP(4);
}

DEVI void exchange_halves(LAS char* lds, const f32x4 (&acc)[5], f32x4 (&fin)[3], int lane, int w) {
  LAS char* d = lds + L_EXH + (w & 3) * 5120 + lane * 16;
  if (w < 4) { *(LAS f32x4*)(d + 3072) = acc[3]; *(LAS f32x4*)(d + 4096) = acc[4]; }
  else { *(LAS f32x4*)(d) = acc[0]; *(LAS f32x4*)(d + 1024) = acc[1]; *(LAS f32x4*)(d + 2048) = acc[2]; }
  lbar();
  if (w < 4) { f32x4 u0 = *(LAS f32x4*)(d), u1 = *(LAS f32x4*)(d + 1024), u2 = *(LAS f32x4*)(d + 2048); fin[0] = acc[0] + u0; fin[1] = acc[1] + u1; fin[2] = acc[2] + u2; }
  else { f32x4 u0 = *(LAS f32x4*)(d + 3072), u1 = *(LAS f32x4*)(d + 4096); fin[0] = acc[3] + u0; fin[1] = acc[4] + u1; fin[2] = f32x4{0.f, 0.f, 0.f, 0.f}; }
}

DEVI void phase2_prefetch(const Params& p, LAS char* lds, int lane, int w, u64 tag, bool wait0) {
  const int x8 = blockIdx.x & 7, r = blockIdx.x >> 3;
  const int ti = 2 * x8 + (r >> 4), hb = (r >> 3) & 1, j = r & 7;
  rsrc_t ws = mk_rsrc(p.ws, WS_END);
  if (w == 7) return;
#pragma unroll
  for (int n = 0; n < 15; ++n) { const int f = n * 7 + w; if (n < 14 || f < 100) dma16<16>(ws, lds + f * 1024, lane * 16, WS_WO + (unsigned)j * 102400 + f * 1024); }
  {
    rsrc_t xs = mk_rsrc(p.x, T_ * 1280);
    for (int n = w; n < 10; n += 7) {
      int q = n * 64 + lane, row = q / 10, ch = q - row * 10;
      dma16<0>(xs, lds + L_STASH + n * 1024, (unsigned)(128 * ti + 64 * hb + row) * 1280 + 160 * j + 16 * ch, 0);
    }
  }
}
DEVI void phase2(const Params& p, LAS char* lds, int lane, int w, u64 tag, bool wait1 STAMP_ARG) {
  const int x8 = blockIdx.x & 7, r = blockIdx.x >> 3;
  const int ti = 2 * x8 + (r >> 4), hb = (r >> 3) & 1, j = r & 7;
  rsrc_t ws = mk_rsrc(p.ws, WS_END);
  const v4u srd = mk_srd(p.ws, WS_END);
  const unsigned l16 = lane * 16;
  const int b = w & 3, kh = w >> 2, blk = 8 * ti + 4 * hb + b;
  if (wait1 && w == 7) wait_flags(p.ws, WS_F1 + (ti * 16 + lane) * 8, lane < 16, tag);
  WAITV(0); BAR(); asm volatile("" ::: "memory");
  STAMP(6);
  v4u a[10];
  const unsigned oa = WS_O + (unsigned)blk * 20480 + 10 * kh * 1024;
#pragma unroll
  for (int s = 0; s < 10; ++s) aload(a[s], srd, l16, oa + s * 1024);
  f32x4 acc[5];
#pragma unroll
  for (int t = 0; t < 5; ++t) acc[t] = f32x4{0.f, 0.f, 0.f, 0.f};
  v4u B[2][5];
  LAS char* bb = lds + kh * 51200 + l16;
#pragma unroll
  for (int t = 0; t < 5; ++t) B[0][t] = ldsr16(bb + t * 1024);
  SCHED0();
#pragma unroll
  for (int s = 0; s < 10; ++s) {
    asm volatile("s_waitcnt vmcnt(%1)" : "+v"(a[s]) : "i"(9 - s) : "memory");
    if (s + 1 < 10) {
#pragma unroll
      for (int t = 0; t < 5; ++t) B[(s + 1) & 1][t] = ldsr16(bb + (s + 1) * 5120 + t * 1024);
    }
#pragma unroll
    for (int t = 0; t < 5; ++t) acc[t] = mfma(B[s & 1][t], a[s], acc[t]);
#pragma unroll
    for (int t = 0; t < 5; ++t) { __builtin_amdgcn_sched_group_barrier(0x100, 1, 0); __builtin_amdgcn_sched_group_barrier(0x8, 1, 0); }
    SCHED0();
  }
  STAMP(7);
  const int g = lane >> 4, c = lane & 15, row = 16 * b + c, mt0 = 3 * kh;
  LAS char* sx = lds + L_STASH + row * 160 + mt0 * 32 + g * 8;
  v2u xv[3];
#pragma unroll
  for (int e = 0; e < 3; ++e) xv[e] = *(LAS v2u*)(sx + (e < 2 || kh == 0 ? e : 0) * 32);
  f32x4 fin[3];
  exchange_halves(lds, acc, fin, lane, w);
  v2u hv[3];
  float ssum = 0.f, ssq = 0.f;
#pragma unroll
  for (int e = 0; e < 3; ++e) {
    hv[e] = v2u{pk2(fin[e][0] + bflo(xv[e].x), fin[e][1] + bfhi(xv[e].x)), pk2(fin[e][2] + bflo(xv[e].y), fin[e][3] + bfhi(xv[e].y))};
    if (e < 2 || kh == 0) {
      *(LAS v2u*)(sx + e * 32) = hv[e];
      const float h0 = bflo(hv[e].x), h1 = bfhi(hv[e].x), h2 = bflo(hv[e].y), h3 = bfhi(hv[e].y);
      ssum += (h0 + h1) + (h2 + h3); ssq += fmaf(h0, h0, h1 * h1) + fmaf(h2, h2, h3 * h3);
    }
  }
  {
    auto sx0 = __builtin_amdgcn_permlane16_swap(hv[0].x, hv[1].x, false, false); auto sy0 = __builtin_amdgcn_permlane16_swap(hv[0].y, hv[1].y, false, false);
    const int G = 2 * (mt0 + (g & 1)) + (g >> 1);
    st16(ws, v4u{sx0[0], sy0[0], sx0[1], sy0[1]}, c * 16 + (10 * j + G) * 256, WS_H + (unsigned)blk * 20480);
    if (kh == 0) __builtin_amdgcn_raw_buffer_store_b64(hv[2], ws, WS_H + (unsigned)blk * 20480 + (10 * j + 4 + (g >> 1)) * 256 + c * 16 + (g & 1) * 8, 0, 16);
  }
  ssum = xrow_sum(ssum); ssq = xrow_sum(ssq);
  {
    const int idx = 2 * j + kh;
    const int e = idx & 3;
    if (lane < 16) {
      v2u sv = {__float_as_uint(ssum), __float_as_uint(ssq)};
      __builtin_amdgcn_raw_buffer_store_b64(sv, ws, WS_ST + (unsigned)blk * 2048 + (e >> 1) * 1024 + (idx >> 2) * 256 + c * 16 + (e & 1) * 8, 0, 16);
    }
  }
  STAMP(8);
}

DEVI void phase3_prefetch(const Params& p, LAS char* lds, int lane, int w, u64 tag, bool wait0) {
  const int x8 = blockIdx.x & 7, r = blockIdx.x >> 3;
  const int jj = 8 * (x8 & 1) + (r & 7);
  rsrc_t ws = mk_rsrc(p.ws, WS_END);
  if (w == 7) return;
  if (w == 0) { rsrc_t bs = mk_rsrc(p.b1, F_ * 2); dma16<0>(bs, lds + L_P3_B1, lane < 12 ? 192 * jj + lane * 16 : 0x7fffffffu, 0); }
#pragma unroll
  for (int n = 0; n < 18; ++n) { const int f = n * 7 + w; if (n < 17 || f < 120) dma16<16>(ws, lds + f * 1024, lane * 16, WS_W1 + (unsigned)jj * 122880 + f * 1024); }
}
#define P3D 12
DEVI void phase3(const Params& p, LAS char* lds, int lane, int w, u64 tag, bool wait2 STAMP_ARG) {
  const int x8 = blockIdx.x & 7, r = blockIdx.x >> 3;
  const int ti = 4 * (x8 >> 1) + (r >> 3), jj = 8 * (x8 & 1) + (r & 7);
  rsrc_t ws = mk_rsrc(p.ws, WS_END);
  const v4u srd = mk_srd(p.ws, WS_END);
  const unsigned l16 = lane * 16;
  const int blk = 8 * ti + w;
  WAITV(0); BAR(); asm volatile("" ::: "memory");
  if (w < 6) {
    v4u cf[20];
#pragma unroll
    for (int ks = 0; ks < 20; ++ks) cf[ks] = ldsr16(lds + (ks * 6 + w) * 1024 + l16);
    SCHED0();
    f32x4 cs0 = {0.f, 0.f, 0.f, 0.f}, cs1 = cs0;
    const v4u ones = {0x3f803f80u, 0x3f803f80u, 0x3f803f80u, 0x3f803f80u};
#pragma unroll
    for (int ks = 0; ks < 20; ks += 2) { cs0 = mfma(cf[ks], ones, cs0); cs1 = mfma(cf[ks + 1], ones, cs1); }
    if ((lane & 15) == 0) *(LAS f32x4*)(lds + L_P3_B1 + 1024 + (w * 16 + (lane >> 4) * 4) * 4) = cs0 + cs1;
  }
  if (wait2 && w == 7) wait_flags(p.ws, WS_F2 + (ti * 16 + lane) * 8, lane < 16, tag);
  lbar();
  STAMP(10);
  v4u a[20], st0, st1;
  const unsigned ha = WS_H + (unsigned)blk * 20480;
#pragma unroll
  for (int s = 0; s < P3D; ++s) aload(a[s], srd, l16, ha + s * 1024);
  f32x4 acc[6];
#pragma unroll
  for (int t = 0; t < 6; ++t) acc[t] = f32x4{0.f, 0.f, 0.f, 0.f};
  v4u B[2][6];
  LAS char* bb = lds + l16;
#pragma unroll
  for (int t = 0; t < 6; ++t) B[0][t] = ldsr16(bb + t * 1024);
  SCHED0();
#pragma unroll
  for (int s = 0; s < 20; ++s) {
    asm volatile("s_waitcnt vmcnt(%1)" : "+v"(a[s]) : "i"(s + P3D - 1 < 22 ? P3D - 1 : 21 - s) : "memory");
    if (s + P3D < 20) aload(a[s + P3D], srd, l16, ha + (s + P3D) * 1024);
    else if (s + P3D == 20) aload(st0, srd, l16, WS_ST + (unsigned)blk * 2048);
    else if (s + P3D == 21) aload(st1, srd, l16, WS_ST + (unsigned)blk * 2048 + 1024);
    if (s + 1 < 20) {
#pragma unroll
      for (int t = 0; t < 6; ++t) B[(s + 1) & 1][t] = ldsr16(bb + (s + 1) * 6144 + t * 1024);
    }
#pragma unroll
    for (int t = 0; t < 6; ++t) acc[t] = mfma(B[s & 1][t], a[s], acc[t]);
#pragma unroll
    for (int t = 0; t < 6; ++t) { __builtin_amdgcn_sched_group_barrier(0x100, 1, 0); __builtin_amdgcn_sched_group_barrier(0x8, 1, 0); }
    SCHED0();
  }
  STAMP(11);
  const int g = lane >> 4;
  f32x4 cs[6]; v2u bb1[6];
#pragma unroll
  for (int t = 0; t < 6; ++t) { cs[t] = *(LAS f32x4*)(lds + L_P3_B1 + 1024 + (t * 16 + g * 4) * 4); bb1[t] = *(LAS v2u*)(lds + L_P3_B1 + (t * 16 + g * 4) * 2); }
  WAITV2(0, st0, st1);
  SCHED0();
  float s1 = (__uint_as_float(st0[0]) + __uint_as_float(st0[2])) + (__uint_as_float(st1[0]) + __uint_as_float(st1[2]));
  float s2 = (__uint_as_float(st0[1]) + __uint_as_float(st0[3])) + (__uint_as_float(st1[1]) + __uint_as_float(st1[3]));
  s1 = xrow_sum(s1); s2 = xrow_sum(s2);
  const float mean = s1 * (1.f / 640.f);
  const float var = fmaxf(s2 * (1.f / 640.f) - mean * mean, 0.f);
  const float rstd = __builtin_amdgcn_rsqf(var + 1e-5f), mr = -mean * rstd;
  unsigned o[12];
#pragma unroll
  for (int t = 0; t < 6; ++t) {
    float y0 = fmaxf(fmaf(rstd, acc[t][0], fmaf(mr, cs[t][0], bflo(bb1[t].x))), 0.f), y1 = fmaxf(fmaf(rstd, acc[t][1], fmaf(mr, cs[t][1], bfhi(bb1[t].x))), 0.f);
    float y2 = fmaxf(fmaf(rstd, acc[t][2], fmaf(mr, cs[t][2], bflo(bb1[t].y))), 0.f), y3 = fmaxf(fmaf(rstd, acc[t][3], fmaf(mr, cs[t][3], bfhi(bb1[t].y))), 0.f);
    o[2 * t] = pk2(y0 * y0, y1 * y1); o[2 * t + 1] = pk2(y2 * y2, y3 * y3);
  }
#pragma unroll
  for (int q = 0; q < 3; ++q) st16(ws, v4u{o[4 * q], o[4 * q + 1], o[4 * q + 2], o[4 * q + 3]}, l16, WS_H1 + (unsigned)blk * 49152 + (3 * jj + q) * 1024);
  STAMP(12);
}

DEVI void phase4_prefetch(const Params& p, LAS char* lds, int lane, int w, u64 tag, bool wait0) {
  const int r = blockIdx.x >> 3, j = r & 7;
  rsrc_t ws = mk_rsrc(p.ws, WS_END);
  if (w == 7) return;
  if (w == 0) { rsrc_t bs = mk_rsrc(p.b2, D_ * 2); dma16<0>(bs, lds + L_P4_B2, lane < 10 ? 160 * j + lane * 16 : 0x7fffffffu, 0); }
#pragma unroll
  for (int n = 0; n < 18; ++n) { const int f = n * 7 + w; if (n < 17 || f < 120) dma16<16>(ws, lds + f * 1024, lane * 16, WS_W2 + (unsigned)j * 245760 + f * 1024); }
}
#define P4DD 12
constexpr int P4D = P4DD;
constexpr int p4_nb(int r) { return (r == 3 || r == 7 || r == 11) ? 2 : ((r >= 4 && r <= 14) ? 1 : 0); }
constexpr int p4_na(int r) { return r + P4D < 24 ? 1 : 0; }
constexpr int p4_sum(int r0, int r1) { int n = 0; for (int r = r0; r < r1; ++r) n += p4_na(r) + p4_nb(r); return n; }
constexpr int p4_younger(int s) { return s < P4D ? (P4D - 1 - s) + p4_sum(0, s) : p4_nb(s - P4D) + p4_sum(s - P4D + 1, s); }
DEVI void phase4(const Params& p, LAS char* lds, int lane, int w, u64 tag, bool wait3, bool have_stash STAMP_ARG) {
  const int x8 = blockIdx.x & 7, r = blockIdx.x >> 3;
  const int ti = 2 * x8 + (r >> 4), hb = (r >> 3) & 1, j = r & 7;
  rsrc_t ws = mk_rsrc(p.ws, WS_END);
  const v4u srd = mk_srd(p.ws, WS_END);
  const unsigned l16 = lane * 16;
  const int b = w & 3, kh = w >> 2, blk = 8 * ti + 4 * hb + b;
  if (wait3 && w == 7) wait_flags(p.ws, WS_F3 + (ti * 16 + lane) * 8, lane < 16, tag);
  WAITV(0); BAR(); asm volatile("" ::: "memory");
  STAMP(14);
  const unsigned ha = WS_H1 + (unsigned)blk * 49152 + kh * 24 * 1024, wb = WS_W2 + (unsigned)j * 245760;
  v4u a[24];
#pragma unroll
  for (int s = 0; s < P4D; ++s) aload(a[s], srd, l16, ha + s * 1024);
  f32x4 acc[5];
#pragma unroll
  for (int t = 0; t < 5; ++t) acc[t] = f32x4{0.f, 0.f, 0.f, 0.f};
  v4u B[2][5];
  LAS char* bb = lds + kh * 20480 + l16;
#define P4_READ(s) do { _Pragma("unroll") for (int t = 0; t < 5; ++t) B[(s) & 1][t] = ldsr16(bb + (((s) >> 2) % 3) * 40960 + ((s) & 3) * 5120 + t * 1024); } while (0)
#define P4_B(stage, n) dma16<16>(ws, lds + ((stage) % 3) * 40960 + ((n) * 8 + w) * 1024, l16, wb + (stage) * 40960 + ((n) * 8 + w) * 1024)
  P4_READ(0);
  SCHED0();
#pragma unroll
  for (int s = 0; s < 24; ++s) {
    if ((s & 3) == 3 && s < 23) {
      if (s == 11 || s == 15 || s == 19) asm volatile("s_waitcnt vmcnt(%0) lgkmcnt(0)" ::"i"(p4_sum(s - 4, s)) : "memory");
      else asm volatile("s_waitcnt lgkmcnt(0)" ::: "memory");
      BAR(); asm volatile("" ::: "memory");
    }
    asm volatile("s_waitcnt vmcnt(%1)" : "+v"(a[s]) : "i"(p4_younger(s)) : "memory");
    if (s + P4D < 24) aload(a[s + P4D], srd, l16, ha + (s + P4D) * 1024);
    if (s + 1 < 24) P4_READ(s + 1);
#pragma unroll
    for (int t = 0; t < 5; ++t) acc[t] = mfma(B[s & 1][t], a[s], acc[t]);
#pragma unroll
    for (int t = 0; t < 5; ++t) { __builtin_amdgcn_sched_group_barrier(0x100, 1, 0); __builtin_amdgcn_sched_group_barrier(0x8, 1, 0); }
    SCHED0();
    if (p4_nb(s) > 0) {
      const int stage = 3 + (s - 3) / 4, first = ((s - 3) & 3) == 0 ? 0 : ((s - 3) & 3) + 1;
      P4_B(stage, first);
      if (p4_nb(s) == 2) P4_B(stage, first + 1);
      SCHED0();
    }
  }
#undef P4_READ
#undef P4_B
  STAMP(15);
  const int g = lane >> 4, c = lane & 15, row = 16 * b + c, mt0 = 3 * kh;
  LAS char* sx = lds + L_STASH + row * 160 + mt0 * 32 + g * 8;
  v2u hv[3], bv[3];
#pragma unroll
  for (int e = 0; e < 3; ++e) {
    const int ee = (e < 2 || kh == 0) ? e : 0;
    if (have_stash) hv[e] = *(LAS v2u*)(sx + ee * 32);
    else { const int f8 = 10 * j + 2 * (mt0 + ee) + (g >> 1); hv[e] = __builtin_amdgcn_raw_buffer_load_b64(ws, WS_H + (unsigned)blk * 20480 + f8 * 256 + c * 16 + (g & 1) * 8, 0, 0); }
    bv[e] = *(LAS v2u*)(lds + L_P4_B2 + (mt0 + ee) * 32 + g * 8);
  }
  f32x4 fin[3];
  exchange_halves(lds, acc, fin, lane, w);
#pragma unroll
  for (int e = 0; e < 3; ++e) {
    if (e < 2 || kh == 0)
      *(LAS v2u*)(sx + e * 32) = v2u{pk2(fin[e][0] + bflo(bv[e].x) + bflo(hv[e].x), fin[e][1] + bfhi(bv[e].x) + bfhi(hv[e].x)),
                                     pk2(fin[e][2] + bflo(bv[e].y) + bflo(hv[e].y), fin[e][3] + bfhi(bv[e].y) + bfhi(hv[e].y))};
  }
  lbar();
  {
    const int q0 = w * 64 + lane, q1 = q0 + 512;
    const v4u o0 = *(LAS v4u*)(lds + L_STASH + q0 * 16);
    v4u o1 = o0; if (w < 2) o1 = *(LAS v4u*)(lds + L_STASH + q1 * 16);
    const int r0 = q0 / 10, r1 = q1 / 10;
    rsrc_t outr = mk_rsrc(p.out, T_ * 1280);
    const unsigned ob = (unsigned)(128 * ti + 64 * hb) * 1280 + 160 * j;
    __builtin_amdgcn_raw_buffer_store_b128(o0, outr, ob + r0 * 1280 + (q0 - r0 * 10) * 16, 0, 0);
    if (w < 2) __builtin_amdgcn_raw_buffer_store_b128(o1, outr, ob + r1 * 1280 + (q1 - r1 * 10) * 16, 0, 0);
  }
  STAMP(16);
}

DEVI void wg_publish(const Params& p, unsigned off, u64 tag) {
  WAITV(0); lbar();
  if (threadIdx.x == 448) publish(p.ws, off, tag);
}

template <int PB, int PE>
__global__ __launch_bounds__(512, 2) __attribute__((amdgpu_waves_per_eu(2, 2))) void fused_block(Params p) {
  extern __shared__ __attribute__((aligned(16))) char smem[];
  LAS char* lds = (LAS char*)smem;
  const int lane = threadIdx.x & 63, w = __builtin_amdgcn_readfirstlane(threadIdx.x >> 6);
  const int x8 = blockIdx.x & 7, r = blockIdx.x >> 3;
  u64 tag = 1;
  unsigned pending = 0;
  STAMP(0);
  constexpr bool ALL = (PB == 0 && PE == 5);
  if (PB <= 0 && 0 < PE) {
    pending = phase0(p, lds, lane, w, tag, ALL STAMP_PASS);
    if (!ALL && PE == 1 && NLAUNCH == 2) {
      if (blockIdx.x == 0) for (int i = threadIdx.x; i < 3 * 512; i += 512) ((GAS u64*)(p.ws + WS_F1))[i] = 0;
    }
    STAMP(1);
  }
  constexpr bool IN1 = PB <= 1 && 2 < PE + 0, IN2 = PB <= 2 && 3 < PE + 0, IN3 = PB <= 3 && 4 < PE + 0;
  if (PB <= 1 && 1 < PE) {
    phase1(p, lds, lane, w, tag, ALL, pending STAMP_PASS);
    if (IN1) {
      const int ti = 4 * (x8 >> 1) + (r >> 3), pr = 8 * (x8 & 1) + (r & 7);
      wg_publish(p, WS_F1 + (ti * 16 + pr) * 8, tag);
    }
    STAMP(5);
  }
  if (PB <= 2 && 2 < PE) {
    phase2_prefetch(p, lds, lane, w, tag, ALL);
    phase2(p, lds, lane, w, tag, IN1 STAMP_PASS);
    if (IN2) {
      const int ti = 2 * x8 + (r >> 4);
      wg_publish(p, WS_F2 + (ti * 16 + (r & 15)) * 8, tag);
    }
    STAMP(9);
  }
  if (PB <= 3 && 3 < PE) {
    phase3_prefetch(p, lds, lane, w, tag, ALL);
    phase3(p, lds, lane, w, tag, IN2 STAMP_PASS);
    if (IN3) {
      const int ti = 4 * (x8 >> 1) + (r >> 3), jj = 8 * (x8 & 1) + (r & 7);
      wg_publish(p, WS_F3 + (ti * 16 + jj) * 8, tag);
    }
    STAMP(13);
  }
  if (PB <= 4 && 4 < PE) {
    phase4_prefetch(p, lds, lane, w, tag, ALL);
    if (ALL && blockIdx.x == 0 && w == 7) {
      for (int q = 0; q < 4; ++q) wait_flags(p.ws, WS_F1 + (q * 64 + lane) * 8, true, tag);
      if (lane == 0) publish(p.ws, WS_NONCE, tag);
    }
    phase4(p, lds, lane, w, tag, IN3, PB <= 2 STAMP_PASS);
  }
}

extern "C" void kernel_launch(void* const* d_in, const int* in_sizes, int n_in, void* d_out, int out_size, void* d_ws, size_t ws_size, hipStream_t stream) {
  Params p;
  p.x = (const unsigned short*)d_in[0]; p.wqkv = (const unsigned short*)d_in[1]; p.wo = (const unsigned short*)d_in[2];
  p.w1 = (const unsigned short*)d_in[4]; p.b1 = (const unsigned short*)d_in[5]; p.w2 = (const unsigned short*)d_in[6]; p.b2 = (const unsigned short*)d_in[7];
  p.out = (unsigned short*)d_out; p.ws = (unsigned char*)d_ws;
#define LAUNCH(a, b) do { hipFuncSetAttribute((const void*)fused_block<a, b>, hipFuncAttributeMaxDynamicSharedMemorySize, LDS_BYTES); \
    hipLaunchKernelGGL((fused_block<a, b>), dim3(256), dim3(512), LDS_BYTES, stream, p); } while (0)
  LAUNCH(0, 5);
}
```

```cpp
#include <hip/hip_runtime.h>
#include <hip/hip_bf16.h>
#include <stdint.h>

#define GAS __attribute__((address_space(1)))
#define LAS __attribute__((address_space(3)))
#define DEVI __device__ __forceinline__
typedef unsigned v4u __attribute__((ext_vector_type(4)));
typedef unsigned v2u __attribute__((ext_vector_type(2)));
typedef float f32x4 __attribute__((ext_vector_type(4)));
typedef short bf16x8 __attribute__((ext_vector_type(8)));
typedef short s16x4 __attribute__((ext_vector_type(4)));
typedef __bf16 bf16x2 __attribute__((ext_vector_type(2)));
typedef unsigned long long u64;
typedef GAS u64 gu64;
#define RLX_AGENT __ATOMIC_RELAXED, __HIP_MEMORY_SCOPE_AGENT

#define NLAUNCH 1
#define OUT_SC1 0
#define WS_AT_END 0
#define TOUCH 0
#define P1_SAME 0
#define POLL_SLEEP 2
#define PADTO 45
#define DIAG 0

constexpr int T_ = 2048, D_ = 640, F_ = 1536;
constexpr unsigned WS_NONCE = 0;
constexpr unsigned WS_F0Q = 40960;
constexpr unsigned WS_F0O = 8192;
constexpr unsigned WS_F0A = 12288;
constexpr unsigned WS_F0B = 16384;
constexpr unsigned WS_F0X = 20480;
constexpr unsigned WS_F1 = 24576;
constexpr unsigned WS_F2 = 28672;
constexpr unsigned WS_F3 = 32768;
constexpr unsigned WS_XN = 131072;
constexpr unsigned WS_WQ = WS_XN + 128 * 20480;
constexpr unsigned WS_WO = WS_WQ + 16 * 20 * 8 * 1024;
constexpr unsigned WS_W1 = WS_WO + 8 * 20 * 5 * 1024;
constexpr unsigned WS_W2 = WS_W1 + 16 * 20 * 6 * 1024;
constexpr unsigned WS_O = WS_W2 + 8 * 48 * 5 * 1024;
constexpr unsigned WS_H = WS_O + 128 * 20480;
constexpr unsigned WS_H1 = WS_H + 128 * 20480;
constexpr unsigned WS_ST = WS_H1 + 128 * 49152;
constexpr unsigned WS_END = WS_ST + 128 * 2048;
constexpr unsigned LDS_BYTES = 163840;
constexpr unsigned L_STASH = 153600;
constexpr unsigned L_P0 = 104448;
constexpr unsigned L_TAG = 153600;
constexpr unsigned L_GO = 153616;
#define SHADOW_SLEEP 24
constexpr unsigned P1_STAGE = 34816;
constexpr unsigned L_P1_EX = 102400;
constexpr unsigned P1_RS = 264;
constexpr unsigned L_EXH = 124928;
constexpr unsigned L_P3_B1 = 122880;
constexpr unsigned L_P4_B2 = 145408;

struct Params {
  const unsigned short* x; const unsigned short* wqkv; const unsigned short* wo; const unsigned short* w1; const unsigned short* b1;
  const unsigned short* w2; const unsigned short* b2; unsigned short* out; unsigned char* ws;
};

typedef __amdgpu_buffer_rsrc_t rsrc_t;
DEVI rsrc_t mk_rsrc(const void* p, unsigned bytes) { return __builtin_amdgcn_make_buffer_rsrc((void*)p, 0, bytes, 0x00020000); }
DEVI v4u mk_srd(const void* p, unsigned bytes) {
  u64 a = (u64)p; v4u r; r.x = (unsigned)a; r.y = (unsigned)(a >> 32) & 0xffffu; r.z = bytes; r.w = 0x00020000u; return r;
}
template <int AUX> DEVI void dma16(rsrc_t rs, LAS char* lds, unsigned voff, unsigned soff) {
  __builtin_amdgcn_raw_ptr_buffer_load_lds(rs, (LAS void*)lds, 16, voff, soff, 0, AUX);
}
DEVI void aload(v4u& d, v4u srd, unsigned voff, unsigned soff) {
  asm volatile("buffer_load_dwordx4 %0, %1, %2, %3 offen sc1" : "=v"(d) : "v"(voff), "s"(srd), "s"(soff) : "memory");
}
DEVI void st16(rsrc_t rs, v4u v, unsigned voff, unsigned soff) { __builtin_amdgcn_raw_buffer_store_b128(v, rs, voff + soff, 0, 16); }
#define WAITV(n) asm volatile("s_waitcnt vmcnt(%0)" ::"i"(n) : "memory")
#define WAITV1(n, a) asm volatile("s_waitcnt vmcnt(%1)" : "+v"(a) : "i"(n) : "memory")
#define WAITV2(n, a, b) asm volatile("s_waitcnt vmcnt(%2)" : "+v"(a), "+v"(b) : "i"(n) : "memory")
#define WAITV4(n, a, b, c, d) asm volatile("s_waitcnt vmcnt(%4)" : "+v"(a), "+v"(b), "+v"(c), "+v"(d) : "i"(n) : "memory")
#define WAITL0() asm volatile("s_waitcnt lgkmcnt(0)" ::: "memory")
#define BAR() __builtin_amdgcn_s_barrier()
#define SCHED0() __builtin_amdgcn_sched_barrier(0)
DEVI void lbar() { WAITL0(); BAR(); asm volatile("" ::: "memory"); }
DEVI void rbar() { asm volatile("" ::: "memory"); BAR(); asm volatile("" ::: "memory"); }

DEVI unsigned pk2(float a, float b) { bf16x2 p; p[0] = (__bf16)a; p[1] = (__bf16)b; return __builtin_bit_cast(unsigned, p); }
DEVI float bflo(unsigned u) { return __uint_as_float(u << 16); }
DEVI float bfhi(unsigned u) { return __uint_as_float(u & 0xffff0000u); }
DEVI bf16x8 asbf(v4u v) { return __builtin_bit_cast(bf16x8, v); }
DEVI f32x4 mfma(v4u a, v4u b, f32x4 c) { return __builtin_amdgcn_mfma_f32_16x16x32_bf16(asbf(a), asbf(b), c, 0, 0, 0); }
DEVI v4u ldsr16(LAS char* p) { return *(LAS v4u*)p; }
DEVI float fsel(bool c, float a, float b) { asm("" : "+v"(a)); asm("" : "+v"(b)); return c ? a : b; }
DEVI float xrow_max(float v) {
  unsigned u = __float_as_uint(v);
  auto a = __builtin_amdgcn_permlane32_swap(u, u, false, false); v = fmaxf(__uint_as_float(a[0]), __uint_as_float(a[1]));
  u = __float_as_uint(v);
  auto b = __builtin_amdgcn_permlane16_swap(u, u, false, false); return fmaxf(__uint_as_float(b[0]), __uint_as_float(b[1]));
}
DEVI float xrow_sum(float v) {
  unsigned u = __float_as_uint(v);
  auto a = __builtin_amdgcn_permlane32_swap(u, u, false, false); v = __uint_as_float(a[0]) + __uint_as_float(a[1]);
  u = __float_as_uint(v);
  auto b = __builtin_amdgcn_permlane16_swap(u, u, false, false); return __uint_as_float(b[0]) + __uint_as_float(b[1]);
}
DEVI float row16_sum(float v) {
  v += __uint_as_float(__builtin_amdgcn_update_dpp(0u, __float_as_uint(v), 0x128, 0xf, 0xf, false));
  v += __uint_as_float(__builtin_amdgcn_update_dpp(0u, __float_as_uint(v), 0x124, 0xf, 0xf, false));
  v += __uint_as_float(__builtin_amdgcn_update_dpp(0u, __float_as_uint(v), 0x122, 0xf, 0xf, false));
  v += __uint_as_float(__builtin_amdgcn_update_dpp(0u, __float_as_uint(v), 0x121, 0xf, 0xf, false));
  return v;
}

DEVI u64 rfl64(u64 v) { return ((u64)__builtin_amdgcn_readfirstlane((unsigned)(v >> 32)) << 32) | (unsigned)__builtin_amdgcn_readfirstlane((unsigned)v); }
DEVI void publish(unsigned char* ws, unsigned off, u64 tag) { __hip_atomic_store((gu64*)(ws + off), tag, RLX_AGENT); }
DEVI void wait_flags(unsigned char* ws, unsigned laneoff, bool active, u64 tag) {
  for (unsigned sp = 0; sp < (1u << 18); ++sp) {
    u64 v = tag;
    if (active) v = __hip_atomic_load((gu64*)(ws + laneoff), RLX_AGENT);
    if (__all(v == tag)) break;
    __builtin_amdgcn_s_sleep(POLL_SLEEP);
  }
  __builtin_amdgcn_fence(__ATOMIC_ACQUIRE, "wavefront");
  asm volatile("" ::: "memory");
}

DEVI void wait_flags2(unsigned char* ws, unsigned off1, bool act1, unsigned off2, bool act2, u64 tag) {
  for (unsigned sp = 0; sp < (1u << 18); ++sp) {
    u64 v1 = tag, v2 = tag;
    if (act1) v1 = __hip_atomic_load((gu64*)(ws + off1), RLX_AGENT);
    if (act2) v2 = __hip_atomic_load((gu64*)(ws + off2), RLX_AGENT);
    if (__all(v1 == tag && v2 == tag)) break;
    __builtin_amdgcn_s_sleep(POLL_SLEEP);
  }
  __builtin_amdgcn_fence(__ATOMIC_ACQUIRE, "wavefront");
  asm volatile("" ::: "memory");
}

#define STAMP_ARG
#define STAMP_PASS
#define STAMP(id) do {} while (0)

template <int KIND>
DEVI unsigned retile_unit(const Params& p, rsrc_t ws, LAS char* st, int lane, int a, int ks STAMP_ARG) {
  constexpr int NCH = KIND == 2 ? 12 : 10;
  constexpr int NI = 32 * NCH / 64;
  constexpr int NT = KIND == 2 ? 6 : 5;
  constexpr unsigned RS = KIND == 2 ? 3072u : 1280u;
  const void* src = KIND == 1 ? (const void*)p.wo : KIND == 2 ? (const void*)p.w1 : (const void*)p.w2;
  constexpr unsigned SB = KIND == 1 ? 640u * 1280u : KIND == 2 ? 640u * 3072u : 1536u * 1280u;
  rsrc_t rs = mk_rsrc(src, SB);
#pragma unroll
  for (int n = 0; n < NI; ++n) {
    int q = n * 64 + lane;
    int rho = q / NCH, ch = q - rho * NCH;
    int row; unsigned col;
    const int g = rho >> 3, t = (rho >> 2) & 1, e = rho & 3;
    if (KIND == 1) { row = ks < 16 ? (2 * ks + t) * 20 + 4 * g + e : (2 * (4 * (ks - 16) + g) + t) * 20 + 16 + e; col = 160 * a + 16 * ch; }
    else if (KIND == 2) { row = 32 * ks + rho; col = 192 * a + 16 * ch; }
    else { row = 32 * ks + 16 * t + 4 * g + e; col = 160 * a + 16 * ch; }
    dma16<0>(rs, st + n * 1024, (unsigned)row * RS + col, 0);
  }
  WAITV(0);
  STAMP(18);
  unsigned dst;
  if (KIND == 1) dst = WS_WO + (unsigned)(a * 20 + ks) * 5120;
  else if (KIND == 2) dst = WS_W1 + (unsigned)(a * 20 + ks) * 6144;
  else { int kh = ks / 24, s = ks - kh * 24; dst = WS_W2 + (unsigned)a * 245760 + (s >> 2) * 40960 + kh * 20480 + (s & 3) * 5120; }
  const int g = lane >> 4, q4 = (lane & 15) >> 2, p4 = lane & 3;
  constexpr int RB = NCH * 16;
  LAS char* base = st + (8 * g + q4) * RB + 8 * p4;
#pragma unroll
  for (int t = 0; t < NT; ++t) {
    s16x4 lo = __builtin_amdgcn_ds_read_tr16_b64_v4i16((LAS s16x4*)(base + 32 * t));
    s16x4 hi = __builtin_amdgcn_ds_read_tr16_b64_v4i16((LAS s16x4*)(base + 4 * RB + 32 * t));
    v2u l2 = __builtin_bit_cast(v2u, lo), h2 = __builtin_bit_cast(v2u, hi);
    st16(ws, v4u{l2.x, l2.y, h2.x, h2.y}, lane * 16, dst + t * 1024);
  }
  STAMP(19);
  return KIND == 1 ? WS_F0O + (a * 32 + ks) * 8 : KIND == 2 ? WS_F0A + (a * 32 + ks) * 8 : WS_F0B + (a * 64 + ks) * 8;
}

DEVI void wqkv_unit(const Params& p, rsrc_t ws, LAS char* lds, int lane, int w, int u, u64 nonce, u64& tag, bool flags STAMP_ARG) {
  const int ks = u >> 2, hf = (u >> 1) & 1, rh = u & 1;
  rsrc_t rs = mk_rsrc(p.wqkv, 640u * 3840u);
#pragma unroll
  for (int m = 0; m < 4; ++m) {
    const int n = w + 8 * m;
    if (n < 30) {
      const int q = n * 64 + lane, row = q / 120, ch = q - row * 120, sec = ch / 40;
      dma16<0>(rs, lds + L_P0 + n * 1024, (unsigned)(32 * ks + 16 * rh + row) * 3840u + sec * 1280 + 640 * hf + 16 * (ch - sec * 40), 0);
    }
  }
  STAMP(17);
  WAITV(0);
  if (flags && w == 0) { asm volatile("" : "+v"(nonce)); if (lane == 0) *(LAS u64*)(lds + L_TAG) = nonce * 6364136223846793005ull + 1442695040888963407ull; }
  lbar();
  STAMP(18);
  if (flags) tag = rfl64(*(LAS u64*)(lds + L_TAG));
  const int gl = (lane >> 4) & 1, th = lane >> 5, q4 = (lane & 15) >> 2, p4 = lane & 3;
  LAS char* base = lds + L_P0 + (8 * gl + q4) * 1920 + 80 * w;
  const unsigned dst = WS_WQ + (unsigned)((8 * hf + w) * 20 + ks) * 8192;
  const unsigned vo = th * 1024 + (2 * rh + gl) * 256 + (lane & 15) * 16;
#pragma unroll
  for (int m = 0; m < 4; ++m) {
    int cb;
    if (m == 0) cb = 640 + 40 * th + 8 * p4;
    else if (m == 1) cb = th == 0 ? (p4 < 2 ? 640 : 0) + 32 + 40 * (p4 & 1) : 8 * p4;
    else if (m == 2) cb = th == 0 ? 40 + 8 * p4 : 1280 + 8 * p4;
    else cb = th == 0 ? 1280 + 40 + 8 * p4 : 1280 + 32 + 40 * (p4 & 1);
    s16x4 lo = __builtin_amdgcn_ds_read_tr16_b64_v4i16((LAS s16x4*)(base + cb));
    s16x4 hi = __builtin_amdgcn_ds_read_tr16_b64_v4i16((LAS s16x4*)(base + 4 * 1920 + cb));
    v2u l2 = __builtin_bit_cast(v2u, lo), h2 = __builtin_bit_cast(v2u, hi);
    v4u v = {l2.x, l2.y, h2.x, h2.y};
    if (m == 3 && th == 1 && (lane & 15) >= 8) v = v4u{0u, 0u, 0u, 0u};
    st16(ws, v, vo, dst + m * 2048);
  }
  STAMP(19);
  if (flags) { WAITV(0); STAMP(20); if (lane == 0) publish(p.ws, WS_F0Q + ((8 * hf + w) * 64 + ks * 2 + rh) * 8, tag); }
}

DEVI void ln_unit(const Params& p, rsrc_t ws, LAS char* lds, int lane, int w, int tb, u64 nonce, u64& tag, bool flags STAMP_ARG) {
  const int t = lane >> 4, i = lane & 15, c = 4 * w + t;
  const GAS v4u* src = (const GAS v4u*)((const GAS char*)p.x + (size_t)(tb * 16 + c) * 1280) + i;
  v4u r[5];
#pragma unroll
  for (int n = 0; n < 5; ++n) r[n] = src[16 * n];
  STAMP(17);
  float v[40];
#pragma unroll
  for (int n = 0; n < 5; ++n)
#pragma unroll
    for (int e = 0; e < 4; ++e) { v[n * 8 + 2 * e] = bflo(r[n][e]); v[n * 8 + 2 * e + 1] = bfhi(r[n][e]); }
  float s4[4] = {0.f, 0.f, 0.f, 0.f};
#pragma unroll
  for (int e = 0; e < 40; ++e) s4[e & 3] += v[e];
  const float mean = row16_sum((s4[0] + s4[1]) + (s4[2] + s4[3])) * (1.f / 640.f);
  float q4[4] = {0.f, 0.f, 0.f, 0.f};
#pragma unroll
  for (int e = 0; e < 40; ++e) { v[e] -= mean; q4[e & 3] = fmaf(v[e], v[e], q4[e & 3]); }
  const float rstd = __builtin_amdgcn_rsqf(row16_sum((q4[0] + q4[1]) + (q4[2] + q4[3])) * (1.f / 640.f) + 1e-5f);
  LAS char* st = lds + L_P0;
#pragma unroll
  for (int n = 0; n < 5; ++n) {
    v4u o;
#pragma unroll
    for (int e = 0; e < 4; ++e) o[e] = pk2(v[n * 8 + 2 * e] * rstd, v[n * 8 + 2 * e + 1] * rstd);
    *(LAS v4u*)(st + (i + 16 * n) * 272 + c * 16) = o;
  }
  if (flags && w == 0) { if (lane == 0) *(LAS u64*)(lds + L_TAG) = nonce * 6364136223846793005ull + 1442695040888963407ull; }
  STAMP(18);
  lbar();
  if (flags) tag = rfl64(*(LAS u64*)(lds + L_TAG));
#pragma unroll
  for (int n = 0; n < 5; ++n) {
    const int ks = w * 5 + n;
    v4u o = ldsr16(st + (4 * ks + (lane >> 4)) * 272 + (lane & 15) * 16);
    st16(ws, o, lane * 16, WS_XN + (unsigned)tb * 20480 + ks * 1024);
  }
  STAMP(19);
  if (flags) { WAITV(0); STAMP(20); if (lane == 0) publish(p.ws, WS_F0X + (tb * 4 + w) * 8, tag); }
}

DEVI unsigned phase0(const Params& p, LAS char* lds, int lane, int w, u64& tag, bool flags STAMP_ARG) {
  rsrc_t ws = mk_rsrc(p.ws, WS_END);
  const int cu = blockIdx.x;
  u64 nonce = 0;
  if (flags && w == 0) nonce = __hip_atomic_load((gu64*)(p.ws + WS_NONCE), RLX_AGENT);
  if (w >= 8) { if (w == 8 && lane == 0) *(LAS unsigned*)(lds + L_GO) = 0u; lbar(); tag = rfl64(*(LAS u64*)(lds + L_TAG)); return 0; }
  if (cu < 128 && w < 4) { ln_unit(p, ws, lds, lane, w, cu, nonce, tag, flags STAMP_PASS); return 0; }
  if (cu >= 128 && cu < 208) { wqkv_unit(p, ws, lds, lane, w, cu - 128, nonce, tag, flags STAMP_PASS); return 0; }
  int slot;
  if (cu < 128) slot = cu * 4 + (w - 4);
  else {
    slot = 512 + (cu - 208) * 8 + w;
    if (flags && w == 0 && lane == 0) *(LAS u64*)(lds + L_TAG) = nonce * 6364136223846793005ull + 1442695040888963407ull;
  }
  lbar();
  if (flags) tag = rfl64(*(LAS u64*)(lds + L_TAG));
  if (cu >= 208) __builtin_amdgcn_s_sleep(SHADOW_SLEEP);
  STAMP(17);
  LAS char* st = lds + L_P0 + (cu < 128 ? 21760 + (w - 4) * 6144 : w * 6144);
  if (slot < 160) return retile_unit<1>(p, ws, st, lane, slot & 7, slot >> 3 STAMP_PASS);
  if (slot < 480) { int u = slot - 160; return retile_unit<2>(p, ws, st, lane, u & 15, u >> 4 STAMP_PASS); }
  if (slot < 864) { int u = slot - 480; return retile_unit<3>(p, ws, st, lane, u & 7, u >> 3 STAMP_PASS); }
  return 0;
}

template <int NB>
DEVI void p1_loop(LAS char* lds, unsigned t0o, unsigned t1o, unsigned xo, f32x4 (&acc)[2][5]) {
  v4u W[2][2], X[2][NB];
#define P1_RO(ks) ((((ks) >> 1) & 3) * P1_STAGE + ((ks) & 1) * 17408)
#define P1_READ(ks) do { W[(ks) & 1][0] = ldsr16(lds + P1_RO(ks) + t0o); W[(ks) & 1][1] = ldsr16(lds + P1_RO(ks) + t1o); \
    _Pragma("unroll") for (int b = 0; b < NB; ++b) X[(ks) & 1][b] = ldsr16(lds + P1_RO(ks) + xo + b * 1024); } while (0)
  rbar();
  P1_READ(0);
  SCHED0();
#pragma unroll
  for (int ks = 0; ks < 20; ++ks) {
    if ((ks & 1) && ks < 19) lbar();
    if (ks + 1 < 20) P1_READ(ks + 1);
#pragma unroll
    for (int b = 0; b < NB; ++b) { acc[0][b] = mfma(W[ks & 1][0], X[ks & 1][b], acc[0][b]); acc[1][b] = mfma(W[ks & 1][1], X[ks & 1][b], acc[1][b]); }
    if (ks & 1) {
      __builtin_amdgcn_sched_group_barrier(0x100, 2, 0);
#pragma unroll
      for (int b = 0; b < NB; ++b) { __builtin_amdgcn_sched_group_barrier(0x8, 2, 0); __builtin_amdgcn_sched_group_barrier(0x100, 1, 0); }
    } else {
      __builtin_amdgcn_sched_group_barrier(0x100, 2, 0); __builtin_amdgcn_sched_group_barrier(0x8, 1, 0);
      __builtin_amdgcn_sched_group_barrier(0x100, 2, 0); __builtin_amdgcn_sched_group_barrier(0x8, 1, 0);
      __builtin_amdgcn_sched_group_barrier(0x100, 2, 0); __builtin_amdgcn_sched_group_barrier(0x8, 1, 0);
      __builtin_amdgcn_sched_group_barrier(0x100, NB - 4, 0); __builtin_amdgcn_sched_group_barrier(0x8, 2 * NB - 3, 0);
    }
    SCHED0();
  }
#undef P1_RO
#undef P1_READ
}

DEVI void phase1(const Params& p, LAS char* lds, int lane, int w, u64 tag, unsigned pending STAMP_ARG) {
  const int x8 = blockIdx.x & 7, r = blockIdx.x >> 3;
  const int ti = 4 * (x8 >> 1) + (r >> 3), pr = 8 * (x8 & 1) + (r & 7);
  rsrc_t ws = mk_rsrc(p.ws, WS_END);
  const unsigned l16 = lane * 16;
  if (pending) { WAITV(0); if (lane == 0) publish(p.ws, pending, tag); }
  STAMP(2);
  const int np = w & 3, rg = w >> 2;
  const int T0 = np == 0 ? 0 : np == 1 ? 2 : np == 2 ? 5 : 3, T1 = np == 0 ? 1 : np == 1 ? 7 : np == 2 ? 6 : 4;
  f32x4 acc[2][5];
#pragma unroll
  for (int e = 0; e < 2; ++e)
#pragma unroll
    for (int b = 0; b < 5; ++b) acc[e][b] = f32x4{0.f, 0.f, 0.f, 0.f};
  const unsigned t0o = (9 + T0) * 1024 + l16, t1o = (9 + T1) * 1024 + l16;
  if (rg == 0) p1_loop<5>(lds, t0o, t1o, l16, acc);
  else p1_loop<4>(lds, t0o, t1o, 5 * 1024 + l16, acc);
  STAMP(3);
  const int g = lane >> 4, c = lane & 15;
  {
    LAS char* d = lds + L_P1_EX + (rg * 80 + c) * P1_RS + g * 8;
#pragma unroll
    for (int b = 0; b < 5; ++b) {
      if (b < 4 || rg == 0) {
        *(LAS v2u*)(d + b * 16 * P1_RS + T0 * 32) = v2u{pk2(acc[0][b][0], acc[0][b][1]), pk2(acc[0][b][2], acc[0][b][3])};
        *(LAS v2u*)(d + b * 16 * P1_RS + T1 * 32) = v2u{pk2(acc[1][b][0], acc[1][b][1]), pk2(acc[1][b][2], acc[1][b][3])};
      }
    }
  }
  STAMP(22);
  lbar();
  STAMP(23);
  const int blk = 8 * ti + w;
  LAS char* own = lds + L_P1_EX + ((w + 1) * 16 + c) * P1_RS + g * 8;
  LAS char* trp = lds + L_P1_EX + ((w + 1) * 16 + 4 * g + (c >> 2)) * P1_RS + (c & 3) * 8;
  v2u ka[2], kp[2], qa[2], qr[2], va[2], vp[2];
  const v2u kr = *(LAS v2u*)(own + 64), krp = *(LAS v2u*)(own + 64 - 16 * P1_RS);
#pragma unroll
  for (int hh = 0; hh < 2; ++hh) {
    ka[hh] = *(LAS v2u*)(own + hh * 32); kp[hh] = *(LAS v2u*)(own + hh * 32 - 16 * P1_RS);
    qa[hh] = *(LAS v2u*)(own + (3 + hh) * 32);
    qr[hh] = *(LAS v2u*)(own - g * 8 + 64 + 16 + hh * 8);
    va[hh] = __builtin_bit_cast(v2u, __builtin_amdgcn_ds_read_tr16_b64_v4i16((LAS s16x4*)(trp + (5 + hh) * 32)));
    vp[hh] = __builtin_bit_cast(v2u, __builtin_amdgcn_ds_read_tr16_b64_v4i16((LAS s16x4*)(trp + (5 + hh) * 32 - 16 * P1_RS)));
  }
  const v2u vr = __builtin_bit_cast(v2u, __builtin_amdgcn_ds_read_tr16_b64_v4i16((LAS s16x4*)(trp + 7 * 32)));
  const v2u vrp = __builtin_bit_cast(v2u, __builtin_amdgcn_ds_read_tr16_b64_v4i16((LAS s16x4*)(trp + 7 * 32 - 16 * P1_RS)));
  STAMP(24);
  SCHED0();
  const int odd = c & 1, ce = c - odd;
  const bool havePrev = (ti > 0) || (w > 0);
  const bool v1a = havePrev && (4 * g >= ce + 2), v1b = havePrev && (4 * g >= ce), v2a = ce >= 4 * g, v2b = ce - 2 >= 4 * g;
  const float c2 = 0.22360679774997896f * 1.4426950408889634f, NEG = -3.0e38f;
  const f32x4 z = {0.f, 0.f, 0.f, 0.f};
  f32x4 S1[2], S2[2];
#pragma unroll
  for (int hh = 0; hh < 2; ++hh) {
    const bool mine = g == hh;
    const v4u Q = {qa[hh].x, qa[hh].y, mine ? qr[hh].x : 0u, mine ? qr[hh].y : 0u};
    S1[hh] = mfma(v4u{kp[hh].x, kp[hh].y, krp.x, krp.y}, Q, z);
    S2[hh] = mfma(v4u{ka[hh].x, ka[hh].y, kr.x, kr.y}, Q, z);
  }
  float s1a[2], s1b[2], s2a[2], s2b[2], m[2], inv[2];
#pragma unroll
  for (int hh = 0; hh < 2; ++hh) {
    s1a[hh] = fsel(v1a, fsel(odd, S1[hh][1], S1[hh][0]), NEG); s1b[hh] = fsel(v1b, fsel(odd, S1[hh][3], S1[hh][2]), NEG);
    s2a[hh] = fsel(v2a, fsel(odd, S2[hh][1], S2[hh][0]), NEG); s2b[hh] = fsel(v2b, fsel(odd, S2[hh][3], S2[hh][2]), NEG);
    m[hh] = fmaxf(fmaxf(s1a[hh], s1b[hh]), fmaxf(s2a[hh], s2b[hh]));
  }
  {
    auto a0 = __builtin_amdgcn_permlane32_swap(__float_as_uint(m[0]), __float_as_uint(m[0]), false, false);
    auto a1 = __builtin_amdgcn_permlane32_swap(__float_as_uint(m[1]), __float_as_uint(m[1]), false, false);
    m[0] = fmaxf(__uint_as_float(a0[0]), __uint_as_float(a0[1])); m[1] = fmaxf(__uint_as_float(a1[0]), __uint_as_float(a1[1]));
    auto b0 = __builtin_amdgcn_permlane16_swap(__float_as_uint(m[0]), __float_as_uint(m[0]), false, false);
    auto b1 = __builtin_amdgcn_permlane16_swap(__float_as_uint(m[1]), __float_as_uint(m[1]), false, false);
    m[0] = fmaxf(__uint_as_float(b0[0]), __uint_as_float(b0[1])); m[1] = fmaxf(__uint_as_float(b1[0]), __uint_as_float(b1[1]));
  }
#pragma unroll
  for (int hh = 0; hh < 2; ++hh) {
    s1a[hh] = __builtin_amdgcn_exp2f((s1a[hh] - m[hh]) * c2); s1b[hh] = __builtin_amdgcn_exp2f((s1b[hh] - m[hh]) * c2);
    s2a[hh] = __builtin_amdgcn_exp2f((s2a[hh] - m[hh]) * c2); s2b[hh] = __builtin_amdgcn_exp2f((s2b[hh] - m[hh]) * c2);
    inv[hh] = (s1a[hh] + s1b[hh]) + (s2a[hh] + s2b[hh]);
  }
  {
    auto a0 = __builtin_amdgcn_permlane32_swap(__float_as_uint(inv[0]), __float_as_uint(inv[0]), false, false);
    auto a1 = __builtin_amdgcn_permlane32_swap(__float_as_uint(inv[1]), __float_as_uint(inv[1]), false, false);
    inv[0] = __uint_as_float(a0[0]) + __uint_as_float(a0[1]); inv[1] = __uint_as_float(a1[0]) + __uint_as_float(a1[1]);
    auto b0 = __builtin_amdgcn_permlane16_swap(__float_as_uint(inv[0]), __float_as_uint(inv[0]), false, false);
    auto b1 = __builtin_amdgcn_permlane16_swap(__float_as_uint(inv[1]), __float_as_uint(inv[1]), false, false);
    inv[0] = __builtin_amdgcn_rcpf(__uint_as_float(b0[0]) + __uint_as_float(b0[1])); inv[1] = __builtin_amdgcn_rcpf(__uint_as_float(b1[0]) + __uint_as_float(b1[1]));
  }
  STAMP(25);
  f32x4 OA[2], OR[2];
  const unsigned sh = 16 * odd;
#pragma unroll
  for (int hh = 0; hh < 2; ++hh) {
    const v4u P = {pk2(s1a[hh] * inv[hh], 0.f) << sh, pk2(s1b[hh] * inv[hh], 0.f) << sh, pk2(s2a[hh] * inv[hh], 0.f) << sh, pk2(s2b[hh] * inv[hh], 0.f) << sh};
    OA[hh] = mfma(v4u{vp[hh].x, vp[hh].y, va[hh].x, va[hh].y}, P, z);
    OR[hh] = mfma(v4u{vrp.x, vrp.y, vr.x, vr.y}, P, z);
  }
  {
    v4u om = {pk2(OA[0][0], OA[0][1]), pk2(OA[0][2], OA[0][3]), pk2(OA[1][0], OA[1][1]), pk2(OA[1][2], OA[1][3])};
    st16(ws, om, l16, WS_O + (unsigned)blk * 20480 + pr * 1024);
    unsigned r0x = pk2(OR[0][0], OR[0][1]), r0y = pk2(OR[0][2], OR[0][3]), r1x = pk2(OR[1][0], OR[1][1]), r1y = pk2(OR[1][2], OR[1][3]);
    auto t0 = __builtin_amdgcn_permlane16_swap(r1x, r1x, false, false); auto t1 = __builtin_amdgcn_permlane16_swap(r1y, r1y, false, false);
    v4u orr = {r0x, r0y, t0[1], t1[1]};
    if (lane < 16) st16(ws, orr, l16, WS_O + (unsigned)blk * 20480 + (16 + (pr >> 2)) * 1024 + (pr & 3) * 256);
  }
  STAMP(4);
}

DEVI void exchange_halves(LAS char* lds, const f32x4 (&acc)[5], f32x4 (&fin)[3], int lane, int w) {
  LAS char* d = lds + L_EXH + (w & 3) * 5120 + lane * 16;
  if (w < 4) { *(LAS f32x4*)(d + 3072) = acc[3]; *(LAS f32x4*)(d + 4096) = acc[4]; }
  else { *(LAS f32x4*)(d) = acc[0]; *(LAS f32x4*)(d + 1024) = acc[1]; *(LAS f32x4*)(d + 2048) = acc[2]; }
  lbar();
  if (w < 4) { f32x4 u0 = *(LAS f32x4*)(d), u1 = *(LAS f32x4*)(d + 1024), u2 = *(LAS f32x4*)(d + 2048); fin[0] = acc[0] + u0; fin[1] = acc[1] + u1; fin[2] = acc[2] + u2; }
  else { f32x4 u0 = *(LAS f32x4*)(d + 3072), u1 = *(LAS f32x4*)(d + 4096); fin[0] = acc[3] + u0; fin[1] = acc[4] + u1; fin[2] = f32x4{0.f, 0.f, 0.f, 0.f}; }
}

DEVI void phase2(const Params& p, LAS char* lds, int lane, int w STAMP_ARG) {
  const int x8 = blockIdx.x & 7, r = blockIdx.x >> 3;
  const int ti = 2 * x8 + (r >> 4), hb = (r >> 3) & 1, j = r & 7;
  rsrc_t ws = mk_rsrc(p.ws, WS_END);
  const v4u srd = mk_srd(p.ws, WS_END);
  const unsigned l16 = lane * 16;
  const int b = w & 3, kh = w >> 2, blk = 8 * ti + 4 * hb + b;
  rbar();
  STAMP(6);
  v4u a[10];
  const unsigned oa = WS_O + (unsigned)blk * 20480 + 10 * kh * 1024;
#pragma unroll
  for (int s = 0; s < 10; ++s) aload(a[s], srd, l16, oa + s * 1024);
  f32x4 acc[5];
#pragma unroll
  for (int t = 0; t < 5; ++t) acc[t] = f32x4{0.f, 0.f, 0.f, 0.f};
  v4u B[2][5];
  LAS char* bb = lds + kh * 51200 + l16;
#pragma unroll
  for (int t = 0; t < 5; ++t) B[0][t] = ldsr16(bb + t * 1024);
  SCHED0();
#pragma unroll
  for (int s = 0; s < 10; ++s) {
    asm volatile("s_waitcnt vmcnt(%1)" : "+v"(a[s]) : "i"(9 - s) : "memory");
    if (s + 1 < 10) {
#pragma unroll
      for (int t = 0; t < 5; ++t) B[(s + 1) & 1][t] = ldsr16(bb + (s + 1) * 5120 + t * 1024);
    }
#pragma unroll
    for (int t = 0; t < 5; ++t) acc[t] = mfma(B[s & 1][t], a[s], acc[t]);
#pragma unroll
    for (int t = 0; t < 5; ++t) { __builtin_amdgcn_sched_group_barrier(0x100, 1, 0); __builtin_amdgcn_sched_group_barrier(0x8, 1, 0); }
    SCHED0();
  }
  STAMP(7);
  const int g = lane >> 4, c = lane & 15, row = 16 * b + c, mt0 = 3 * kh;
  LAS char* sx = lds + L_STASH + row * 160 + mt0 * 32 + g * 8;
  v2u xv[3];
#pragma unroll
  for (int e = 0; e < 3; ++e) xv[e] = *(LAS v2u*)(sx + (e < 2 || kh == 0 ? e : 0) * 32);
  f32x4 fin[3];
  exchange_halves(lds, acc, fin, lane, w);
  v2u hv[3];
  float ssum = 0.f, ssq = 0.f;
#pragma unroll
  for (int e = 0; e < 3; ++e) {
    hv[e] = v2u{pk2(fin[e][0] + bflo(xv[e].x), fin[e][1] + bfhi(xv[e].x)), pk2(fin[e][2] + bflo(xv[e].y), fin[e][3] + bfhi(xv[e].y))};
    if (e < 2 || kh == 0) {
      *(LAS v2u*)(sx + e * 32) = hv[e];
      const float h0 = bflo(hv[e].x), h1 = bfhi(hv[e].x), h2 = bflo(hv[e].y), h3 = bfhi(hv[e].y);
      ssum += (h0 + h1) + (h2 + h3); ssq += fmaf(h0, h0, h1 * h1) + fmaf(h2, h2, h3 * h3);
    }
  }
  {
    auto sx0 = __builtin_amdgcn_permlane16_swap(hv[0].x, hv[1].x, false, false); auto sy0 = __builtin_amdgcn_permlane16_swap(hv[0].y, hv[1].y, false, false);
    const int G = 2 * (mt0 + (g & 1)) + (g >> 1);
    st16(ws, v4u{sx0[0], sy0[0], sx0[1], sy0[1]}, c * 16 + (10 * j + G) * 256, WS_H + (unsigned)blk * 20480);
    if (kh == 0) __builtin_amdgcn_raw_buffer_store_b64(hv[2], ws, WS_H + (unsigned)blk * 20480 + (10 * j + 4 + (g >> 1)) * 256 + c * 16 + (g & 1) * 8, 0, 16);
  }
  ssum = xrow_sum(ssum); ssq = xrow_sum(ssq);
  {
    const int idx = 2 * j + kh;
    const int e = idx & 3;
    if (lane < 16) {
      v2u sv = {__float_as_uint(ssum), __float_as_uint(ssq)};
      __builtin_amdgcn_raw_buffer_store_b64(sv, ws, WS_ST + (unsigned)blk * 2048 + (e >> 1) * 1024 + (idx >> 2) * 256 + c * 16 + (e & 1) * 8, 0, 16);
    }
  }
  STAMP(8);
}

#define P3D 8
DEVI void phase3(const Params& p, LAS char* lds, int lane, int w STAMP_ARG) {
  const int x8 = blockIdx.x & 7, r = blockIdx.x >> 3;
  const int ti = 4 * (x8 >> 1) + (r >> 3), jj = 8 * (x8 & 1) + (r & 7);
  rsrc_t ws = mk_rsrc(p.ws, WS_END);
  const v4u srd = mk_srd(p.ws, WS_END);
  const unsigned l16 = lane * 16;
  const int blk = 8 * ti + w;
  rbar();
  if (w < 6) {
    v4u cf[20];
#pragma unroll
    for (int ks = 0; ks < 20; ++ks) cf[ks] = ldsr16(lds + (ks * 6 + w) * 1024 + l16);
    SCHED0();
    f32x4 cs0 = {0.f, 0.f, 0.f, 0.f}, cs1 = cs0;
    const v4u ones = {0x3f803f80u, 0x3f803f80u, 0x3f803f80u, 0x3f803f80u};
#pragma unroll
    for (int ks = 0; ks < 20; ks += 2) { cs0 = mfma(cf[ks], ones, cs0); cs1 = mfma(cf[ks + 1], ones, cs1); }
    if ((lane & 15) == 0) *(LAS f32x4*)(lds + L_P3_B1 + 1024 + (w * 16 + (lane >> 4) * 4) * 4) = cs0 + cs1;
  }
  lbar();
  STAMP(10);
  v4u a[20], st0, st1;
  const unsigned ha = WS_H + (unsigned)blk * 20480;
#pragma unroll
  for (int s = 0; s < P3D; ++s) aload(a[s], srd, l16, ha + s * 1024);
  f32x4 acc[6];
#pragma unroll
  for (int t = 0; t < 6; ++t) acc[t] = f32x4{0.f, 0.f, 0.f, 0.f};
  v4u B[2][6];
  LAS char* bb = lds + l16;
#pragma unroll
  for (int t = 0; t < 6; ++t) B[0][t] = ldsr16(bb + t * 1024);
  SCHED0();
#pragma unroll
  for (int s = 0; s < 20; ++s) {
    asm volatile("s_waitcnt vmcnt(%1)" : "+v"(a[s]) : "i"(s + P3D - 1 < 22 ? P3D - 1 : 21 - s) : "memory");
    if (s + P3D < 20) aload(a[s + P3D], srd, l16, ha + (s + P3D) * 1024);
    else if (s + P3D == 20) aload(st0, srd, l16, WS_ST + (unsigned)blk * 2048);
    else if (s + P3D == 21) aload(st1, srd, l16, WS_ST + (unsigned)blk * 2048 + 1024);
    if (s + 1 < 20) {
#pragma unroll
      for (int t = 0; t < 6; ++t) B[(s + 1) & 1][t] = ldsr16(bb + (s + 1) * 6144 + t * 1024);
    }
#pragma unroll
    for (int t = 0; t < 6; ++t) acc[t] = mfma(B[s & 1][t], a[s], acc[t]);
#pragma unroll
    for (int t = 0; t < 6; ++t) { __builtin_amdgcn_sched_group_barrier(0x100, 1, 0); __builtin_amdgcn_sched_group_barrier(0x8, 1, 0); }
    SCHED0();
  }
  lbar();
  STAMP(11);
  const int g = lane >> 4;
  f32x4 cs[6]; v2u bb1[6];
#pragma unroll
  for (int t = 0; t < 6; ++t) { cs[t] = *(LAS f32x4*)(lds + L_P3_B1 + 1024 + (t * 16 + g * 4) * 4); bb1[t] = *(LAS v2u*)(lds + L_P3_B1 + (t * 16 + g * 4) * 2); }
  WAITV2(0, st0, st1);
  SCHED0();
  float s1 = (__uint_as_float(st0[0]) + __uint_as_float(st0[2])) + (__uint_as_float(st1[0]) + __uint_as_float(st1[2]));
  float s2 = (__uint_as_float(st0[1]) + __uint_as_float(st0[3])) + (__uint_as_float(st1[1]) + __uint_as_float(st1[3]));
  s1 = xrow_sum(s1); s2 = xrow_sum(s2);
  const float mean = s1 * (1.f / 640.f);
  const float var = fmaxf(s2 * (1.f / 640.f) - mean * mean, 0.f);
  const float rstd = __builtin_amdgcn_rsqf(var + 1e-5f), mr = -mean * rstd;
  unsigned o[12];
#pragma unroll
  for (int t = 0; t < 6; ++t) {
    float y0 = fmaxf(fmaf(rstd, acc[t][0], fmaf(mr, cs[t][0], bflo(bb1[t].x))), 0.f), y1 = fmaxf(fmaf(rstd, acc[t][1], fmaf(mr, cs[t][1], bfhi(bb1[t].x))), 0.f);
    float y2 = fmaxf(fmaf(rstd, acc[t][2], fmaf(mr, cs[t][2], bflo(bb1[t].y))), 0.f), y3 = fmaxf(fmaf(rstd, acc[t][3], fmaf(mr, cs[t][3], bfhi(bb1[t].y))), 0.f);
    o[2 * t] = pk2(y0 * y0, y1 * y1); o[2 * t + 1] = pk2(y2 * y2, y3 * y3);
  }
#pragma unroll
  for (int q = 0; q < 3; ++q) st16(ws, v4u{o[4 * q], o[4 * q + 1], o[4 * q + 2], o[4 * q + 3]}, l16, WS_H1 + (unsigned)blk * 49152 + (3 * jj + q) * 1024);
  STAMP(12);
}

#define P4D 8
DEVI void phase4(const Params& p, LAS char* lds, int lane, int w STAMP_ARG) {
  const int x8 = blockIdx.x & 7, r = blockIdx.x >> 3;
  const int ti = 2 * x8 + (r >> 4), hb = (r >> 3) & 1, j = r & 7;
  const v4u srd = mk_srd(p.ws, WS_END);
  const unsigned l16 = lane * 16;
  const int b = w & 3, kh = w >> 2, blk = 8 * ti + 4 * hb + b;
  rbar();
  STAMP(14);
  const unsigned ha = WS_H1 + (unsigned)blk * 49152 + kh * 24 * 1024;
  v4u a[24];
#pragma unroll
  for (int s = 0; s < P4D; ++s) aload(a[s], srd, l16, ha + s * 1024);
  f32x4 acc[5];
#pragma unroll
  for (int t = 0; t < 5; ++t) acc[t] = f32x4{0.f, 0.f, 0.f, 0.f};
  v4u B[2][5];
  LAS char* bb = lds + kh * 20480 + l16;
#define P4_READ(s) do { _Pragma("unroll") for (int t = 0; t < 5; ++t) B[(s) & 1][t] = ldsr16(bb + (((s) >> 2) % 3) * 40960 + ((s) & 3) * 5120 + t * 1024); } while (0)
  P4_READ(0);
  SCHED0();
#pragma unroll
  for (int s = 0; s < 24; ++s) {
    if ((s & 3) == 3 && s < 23) lbar();
    asm volatile("s_waitcnt vmcnt(%1)" : "+v"(a[s]) : "i"(s + P4D - 1 < 24 ? P4D - 1 : 23 - s) : "memory");
    if (s + P4D < 24) aload(a[s + P4D], srd, l16, ha + (s + P4D) * 1024);
    if (s + 1 < 24) P4_READ(s + 1);
#pragma unroll
    for (int t = 0; t < 5; ++t) acc[t] = mfma(B[s & 1][t], a[s], acc[t]);
#pragma unroll
    for (int t = 0; t < 5; ++t) { __builtin_amdgcn_sched_group_barrier(0x100, 1, 0); __builtin_amdgcn_sched_group_barrier(0x8, 1, 0); }
    SCHED0();
  }
#undef P4_READ
  STAMP(15);
  const int g = lane >> 4, c = lane & 15, row = 16 * b + c, mt0 = 3 * kh;
  LAS char* sx = lds + L_STASH + row * 160 + mt0 * 32 + g * 8;
  v2u hv[3], bv[3];
#pragma unroll
  for (int e = 0; e < 3; ++e) {
    const int ee = (e < 2 || kh == 0) ? e : 0;
    hv[e] = *(LAS v2u*)(sx + ee * 32);
    bv[e] = *(LAS v2u*)(lds + L_P4_B2 + (mt0 + ee) * 32 + g * 8);
  }
  f32x4 fin[3];
  exchange_halves(lds, acc, fin, lane, w);
#pragma unroll
  for (int e = 0; e < 3; ++e) {
    if (e < 2 || kh == 0)
      *(LAS v2u*)(sx + e * 32) = v2u{pk2(fin[e][0] + bflo(bv[e].x) + bflo(hv[e].x), fin[e][1] + bfhi(bv[e].x) + bfhi(hv[e].x)),
                                     pk2(fin[e][2] + bflo(bv[e].y) + bflo(hv[e].y), fin[e][3] + bfhi(bv[e].y) + bfhi(hv[e].y))};
  }
  lbar();
  {
    const int q0 = w * 64 + lane, q1 = q0 + 512;
    const v4u o0 = *(LAS v4u*)(lds + L_STASH + q0 * 16);
    v4u o1 = o0; if (w < 2) o1 = *(LAS v4u*)(lds + L_STASH + q1 * 16);
    const int r0 = q0 / 10, r1 = q1 / 10;
    rsrc_t outr = mk_rsrc(p.out, T_ * 1280);
    const unsigned ob = (unsigned)(128 * ti + 64 * hb) * 1280 + 160 * j;
    __builtin_amdgcn_raw_buffer_store_b128(o0, outr, ob + r0 * 1280 + (q0 - r0 * 10) * 16, 0, 0);
    if (w < 2) __builtin_amdgcn_raw_buffer_store_b128(o1, outr, ob + r1 * 1280 + (q1 - r1 * 10) * 16, 0, 0);
  }
  STAMP(16);
}

DEVI void loader(const Params& p, LAS char* lds, int lane, int lw, u64 tag STAMP_ARG) {
  const int x8 = blockIdx.x & 7, r = blockIdx.x >> 3;
  const int t2d = 4 * (x8 >> 1) + (r >> 3), c2d = 8 * (x8 & 1) + (r & 7);
  const int tl = 2 * x8 + (r >> 4), hb = (r >> 3) & 1, j = r & 7;
  rsrc_t ws = mk_rsrc(p.ws, WS_END);
  const v4u srd = mk_srd(p.ws, WS_END);
  const unsigned l16 = lane * 16;
  {
    if (lw == 0) {
      int bq = (t2d > 0 ? 8 * t2d - 1 : 0) + (lane >> 2); if (t2d == 0 && lane >= 32) bq = 7;
      wait_flags2(p.ws, WS_F0X + (bq * 4 + (lane & 3)) * 8, lane < 36, WS_F0Q + (c2d * 64 + lane) * 8, lane < 40, tag);
      if (lane == 0) __hip_atomic_store((LAS unsigned*)(lds + L_GO), 1u, __ATOMIC_RELAXED, __HIP_MEMORY_SCOPE_WORKGROUP);
    } else {
      for (unsigned sp = 0; sp < (1u << 22); ++sp) {
        if (__builtin_amdgcn_readfirstlane(__hip_atomic_load((LAS unsigned*)(lds + L_GO), __ATOMIC_RELAXED, __HIP_MEMORY_SCOPE_WORKGROUP))) break;
        __builtin_amdgcn_s_sleep(1);
      }
      asm volatile("" ::: "memory");
    }
  }
  STAMP(2);
  unsigned sb[9], si[9];
#pragma unroll
  for (int i = 0; i < 9; ++i) {
    const int n = (lw + 4 * i) < 34 ? (lw + 4 * i) : 33, e = n >= 17 ? 1 : 0, f = n - 17 * e;
    int blk = 8 * t2d - 1 + f; if (blk < 0) blk = 0;
    sb[i] = f < 9 ? WS_XN + (unsigned)blk * 20480 + e * 1024 : WS_WQ + (unsigned)c2d * 163840 + (e * 8 + (f - 9)) * 1024;
    si[i] = f < 9 ? 2048u : 16384u;
  }
#define P1_STG(s) do { if (lw < 2) dma16<16>(ws, lds + ((s) & 3) * P1_STAGE + (lw + 32) * 1024, l16, sb[8] + (s) * si[8]); \
    _Pragma("unroll") for (int i = 0; i < 8; ++i) dma16<16>(ws, lds + ((s) & 3) * P1_STAGE + (lw + 4 * i) * 1024, l16, sb[i] + (s) * si[i]); } while (0)
  P1_STG(0); P1_STG(1);
  WAITV(8); rbar();
  P1_STG(2); P1_STG(3);
  v2u f1, f2;
  const unsigned fo1 = lane < 20 ? WS_F0O + (j * 32 + lane) * 8 : WS_F0A + (c2d * 32 + (lane < 40 ? lane - 20 : 0)) * 8;
  const unsigned fo2 = WS_F0B + (j * 64 + (lane < 48 ? lane : 0)) * 8;
  const unsigned wo = WS_WO + (unsigned)j * 102400, w1 = WS_W1 + (unsigned)c2d * 122880, w2 = WS_W2 + (unsigned)j * 245760;
#pragma unroll
  for (int q = 1; q <= 9; ++q) {
    if (q <= 8) WAITV(16); else WAITV(8);
    if (q == 7) asm volatile("" : "+v"(f1), "+v"(f2));
    rbar();
    if (q + 3 <= 9) P1_STG(q + 3);
    if (q == 4) {
      asm volatile("buffer_load_dwordx2 %0, %1, %2, 0 offen sc1" : "=v"(f1) : "v"(fo1), "s"(srd) : "memory");
      asm volatile("buffer_load_dwordx2 %0, %1, %2, 0 offen sc1" : "=v"(f2) : "v"(fo2), "s"(srd) : "memory");
    }
    if (q == 7) {
      const bool ok = (((u64)f1.y << 32) | f1.x) == tag && (((u64)f2.y << 32) | f2.x) == tag;
      if (!__all(ok)) wait_flags2(p.ws, fo1, true, fo2, true, tag);
#pragma unroll
      for (int i = 0; i < 8; ++i) { const int f = 68 + lw + 4 * i; dma16<16>(ws, lds + f * 1024, l16, wo + f * 1024); }
    }
  }
#undef P1_STG
  STAMP(3);
  rbar();
#pragma unroll
  for (int i = 0; i < 17; ++i) { const int f = lw + 4 * i; dma16<16>(ws, lds + f * 1024, l16, wo + f * 1024); }
  {
    rsrc_t xs = mk_rsrc(p.x, T_ * 1280);
#pragma unroll
    for (int m = 0; m < 3; ++m) {
      const int n = lw + 4 * m;
      if (n < 10) { const int q = n * 64 + lane, row = q / 10, ch = q - row * 10;
        dma16<0>(xs, lds + L_STASH + n * 1024, (unsigned)(128 * tl + 64 * hb + row) * 1280 + 160 * j + 16 * ch, 0); }
    }
  }
  STAMP(4);
  rbar();
  STAMP(5);
  if (lw == 0) wait_flags(p.ws, WS_F1 + (tl * 16 + lane) * 8, lane < 16, tag);
  WAITV(0); rbar();
  STAMP(6);
  if (lw == 0) { rsrc_t bs = mk_rsrc(p.b1, F_ * 2); dma16<0>(bs, lds + L_P3_B1, lane < 12 ? 192 * c2d + lane * 16 : 0x7fffffffu, 0); }
#pragma unroll
  for (int i = 0; i < 5; ++i) { const int f = 100 + lw + 4 * i; dma16<16>(ws, lds + f * 1024, l16, w1 + f * 1024); }
  rbar();
#pragma unroll
  for (int i = 0; i < 25; ++i) { const int f = lw + 4 * i; dma16<16>(ws, lds + f * 1024, l16, w1 + f * 1024); }
  STAMP(8);
  rbar();
  STAMP(9);
  WAITV(0); rbar();
  if (lw == 0) wait_flags(p.ws, WS_F2 + (t2d * 16 + lane) * 8, lane < 16, tag);
  rbar();
  STAMP(10);
  rbar();
  STAMP(11);
  if (lw == 0) { rsrc_t bs = mk_rsrc(p.b2, D_ * 2); dma16<0>(bs, lds + L_P4_B2, lane < 10 ? 160 * j + lane * 16 : 0x7fffffffu, 0); }
#pragma unroll
  for (int i = 0; i < 30; ++i) { const int f = lw + 4 * i; dma16<16>(ws, lds + f * 1024, l16, w2 + f * 1024); }
  STAMP(12);
  rbar();
  STAMP(13);
  if (lw == 0) wait_flags(p.ws, WS_F3 + (tl * 16 + lane) * 8, lane < 16, tag);
  WAITV(0); rbar();
  STAMP(14);
#define P4_STG(st) do { _Pragma("unroll") for (int i = 0; i < 10; ++i) { const int f = lw + 4 * i; \
    dma16<16>(ws, lds + ((st) % 3) * 40960 + f * 1024, l16, w2 + (st) * 40960 + f * 1024); } } while (0)
  rbar(); P4_STG(3);
  rbar(); P4_STG(4);
  WAITV(10); rbar(); P4_STG(5);
  if (blockIdx.x == 0 && lw == 1) {
    for (int q = 0; q < 4; ++q) wait_flags(p.ws, WS_F1 + (q * 64 + lane) * 8, true, tag);
    if (lane == 0) publish(p.ws, WS_NONCE, tag);
  }
  WAITV(10); rbar();
  WAITV(0); rbar();
#undef P4_STG
  STAMP(15);
  rbar();
  rbar();
  STAMP(16);
}

DEVI void wg_publish(const Params& p, unsigned off, u64 tag) {
  WAITV(0); lbar();
  if (threadIdx.x == 448) publish(p.ws, off, tag);
}

__global__ __launch_bounds__(768, 3) __attribute__((amdgpu_waves_per_eu(3, 3))) void fused_block(Params p) {
  extern __shared__ __attribute__((aligned(16))) char smem[];
  LAS char* lds = (LAS char*)smem;
  const int lane = threadIdx.x & 63, w = __builtin_amdgcn_readfirstlane(threadIdx.x >> 6);
  const int x8 = blockIdx.x & 7, r = blockIdx.x >> 3;
  u64 tag = 0;
  STAMP(0);
  const u64 tstart = __builtin_amdgcn_s_memrealtime();
  const unsigned pending = phase0(p, lds, lane, w, tag, true STAMP_PASS);
  STAMP(1);
  if (w >= 8) loader(p, lds, lane, w - 8, tag STAMP_PASS);
  else {
    const int t2d = 4 * (x8 >> 1) + (r >> 3), c2d = 8 * (x8 & 1) + (r & 7), tl = 2 * x8 + (r >> 4);
    phase1(p, lds, lane, w, tag, pending STAMP_PASS);
    wg_publish(p, WS_F1 + (t2d * 16 + c2d) * 8, tag);
    STAMP(5);
    phase2(p, lds, lane, w STAMP_PASS);
    wg_publish(p, WS_F2 + (tl * 16 + (r & 15)) * 8, tag);
    STAMP(9);
    phase3(p, lds, lane, w STAMP_PASS);
    wg_publish(p, WS_F3 + (t2d * 16 + c2d) * 8, tag);
    STAMP(13);
    phase4(p, lds, lane, w STAMP_PASS);
  }
  while (__builtin_amdgcn_s_memrealtime() - tstart < (u64)(PADTO) * 100) __builtin_amdgcn_s_sleep(1);
}

extern "C" void kernel_launch(void* const* d_in, const int* in_sizes, int n_in, void* d_out, int out_size, void* d_ws, size_t ws_size, hipStream_t stream) {
  Params p;
  p.x = (const unsigned short*)d_in[0]; p.wqkv = (const unsigned short*)d_in[1]; p.wo = (const unsigned short*)d_in[2];
  p.w1 = (const unsigned short*)d_in[4]; p.b1 = (const unsigned short*)d_in[5]; p.w2 = (const unsigned short*)d_in[6]; p.b2 = (const unsigned short*)d_in[7];
  p.out = (unsigned short*)d_out; p.ws = (unsigned char*)d_ws;
  hipFuncSetAttribute((const void*)fused_block, hipFuncAttributeMaxDynamicSharedMemorySize, LDS_BYTES);
  hipLaunchKernelGGL(fused_block, dim3(256), dim3(768), LDS_BYTES, stream, p);
}
```

```cpp
#include <hip/hip_runtime.h>
#include <hip/hip_bf16.h>
#include <stdint.h>

#define GAS __attribute__((address_space(1)))
#define LAS __attribute__((address_space(3)))
#define DEVI __device__ __forceinline__
typedef unsigned v4u __attribute__((ext_vector_type(4)));
typedef unsigned v2u __attribute__((ext_vector_type(2)));
typedef float f32x4 __attribute__((ext_vector_type(4)));
typedef short bf16x8 __attribute__((ext_vector_type(8)));
typedef short s16x4 __attribute__((ext_vector_type(4)));
typedef __bf16 bf16x2 __attribute__((ext_vector_type(2)));
typedef unsigned long long u64;
typedef GAS u64 gu64;
#define RLX_AGENT __ATOMIC_RELAXED, __HIP_MEMORY_SCOPE_AGENT

#define NLAUNCH 1
#define OUT_SC1 0
#define WS_AT_END 0
#define TOUCH 0
#define P1_SAME 0
#define POLL_SLEEP 2
#define P0_POLL_DELAY 64
#define PADTO 45
#define PADMASK 0
#define DIAG 0

constexpr int T_ = 2048, D_ = 640, F_ = 1536;
constexpr unsigned WS_NONCE = 0;
constexpr unsigned WS_F0Q = 40960;
constexpr unsigned WS_F0O = 8192;
constexpr unsigned WS_F0A = 12288;
constexpr unsigned WS_F0B = 16384;
constexpr unsigned WS_F0X = 20480;
constexpr unsigned WS_F1 = 24576;
constexpr unsigned WS_F2 = 28672;
constexpr unsigned WS_F3 = 32768;
constexpr unsigned WS_XN = 131072;
constexpr unsigned WS_WQ = WS_XN + 128 * 20480;
constexpr unsigned WS_WO = WS_WQ + 16 * 20 * 8 * 1024;
constexpr unsigned WS_W1 = WS_WO + 8 * 20 * 5 * 1024;
constexpr unsigned WS_W2 = WS_W1 + 16 * 20 * 6 * 1024;
constexpr unsigned WS_O = WS_W2 + 8 * 48 * 5 * 1024;
constexpr unsigned WS_H = WS_O + 128 * 20480;
constexpr unsigned WS_H1 = WS_H + 128 * 20480;
constexpr unsigned WS_ST = WS_H1 + 128 * 49152;
constexpr unsigned WS_END = WS_ST + 128 * 2048;
constexpr unsigned LDS_BYTES = 163840;
constexpr unsigned L_STASH = 153600;
constexpr unsigned L_P0 = 104448;
constexpr unsigned L_TAG = 153600;
constexpr unsigned L_GO = 153616;
#define SHADOW_SLEEP 24
constexpr unsigned P1_STAGE = 34816;
constexpr unsigned L_P1_EX = 102400;
constexpr unsigned P1_RS = 264;
constexpr unsigned L_EXH = 124928;
constexpr unsigned L_P3_B1 = 122880;
constexpr unsigned L_P4_B2 = 145408;

struct Params {
  const unsigned short* x; const unsigned short* wqkv; const unsigned short* wo; const unsigned short* w1; const unsigned short* b1;
  const unsigned short* w2; const unsigned short* b2; unsigned short* out; unsigned char* ws;
};

typedef __amdgpu_buffer_rsrc_t rsrc_t;
DEVI rsrc_t mk_rsrc(const void* p, unsigned bytes) { return __builtin_amdgcn_make_buffer_rsrc((void*)p, 0, bytes, 0x00020000); }
DEVI v4u mk_srd(const void* p, unsigned bytes) {
  u64 a = (u64)p; v4u r; r.x = (unsigned)a; r.y = (unsigned)(a >> 32) & 0xffffu; r.z = bytes; r.w = 0x00020000u; return r;
}
template <int AUX> DEVI void dma16(rsrc_t rs, LAS char* lds, unsigned voff, unsigned soff) {
  __builtin_amdgcn_raw_ptr_buffer_load_lds(rs, (LAS void*)lds, 16, voff, soff, 0, AUX);
}
DEVI void aload(v4u& d, v4u srd, unsigned voff, unsigned soff) {
  asm volatile("buffer_load_dwordx4 %0, %1, %2, %3 offen sc1" : "=v"(d) : "v"(voff), "s"(srd), "s"(soff) : "memory");
}
DEVI void st16(rsrc_t rs, v4u v, unsigned voff, unsigned soff) { __builtin_amdgcn_raw_buffer_store_b128(v, rs, voff + soff, 0, 16); }
#define WAITV(n) asm volatile("s_waitcnt vmcnt(%0)" ::"i"(n) : "memory")
#define WAITV1(n, a) asm volatile("s_waitcnt vmcnt(%1)" : "+v"(a) : "i"(n) : "memory")
#define WAITV2(n, a, b) asm volatile("s_waitcnt vmcnt(%2)" : "+v"(a), "+v"(b) : "i"(n) : "memory")
#define WAITV4(n, a, b, c, d) asm volatile("s_waitcnt vmcnt(%4)" : "+v"(a), "+v"(b), "+v"(c), "+v"(d) : "i"(n) : "memory")
#define WAITL0() asm volatile("s_waitcnt lgkmcnt(0)" ::: "memory")
#define BAR() __builtin_amdgcn_s_barrier()
#define SCHED0() __builtin_amdgcn_sched_barrier(0)
DEVI void lbar() { WAITL0(); BAR(); asm volatile("" ::: "memory"); }
DEVI void rbar() { asm volatile("" ::: "memory"); BAR(); asm volatile("" ::: "memory"); }

DEVI unsigned pk2(float a, float b) { bf16x2 p; p[0] = (__bf16)a; p[1] = (__bf16)b; return __builtin_bit_cast(unsigned, p); }
DEVI float bflo(unsigned u) { return __uint_as_float(u << 16); }
DEVI float bfhi(unsigned u) { return __uint_as_float(u & 0xffff0000u); }
DEVI bf16x8 asbf(v4u v) { return __builtin_bit_cast(bf16x8, v); }
DEVI f32x4 mfma(v4u a, v4u b, f32x4 c) { return __builtin_amdgcn_mfma_f32_16x16x32_bf16(asbf(a), asbf(b), c, 0, 0, 0); }
DEVI v4u ldsr16(LAS char* p) { return *(LAS v4u*)p; }
DEVI float fsel(bool c, float a, float b) { asm("" : "+v"(a)); asm("" : "+v"(b)); return c ? a : b; }
DEVI float xrow_max(float v) {
  unsigned u = __float_as_uint(v);
  auto a = __builtin_amdgcn_permlane32_swap(u, u, false, false); v = fmaxf(__uint_as_float(a[0]), __uint_as_float(a[1]));
  u = __float_as_uint(v);
  auto b = __builtin_amdgcn_permlane16_swap(u, u, false, false); return fmaxf(__uint_as_float(b[0]), __uint_as_float(b[1]));
}
DEVI float xrow_sum(float v) {
  unsigned u = __float_as_uint(v);
  auto a = __builtin_amdgcn_permlane32_swap(u, u, false, false); v = __uint_as_float(a[0]) + __uint_as_float(a[1]);
  u = __float_as_uint(v);
  auto b = __builtin_amdgcn_permlane16_swap(u, u, false, false); return __uint_as_float(b[0]) + __uint_as_float(b[1]);
}
DEVI float row16_sum(float v) {
  v += __uint_as_float(__builtin_amdgcn_update_dpp(0u, __float_as_uint(v), 0x128, 0xf, 0xf, false));
  v += __uint_as_float(__builtin_amdgcn_update_dpp(0u, __float_as_uint(v), 0x124, 0xf, 0xf, false));
  v += __uint_as_float(__builtin_amdgcn_update_dpp(0u, __float_as_uint(v), 0x122, 0xf, 0xf, false));
  v += __uint_as_float(__builtin_amdgcn_update_dpp(0u, __float_as_uint(v), 0x121, 0xf, 0xf, false));
  return v;
}

DEVI u64 rfl64(u64 v) { return ((u64)__builtin_amdgcn_readfirstlane((unsigned)(v >> 32)) << 32) | (unsigned)__builtin_amdgcn_readfirstlane((unsigned)v); }
DEVI void publish(unsigned char* ws, unsigned off, u64 tag) { __hip_atomic_store((gu64*)(ws + off), tag, RLX_AGENT); }
DEVI void wait_flags(unsigned char* ws, unsigned laneoff, bool active, u64 tag) {
  for (unsigned sp = 0; sp < (1u << 18); ++sp) {
    u64 v = tag;
    if (active) v = __hip_atomic_load((gu64*)(ws + laneoff), RLX_AGENT);
    if (__all(v == tag)) break;
    __builtin_amdgcn_s_sleep(POLL_SLEEP);
  }
  __builtin_amdgcn_fence(__ATOMIC_ACQUIRE, "wavefront");
  asm volatile("" ::: "memory");
}

DEVI void wait_flags2(unsigned char* ws, unsigned off1, bool act1, unsigned off2, bool act2, u64 tag) {
  for (unsigned sp = 0; sp < (1u << 18); ++sp) {
    u64 v1 = tag, v2 = tag;
    if (act1) v1 = __hip_atomic_load((gu64*)(ws + off1), RLX_AGENT);
    if (act2) v2 = __hip_atomic_load((gu64*)(ws + off2), RLX_AGENT);
    if (__all(v1 == tag && v2 == tag)) break;
    __builtin_amdgcn_s_sleep(POLL_SLEEP);
  }
  __builtin_amdgcn_fence(__ATOMIC_ACQUIRE, "wavefront");
  asm volatile("" ::: "memory");
}

#define STAMP_ARG
#define STAMP_PASS
#define STAMP(id) do {} while (0)

template <int KIND>
DEVI unsigned retile_unit(const Params& p, rsrc_t ws, LAS char* st, int lane, int a, int ks STAMP_ARG) {
  constexpr int NCH = KIND == 2 ? 12 : 10;
  constexpr int NI = 32 * NCH / 64;
  constexpr int NT = KIND == 2 ? 6 : 5;
  constexpr unsigned RS = KIND == 2 ? 3072u : 1280u;
  const void* src = KIND == 1 ? (const void*)p.wo : KIND == 2 ? (const void*)p.w1 : (const void*)p.w2;
  constexpr unsigned SB = KIND == 1 ? 640u * 1280u : KIND == 2 ? 640u * 3072u : 1536u * 1280u;
  rsrc_t rs = mk_rsrc(src, SB);
#pragma unroll
  for (int n = 0; n < NI; ++n) {
    int q = n * 64 + lane;
    int rho = q / NCH, ch = q - rho * NCH;
    int row; unsigned col;
    const int g = rho >> 3, t = (rho >> 2) & 1, e = rho & 3;
    if (KIND == 1) { row = ks < 16 ? (2 * ks + t) * 20 + 4 * g + e : (2 * (4 * (ks - 16) + g) + t) * 20 + 16 + e; col = 160 * a + 16 * ch; }
    else if (KIND == 2) { row = 32 * ks + rho; col = 192 * a + 16 * ch; }
    else { row = 32 * ks + 16 * t + 4 * g + e; col = 160 * a + 16 * ch; }
    dma16<0>(rs, st + n * 1024, (unsigned)row * RS + col, 0);
  }
  WAITV(0);
  STAMP(18);
  unsigned dst;
  if (KIND == 1) dst = WS_WO + (unsigned)(a * 20 + ks) * 5120;
  else if (KIND == 2) dst = WS_W1 + (unsigned)(a * 20 + ks) * 6144;
  else { int kh = ks / 24, s = ks - kh * 24; dst = WS_W2 + (unsigned)a * 245760 + (s >> 2) * 40960 + kh * 20480 + (s & 3) * 5120; }
  const int g = lane >> 4, q4 = (lane & 15) >> 2, p4 = lane & 3;
  constexpr int RB = NCH * 16;
  LAS char* base = st + (8 * g + q4) * RB + 8 * p4;
#pragma unroll
  for (int t = 0; t < NT; ++t) {
    s16x4 lo = __builtin_amdgcn_ds_read_tr16_b64_v4i16((LAS s16x4*)(base + 32 * t));
    s16x4 hi = __builtin_amdgcn_ds_read_tr16_b64_v4i16((LAS s16x4*)(base + 4 * RB + 32 * t));
    v2u l2 = __builtin_bit_cast(v2u, lo), h2 = __builtin_bit_cast(v2u, hi);
    st16(ws, v4u{l2.x, l2.y, h2.x, h2.y}, lane * 16, dst + t * 1024);
  }
  STAMP(19);
  return KIND == 1 ? WS_F0O + (a * 32 + ks) * 8 : KIND == 2 ? WS_F0A + (a * 32 + ks) * 8 : WS_F0B + (a * 64 + ks) * 8;
}

DEVI void wqkv_unit(const Params& p, rsrc_t ws, LAS char* lds, int lane, int w, int u, u64 nonce, u64& tag, bool flags STAMP_ARG) {
  const int ks = u >> 2, hf = (u >> 1) & 1, rh = u & 1;
  rsrc_t rs = mk_rsrc(p.wqkv, 640u * 3840u);
#pragma unroll
  for (int m = 0; m < 4; ++m) {
    const int n = w + 8 * m;
    if (n < 30) {
      const int q = n * 64 + lane, row = q / 120, ch = q - row * 120, sec = ch / 40;
      dma16<0>(rs, lds + L_P0 + n * 1024, (unsigned)(32 * ks + 16 * rh + row) * 3840u + sec * 1280 + 640 * hf + 16 * (ch - sec * 40), 0);
    }
  }
  STAMP(17);
  WAITV(0);
  if (flags && w == 0) { asm volatile("" : "+v"(nonce)); if (lane == 0) *(LAS u64*)(lds + L_TAG) = nonce * 6364136223846793005ull + 1442695040888963407ull; }
  lbar();
  STAMP(18);
  if (flags) tag = rfl64(*(LAS u64*)(lds + L_TAG));
  const int gl = (lane >> 4) & 1, th = lane >> 5, q4 = (lane & 15) >> 2, p4 = lane & 3;
  LAS char* base = lds + L_P0 + (8 * gl + q4) * 1920 + 80 * w;
  const unsigned dst = WS_WQ + (unsigned)((8 * hf + w) * 20 + ks) * 8192;
  const unsigned vo = th * 1024 + (2 * rh + gl) * 256 + (lane & 15) * 16;
#pragma unroll
  for (int m = 0; m < 4; ++m) {
    int cb;
    if (m == 0) cb = 640 + 40 * th + 8 * p4;
    else if (m == 1) cb = th == 0 ? (p4 < 2 ? 640 : 0) + 32 + 40 * (p4 & 1) : 8 * p4;
    else if (m == 2) cb = th == 0 ? 40 + 8 * p4 : 1280 + 8 * p4;
    else cb = th == 0 ? 1280 + 40 + 8 * p4 : 1280 + 32 + 40 * (p4 & 1);
    s16x4 lo = __builtin_amdgcn_ds_read_tr16_b64_v4i16((LAS s16x4*)(base + cb));
    s16x4 hi = __builtin_amdgcn_ds_read_tr16_b64_v4i16((LAS s16x4*)(base + 4 * 1920 + cb));
    v2u l2 = __builtin_bit_cast(v2u, lo), h2 = __builtin_bit_cast(v2u, hi);
    v4u v = {l2.x, l2.y, h2.x, h2.y};
    if (m == 3 && th == 1 && (lane & 15) >= 8) v = v4u{0u, 0u, 0u, 0u};
    st16(ws, v, vo, dst + m * 2048);
  }
  STAMP(19);
  if (flags) { WAITV(0); STAMP(20); if (lane == 0) publish(p.ws, WS_F0Q + ((8 * hf + w) * 64 + ks * 2 + rh) * 8, tag); }
}

DEVI void ln_unit(const Params& p, rsrc_t ws, LAS char* lds, int lane, int w, int tb, u64 nonce, u64& tag, bool flags STAMP_ARG) {
  const int t = lane >> 4, i = lane & 15, c = 4 * w + t;
  const GAS v4u* src = (const GAS v4u*)((const GAS char*)p.x + (size_t)(tb * 16 + c) * 1280) + i;
  v4u r[5];
#pragma unroll
  for (int n = 0; n < 5; ++n) r[n] = src[16 * n];
  STAMP(17);
  float v[40];
#pragma unroll
  for (int n = 0; n < 5; ++n)
#pragma unroll
    for (int e = 0; e < 4; ++e) { v[n * 8 + 2 * e] = bflo(r[n][e]); v[n * 8 + 2 * e + 1] = bfhi(r[n][e]); }
  float s4[4] = {0.f, 0.f, 0.f, 0.f};
#pragma unroll
  for (int e = 0; e < 40; ++e) s4[e & 3] += v[e];
  const float mean = row16_sum((s4[0] + s4[1]) + (s4[2] + s4[3])) * (1.f / 640.f);
  float q4[4] = {0.f, 0.f, 0.f, 0.f};
#pragma unroll
  for (int e = 0; e < 40; ++e) { v[e] -= mean; q4[e & 3] = fmaf(v[e], v[e], q4[e & 3]); }
  const float rstd = __builtin_amdgcn_rsqf(row16_sum((q4[0] + q4[1]) + (q4[2] + q4[3])) * (1.f / 640.f) + 1e-5f);
  LAS char* st = lds + L_P0;
#pragma unroll
  for (int n = 0; n < 5; ++n) {
    v4u o;
#pragma unroll
    for (int e = 0; e < 4; ++e) o[e] = pk2(v[n * 8 + 2 * e] * rstd, v[n * 8 + 2 * e + 1] * rstd);
    *(LAS v4u*)(st + (i + 16 * n) * 272 + c * 16) = o;
  }
  if (flags && w == 0) { if (lane == 0) *(LAS u64*)(lds + L_TAG) = nonce * 6364136223846793005ull + 1442695040888963407ull; }
  STAMP(18);
  lbar();
  if (flags) tag = rfl64(*(LAS u64*)(lds + L_TAG));
#pragma unroll
  for (int n = 0; n < 5; ++n) {
    const int ks = w * 5 + n;
    v4u o = ldsr16(st + (4 * ks + (lane >> 4)) * 272 + (lane & 15) * 16);
    st16(ws, o, lane * 16, WS_XN + (unsigned)tb * 20480 + ks * 1024);
  }
  STAMP(19);
  if (flags) { WAITV(0); STAMP(20); if (lane == 0) publish(p.ws, WS_F0X + (tb * 4 + w) * 8, tag); }
}

DEVI unsigned phase0(const Params& p, LAS char* lds, int lane, int w, u64& tag, bool flags STAMP_ARG) {
  rsrc_t ws = mk_rsrc(p.ws, WS_END);
  const int cu = blockIdx.x;
  u64 nonce = 0;
  if (flags && w == 0) nonce = __hip_atomic_load((gu64*)(p.ws + WS_NONCE), RLX_AGENT);
  if (w >= 8) { if (w == 8 && lane == 0) *(LAS unsigned*)(lds + L_GO) = 0u; lbar(); tag = rfl64(*(LAS u64*)(lds + L_TAG)); return 0; }
  if (cu < 128 && w < 4) { ln_unit(p, ws, lds, lane, w, cu, nonce, tag, flags STAMP_PASS); return 0; }
  if (cu >= 128 && cu < 208) { wqkv_unit(p, ws, lds, lane, w, cu - 128, nonce, tag, flags STAMP_PASS); return 0; }
  int slot;
  if (cu < 128) slot = cu * 4 + (w - 4);
  else {
    slot = 512 + (cu - 208) * 8 + w;
    if (flags && w == 0 && lane == 0) *(LAS u64*)(lds + L_TAG) = nonce * 6364136223846793005ull + 1442695040888963407ull;
  }
  lbar();
  if (flags) tag = rfl64(*(LAS u64*)(lds + L_TAG));
  if (cu >= 208) __builtin_amdgcn_s_sleep(SHADOW_SLEEP);
  STAMP(17);
  LAS char* st = lds + L_P0 + (cu < 128 ? 21760 + (w - 4) * 6144 : w * 6144);
  if (slot < 160) return retile_unit<1>(p, ws, st, lane, slot & 7, slot >> 3 STAMP_PASS);
  if (slot < 480) { int u = slot - 160; return retile_unit<2>(p, ws, st, lane, u & 15, u >> 4 STAMP_PASS); }
  if (slot < 864) { int u = slot - 480; return retile_unit<3>(p, ws, st, lane, u & 7, u >> 3 STAMP_PASS); }
  return 0;
}

template <int NB>
DEVI void p1_loop(LAS char* lds, unsigned t0o, unsigned t1o, unsigned xo, f32x4 (&acc)[2][5]) {
  v4u W[2][2], X[2][NB];
#define P1_RO(ks) ((((ks) >> 1) & 3) * P1_STAGE + ((ks) & 1) * 17408)
#define P1_READ(ks) do { W[(ks) & 1][0] = ldsr16(lds + P1_RO(ks) + t0o); W[(ks) & 1][1] = ldsr16(lds + P1_RO(ks) + t1o); \
    _Pragma("unroll") for (int b = 0; b < NB; ++b) X[(ks) & 1][b] = ldsr16(lds + P1_RO(ks) + xo + b * 1024); } while (0)
  rbar();
  P1_READ(0);
  SCHED0();
#pragma unroll
  for (int ks = 0; ks < 20; ++ks) {
    if ((ks & 1) && ks < 19) lbar();
    if (ks + 1 < 20) P1_READ(ks + 1);
#pragma unroll
    for (int b = 0; b < NB; ++b) { acc[0][b] = mfma(W[ks & 1][0], X[ks & 1][b], acc[0][b]); acc[1][b] = mfma(W[ks & 1][1], X[ks & 1][b], acc[1][b]); }
    if (ks & 1) {
      __builtin_amdgcn_sched_group_barrier(0x100, 2, 0);
#pragma unroll
      for (int b = 0; b < NB; ++b) { __builtin_amdgcn_sched_group_barrier(0x8, 2, 0); __builtin_amdgcn_sched_group_barrier(0x100, 1, 0); }
    } else {
      __builtin_amdgcn_sched_group_barrier(0x100, 2, 0); __builtin_amdgcn_sched_group_barrier(0x8, 1, 0);
      __builtin_amdgcn_sched_group_barrier(0x100, 2, 0); __builtin_amdgcn_sched_group_barrier(0x8, 1, 0);
      __builtin_amdgcn_sched_group_barrier(0x100, 2, 0); __builtin_amdgcn_sched_group_barrier(0x8, 1, 0);
      __builtin_amdgcn_sched_group_barrier(0x100, NB - 4, 0); __builtin_amdgcn_sched_group_barrier(0x8, 2 * NB - 3, 0);
    }
    SCHED0();
  }
#undef P1_RO
#undef P1_READ
}

DEVI void phase1(const Params& p, LAS char* lds, int lane, int w, u64 tag, unsigned pending STAMP_ARG) {
  const int x8 = blockIdx.x & 7, r = blockIdx.x >> 3;
  const int ti = 4 * (x8 >> 1) + (r >> 3), pr = 8 * (x8 & 1) + (r & 7);
  rsrc_t ws = mk_rsrc(p.ws, WS_END);
  const unsigned l16 = lane * 16;
  if (pending) { WAITV(0); if (lane == 0) publish(p.ws, pending, tag); }
  STAMP(2);
  const int np = w & 3, rg = w >> 2;
  const int T0 = np == 0 ? 0 : np == 1 ? 2 : np == 2 ? 5 : 3, T1 = np == 0 ? 1 : np == 1 ? 7 : np == 2 ? 6 : 4;
  f32x4 acc[2][5];
#pragma unroll
  for (int e = 0; e < 2; ++e)
#pragma unroll
    for (int b = 0; b < 5; ++b) acc[e][b] = f32x4{0.f, 0.f, 0.f, 0.f};
  const unsigned t0o = (9 + T0) * 1024 + l16, t1o = (9 + T1) * 1024 + l16;
  if (rg == 0) p1_loop<5>(lds, t0o, t1o, l16, acc);
  else p1_loop<4>(lds, t0o, t1o, 5 * 1024 + l16, acc);
  STAMP(3);
  const int g = lane >> 4, c = lane & 15;
  {
    LAS char* d = lds + L_P1_EX + (rg * 80 + c) * P1_RS + g * 8;
#pragma unroll
    for (int b = 0; b < 5; ++b) {
      if (b < 4 || rg == 0) {
        *(LAS v2u*)(d + b * 16 * P1_RS + T0 * 32) = v2u{pk2(acc[0][b][0], acc[0][b][1]), pk2(acc[0][b][2], acc[0][b][3])};
        *(LAS v2u*)(d + b * 16 * P1_RS + T1 * 32) = v2u{pk2(acc[1][b][0], acc[1][b][1]), pk2(acc[1][b][2], acc[1][b][3])};
      }
    }
  }
  STAMP(22);
  lbar();
  STAMP(23);
  const int blk = 8 * ti + w;
  LAS char* own = lds + L_P1_EX + ((w + 1) * 16 + c) * P1_RS + g * 8;
  LAS char* trp = lds + L_P1_EX + ((w + 1) * 16 + 4 * g + (c >> 2)) * P1_RS + (c & 3) * 8;
  v2u ka[2], kp[2], qa[2], qr[2], va[2], vp[2];
  const v2u kr = *(LAS v2u*)(own + 64), krp = *(LAS v2u*)(own + 64 - 16 * P1_RS);
#pragma unroll
  for (int hh = 0; hh < 2; ++hh) {
    ka[hh] = *(LAS v2u*)(own + hh * 32); kp[hh] = *(LAS v2u*)(own + hh * 32 - 16 * P1_RS);
    qa[hh] = *(LAS v2u*)(own + (3 + hh) * 32);
    qr[hh] = *(LAS v2u*)(own - g * 8 + 64 + 16 + hh * 8);
    va[hh] = __builtin_bit_cast(v2u, __builtin_amdgcn_ds_read_tr16_b64_v4i16((LAS s16x4*)(trp + (5 + hh) * 32)));
    vp[hh] = __builtin_bit_cast(v2u, __builtin_amdgcn_ds_read_tr16_b64_v4i16((LAS s16x4*)(trp + (5 + hh) * 32 - 16 * P1_RS)));
  }
  const v2u vr = __builtin_bit_cast(v2u, __builtin_amdgcn_ds_read_tr16_b64_v4i16((LAS s16x4*)(trp + 7 * 32)));
  const v2u vrp = __builtin_bit_cast(v2u, __builtin_amdgcn_ds_read_tr16_b64_v4i16((LAS s16x4*)(trp + 7 * 32 - 16 * P1_RS)));
  STAMP(24);
  SCHED0();
  const int odd = c & 1, ce = c - odd;
  const bool havePrev = (ti > 0) || (w > 0);
  const bool v1a = havePrev && (4 * g >= ce + 2), v1b = havePrev && (4 * g >= ce), v2a = ce >= 4 * g, v2b = ce - 2 >= 4 * g;
  const float c2 = 0.22360679774997896f * 1.4426950408889634f, NEG = -3.0e38f;
  const f32x4 z = {0.f, 0.f, 0.f, 0.f};
  f32x4 S1[2], S2[2];
#pragma unroll
  for (int hh = 0; hh < 2; ++hh) {
    const bool mine = g == hh;
    const v4u Q = {qa[hh].x, qa[hh].y, mine ? qr[hh].x : 0u, mine ? qr[hh].y : 0u};
    S1[hh] = mfma(v4u{kp[hh].x, kp[hh].y, krp.x, krp.y}, Q, z);
    S2[hh] = mfma(v4u{ka[hh].x, ka[hh].y, kr.x, kr.y}, Q, z);
  }
  float s1a[2], s1b[2], s2a[2], s2b[2], m[2], inv[2];
#pragma unroll
  for (int hh = 0; hh < 2; ++hh) {
    s1a[hh] = fsel(v1a, fsel(odd, S1[hh][1], S1[hh][0]), NEG); s1b[hh] = fsel(v1b, fsel(odd, S1[hh][3], S1[hh][2]), NEG);
    s2a[hh] = fsel(v2a, fsel(odd, S2[hh][1], S2[hh][0]), NEG); s2b[hh] = fsel(v2b, fsel(odd, S2[hh][3], S2[hh][2]), NEG);
    m[hh] = fmaxf(fmaxf(s1a[hh], s1b[hh]), fmaxf(s2a[hh], s2b[hh]));
  }
  {
    auto a0 = __builtin_amdgcn_permlane32_swap(__float_as_uint(m[0]), __float_as_uint(m[0]), false, false);
    auto a1 = __builtin_amdgcn_permlane32_swap(__float_as_uint(m[1]), __float_as_uint(m[1]), false, false);
    m[0] = fmaxf(__uint_as_float(a0[0]), __uint_as_float(a0[1])); m[1] = fmaxf(__uint_as_float(a1[0]), __uint_as_float(a1[1]));
    auto b0 = __builtin_amdgcn_permlane16_swap(__float_as_uint(m[0]), __float_as_uint(m[0]), false, false);
    auto b1 = __builtin_amdgcn_permlane16_swap(__float_as_uint(m[1]), __float_as_uint(m[1]), false, false);
    m[0] = fmaxf(__uint_as_float(b0[0]), __uint_as_float(b0[1])); m[1] = fmaxf(__uint_as_float(b1[0]), __uint_as_float(b1[1]));
  }
#pragma unroll
  for (int hh = 0; hh < 2; ++hh) {
    s1a[hh] = __builtin_amdgcn_exp2f((s1a[hh] - m[hh]) * c2); s1b[hh] = __builtin_amdgcn_exp2f((s1b[hh] - m[hh]) * c2);
    s2a[hh] = __builtin_amdgcn_exp2f((s2a[hh] - m[hh]) * c2); s2b[hh] = __builtin_amdgcn_exp2f((s2b[hh] - m[hh]) * c2);
    inv[hh] = (s1a[hh] + s1b[hh]) + (s2a[hh] + s2b[hh]);
  }
  {
    auto a0 = __builtin_amdgcn_permlane32_swap(__float_as_uint(inv[0]), __float_as_uint(inv[0]), false, false);
    auto a1 = __builtin_amdgcn_permlane32_swap(__float_as_uint(inv[1]), __float_as_uint(inv[1]), false, false);
    inv[0] = __uint_as_float(a0[0]) + __uint_as_float(a0[1]); inv[1] = __uint_as_float(a1[0]) + __uint_as_float(a1[1]);
    auto b0 = __builtin_amdgcn_permlane16_swap(__float_as_uint(inv[0]), __float_as_uint(inv[0]), false, false);
    auto b1 = __builtin_amdgcn_permlane16_swap(__float_as_uint(inv[1]), __float_as_uint(inv[1]), false, false);
    inv[0] = __builtin_amdgcn_rcpf(__uint_as_float(b0[0]) + __uint_as_float(b0[1])); inv[1] = __builtin_amdgcn_rcpf(__uint_as_float(b1[0]) + __uint_as_float(b1[1]));
  }
  STAMP(25);
  f32x4 OA[2], OR[2];
  const unsigned sh = 16 * odd;
#pragma unroll
  for (int hh = 0; hh < 2; ++hh) {
    const v4u P = {pk2(s1a[hh] * inv[hh], 0.f) << sh, pk2(s1b[hh] * inv[hh], 0.f) << sh, pk2(s2a[hh] * inv[hh], 0.f) << sh, pk2(s2b[hh] * inv[hh], 0.f) << sh};
    OA[hh] = mfma(v4u{vp[hh].x, vp[hh].y, va[hh].x, va[hh].y}, P, z);
    OR[hh] = mfma(v4u{vrp.x, vrp.y, vr.x, vr.y}, P, z);
  }
  {
    v4u om = {pk2(OA[0][0], OA[0][1]), pk2(OA[0][2], OA[0][3]), pk2(OA[1][0], OA[1][1]), pk2(OA[1][2], OA[1][3])};
    st16(ws, om, l16, WS_O + (unsigned)blk * 20480 + pr * 1024);
    unsigned r0x = pk2(OR[0][0], OR[0][1]), r0y = pk2(OR[0][2], OR[0][3]), r1x = pk2(OR[1][0], OR[1][1]), r1y = pk2(OR[1][2], OR[1][3]);
    auto t0 = __builtin_amdgcn_permlane16_swap(r1x, r1x, false, false); auto t1 = __builtin_amdgcn_permlane16_swap(r1y, r1y, false, false);
    v4u orr = {r0x, r0y, t0[1], t1[1]};
    if (lane < 16) st16(ws, orr, l16, WS_O + (unsigned)blk * 20480 + (16 + (pr >> 2)) * 1024 + (pr & 3) * 256);
  }
  STAMP(4);
}

DEVI void exchange_halves(LAS char* lds, const f32x4 (&acc)[5], f32x4 (&fin)[3], int lane, int w) {
  LAS char* d = lds + L_EXH + (w & 3) * 5120 + lane * 16;
  if (w < 4) { *(LAS f32x4*)(d + 3072) = acc[3]; *(LAS f32x4*)(d + 4096) = acc[4]; }
  else { *(LAS f32x4*)(d) = acc[0]; *(LAS f32x4*)(d + 1024) = acc[1]; *(LAS f32x4*)(d + 2048) = acc[2]; }
  lbar();
  if (w < 4) { f32x4 u0 = *(LAS f32x4*)(d), u1 = *(LAS f32x4*)(d + 1024), u2 = *(LAS f32x4*)(d + 2048); fin[0] = acc[0] + u0; fin[1] = acc[1] + u1; fin[2] = acc[2] + u2; }
  else { f32x4 u0 = *(LAS f32x4*)(d + 3072), u1 = *(LAS f32x4*)(d + 4096); fin[0] = acc[3] + u0; fin[1] = acc[4] + u1; fin[2] = f32x4{0.f, 0.f, 0.f, 0.f}; }
}

DEVI void phase2(const Params& p, LAS char* lds, int lane, int w STAMP_ARG) {
  const int x8 = blockIdx.x & 7, r = blockIdx.x >> 3;
  const int ti = 2 * x8 + (r >> 4), hb = (r >> 3) & 1, j = r & 7;
  rsrc_t ws = mk_rsrc(p.ws, WS_END);
  const v4u srd = mk_srd(p.ws, WS_END);
  const unsigned l16 = lane * 16;
  const int b = w & 3, kh = w >> 2, blk = 8 * ti + 4 * hb + b;
  rbar();
  STAMP(6);
  v4u a[10];
  const unsigned oa = WS_O + (unsigned)blk * 20480 + 10 * kh * 1024;
#pragma unroll
  for (int s = 0; s < 10; ++s) aload(a[s], srd, l16, oa + s * 1024);
  f32x4 acc[5];
#pragma unroll
  for (int t = 0; t < 5; ++t) acc[t] = f32x4{0.f, 0.f, 0.f, 0.f};
  v4u B[2][5];
  LAS char* bb = lds + kh * 51200 + l16;
#pragma unroll
  for (int t = 0; t < 5; ++t) B[0][t] = ldsr16(bb + t * 1024);
  SCHED0();
#pragma unroll
  for (int s = 0; s < 10; ++s) {
    asm volatile("s_waitcnt vmcnt(%1)" : "+v"(a[s]) : "i"(9 - s) : "memory");
    if (s + 1 < 10) {
#pragma unroll
      for (int t = 0; t < 5; ++t) B[(s + 1) & 1][t] = ldsr16(bb + (s + 1) * 5120 + t * 1024);
    }
#pragma unroll
    for (int t = 0; t < 5; ++t) acc[t] = mfma(B[s & 1][t], a[s], acc[t]);
#pragma unroll
    for (int t = 0; t < 5; ++t) { __builtin_amdgcn_sched_group_barrier(0x100, 1, 0); __builtin_amdgcn_sched_group_barrier(0x8, 1, 0); }
    SCHED0();
  }
  STAMP(7);
  const int g = lane >> 4, c = lane & 15, row = 16 * b + c, mt0 = 3 * kh;
  LAS char* sx = lds + L_STASH + row * 160 + mt0 * 32 + g * 8;
  v2u xv[3];
#pragma unroll
  for (int e = 0; e < 3; ++e) xv[e] = *(LAS v2u*)(sx + (e < 2 || kh == 0 ? e : 0) * 32);
  f32x4 fin[3];
  exchange_halves(lds, acc, fin, lane, w);
  v2u hv[3];
  float ssum = 0.f, ssq = 0.f;
#pragma unroll
  for (int e = 0; e < 3; ++e) {
    hv[e] = v2u{pk2(fin[e][0] + bflo(xv[e].x), fin[e][1] + bfhi(xv[e].x)), pk2(fin[e][2] + bflo(xv[e].y), fin[e][3] + bfhi(xv[e].y))};
    if (e < 2 || kh == 0) {
      *(LAS v2u*)(sx + e * 32) = hv[e];
      const float h0 = bflo(hv[e].x), h1 = bfhi(hv[e].x), h2 = bflo(hv[e].y), h3 = bfhi(hv[e].y);
      ssum += (h0 + h1) + (h2 + h3); ssq += fmaf(h0, h0, h1 * h1) + fmaf(h2, h2, h3 * h3);
    }
  }
  {
    auto sx0 = __builtin_amdgcn_permlane16_swap(hv[0].x, hv[1].x, false, false); auto sy0 = __builtin_amdgcn_permlane16_swap(hv[0].y, hv[1].y, false, false);
    const int G = 2 * (mt0 + (g & 1)) + (g >> 1);
    st16(ws, v4u{sx0[0], sy0[0], sx0[1], sy0[1]}, c * 16 + (10 * j + G) * 256, WS_H + (unsigned)blk * 20480);
    if (kh == 0) __builtin_amdgcn_raw_buffer_store_b64(hv[2], ws, WS_H + (unsigned)blk * 20480 + (10 * j + 4 + (g >> 1)) * 256 + c * 16 + (g & 1) * 8, 0, 16);
  }
  ssum = xrow_sum(ssum); ssq = xrow_sum(ssq);
  {
    const int idx = 2 * j + kh;
    const int e = idx & 3;
    if (lane < 16) {
      v2u sv = {__float_as_uint(ssum), __float_as_uint(ssq)};
      __builtin_amdgcn_raw_buffer_store_b64(sv, ws, WS_ST + (unsigned)blk * 2048 + (e >> 1) * 1024 + (idx >> 2) * 256 + c * 16 + (e & 1) * 8, 0, 16);
    }
  }
  STAMP(8);
}

#define P3D 8
DEVI void phase3(const Params& p, LAS char* lds, int lane, int w STAMP_ARG) {
  const int x8 = blockIdx.x & 7, r = blockIdx.x >> 3;
  const int ti = 4 * (x8 >> 1) + (r >> 3), jj = 8 * (x8 & 1) + (r & 7);
  rsrc_t ws = mk_rsrc(p.ws, WS_END);
  const v4u srd = mk_srd(p.ws, WS_END);
  const unsigned l16 = lane * 16;
  const int blk = 8 * ti + w;
  rbar();
  if (w < 6) {
    v4u cf[20];
#pragma unroll
    for (int ks = 0; ks < 20; ++ks) cf[ks] = ldsr16(lds + (ks * 6 + w) * 1024 + l16);
    SCHED0();
    f32x4 cs0 = {0.f, 0.f, 0.f, 0.f}, cs1 = cs0;
    const v4u ones = {0x3f803f80u, 0x3f803f80u, 0x3f803f80u, 0x3f803f80u};
#pragma unroll
    for (int ks = 0; ks < 20; ks += 2) { cs0 = mfma(cf[ks], ones, cs0); cs1 = mfma(cf[ks + 1], ones, cs1); }
    if ((lane & 15) == 0) *(LAS f32x4*)(lds + L_P3_B1 + 1024 + (w * 16 + (lane >> 4) * 4) * 4) = cs0 + cs1;
  }
  lbar();
  STAMP(10);
  v4u a[20], st0, st1;
  const unsigned ha = WS_H + (unsigned)blk * 20480;
#pragma unroll
  for (int s = 0; s < P3D; ++s) aload(a[s], srd, l16, ha + s * 1024);
  f32x4 acc[6];
#pragma unroll
  for (int t = 0; t < 6; ++t) acc[t] = f32x4{0.f, 0.f, 0.f, 0.f};
  v4u B[2][6];
  LAS char* bb = lds + l16;
#pragma unroll
  for (int t = 0; t < 6; ++t) B[0][t] = ldsr16(bb + t * 1024);
  SCHED0();
#pragma unroll
  for (int s = 0; s < 20; ++s) {
    asm volatile("s_waitcnt vmcnt(%1)" : "+v"(a[s]) : "i"(s + P3D - 1 < 22 ? P3D - 1 : 21 - s) : "memory");
    if (s + P3D < 20) aload(a[s + P3D], srd, l16, ha + (s + P3D) * 1024);
    else if (s + P3D == 20) aload(st0, srd, l16, WS_ST + (unsigned)blk * 2048);
    else if (s + P3D == 21) aload(st1, srd, l16, WS_ST + (unsigned)blk * 2048 + 1024);
    if (s + 1 < 20) {
#pragma unroll
      for (int t = 0; t < 6; ++t) B[(s + 1) & 1][t] = ldsr16(bb + (s + 1) * 6144 + t * 1024);
    }
#pragma unroll
    for (int t = 0; t < 6; ++t) acc[t] = mfma(B[s & 1][t], a[s], acc[t]);
#pragma unroll
    for (int t = 0; t < 6; ++t) { __builtin_amdgcn_sched_group_barrier(0x100, 1, 0); __builtin_amdgcn_sched_group_barrier(0x8, 1, 0); }
    SCHED0();
  }
  lbar();
  STAMP(11);
  const int g = lane >> 4;
  f32x4 cs[6]; v2u bb1[6];
#pragma unroll
  for (int t = 0; t < 6; ++t) { cs[t] = *(LAS f32x4*)(lds + L_P3_B1 + 1024 + (t * 16 + g * 4) * 4); bb1[t] = *(LAS v2u*)(lds + L_P3_B1 + (t * 16 + g * 4) * 2); }
  WAITV2(0, st0, st1);
  SCHED0();
  float s1 = (__uint_as_float(st0[0]) + __uint_as_float(st0[2])) + (__uint_as_float(st1[0]) + __uint_as_float(st1[2]));
  float s2 = (__uint_as_float(st0[1]) + __uint_as_float(st0[3])) + (__uint_as_float(st1[1]) + __uint_as_float(st1[3]));
  s1 = xrow_sum(s1); s2 = xrow_sum(s2);
  const float mean = s1 * (1.f / 640.f);
  const float var = fmaxf(s2 * (1.f / 640.f) - mean * mean, 0.f);
  const float rstd = __builtin_amdgcn_rsqf(var + 1e-5f), mr = -mean * rstd;
  unsigned o[12];
#pragma unroll
  for (int t = 0; t < 6; ++t) {
    float y0 = fmaxf(fmaf(rstd, acc[t][0], fmaf(mr, cs[t][0], bflo(bb1[t].x))), 0.f), y1 = fmaxf(fmaf(rstd, acc[t][1], fmaf(mr, cs[t][1], bfhi(bb1[t].x))), 0.f);
    float y2 = fmaxf(fmaf(rstd, acc[t][2], fmaf(mr, cs[t][2], bflo(bb1[t].y))), 0.f), y3 = fmaxf(fmaf(rstd, acc[t][3], fmaf(mr, cs[t][3], bfhi(bb1[t].y))), 0.f);
    o[2 * t] = pk2(y0 * y0, y1 * y1); o[2 * t + 1] = pk2(y2 * y2, y3 * y3);
  }
#pragma unroll
  for (int q = 0; q < 3; ++q) st16(ws, v4u{o[4 * q], o[4 * q + 1], o[4 * q + 2], o[4 * q + 3]}, l16, WS_H1 + (unsigned)blk * 49152 + (3 * jj + q) * 1024);
  STAMP(12);
}

#define P4D 8
DEVI void phase4(const Params& p, LAS char* lds, int lane, int w STAMP_ARG) {
  const int x8 = blockIdx.x & 7, r = blockIdx.x >> 3;
  const int ti = 2 * x8 + (r >> 4), hb = (r >> 3) & 1, j = r & 7;
  const v4u srd = mk_srd(p.ws, WS_END);
  const unsigned l16 = lane * 16;
  const int b = w & 3, kh = w >> 2, blk = 8 * ti + 4 * hb + b;
  rbar();
  STAMP(14);
  const unsigned ha = WS_H1 + (unsigned)blk * 49152 + kh * 24 * 1024;
  v4u a[24];
#pragma unroll
  for (int s = 0; s < P4D; ++s) aload(a[s], srd, l16, ha + s * 1024);
  f32x4 acc[5];
#pragma unroll
  for (int t = 0; t < 5; ++t) acc[t] = f32x4{0.f, 0.f, 0.f, 0.f};
  v4u B[2][5];
  LAS char* bb = lds + kh * 20480 + l16;
#define P4_READ(s) do { _Pragma("unroll") for (int t = 0; t < 5; ++t) B[(s) & 1][t] = ldsr16(bb + (((s) >> 2) % 3) * 40960 + ((s) & 3) * 5120 + t * 1024); } while (0)
  P4_READ(0);
  SCHED0();
#pragma unroll
  for (int s = 0; s < 24; ++s) {
    if ((s & 3) == 3 && s < 23) lbar();
    asm volatile("s_waitcnt vmcnt(%1)" : "+v"(a[s]) : "i"(s + P4D - 1 < 24 ? P4D - 1 : 23 - s) : "memory");
    if (s + P4D < 24) aload(a[s + P4D], srd, l16, ha + (s + P4D) * 1024);
    if (s + 1 < 24) P4_READ(s + 1);
#pragma unroll
    for (int t = 0; t < 5; ++t) acc[t] = mfma(B[s & 1][t], a[s], acc[t]);
#pragma unroll
    for (int t = 0; t < 5; ++t) { __builtin_amdgcn_sched_group_barrier(0x100, 1, 0); __builtin_amdgcn_sched_group_barrier(0x8, 1, 0); }
    SCHED0();
  }
#undef P4_READ
  STAMP(15);
  const int g = lane >> 4, c = lane & 15, row = 16 * b + c, mt0 = 3 * kh;
  LAS char* sx = lds + L_STASH + row * 160 + mt0 * 32 + g * 8;
  v2u hv[3], bv[3];
#pragma unroll
  for (int e = 0; e < 3; ++e) {
    const int ee = (e < 2 || kh == 0) ? e : 0;
    hv[e] = *(LAS v2u*)(sx + ee * 32);
    bv[e] = *(LAS v2u*)(lds + L_P4_B2 + (mt0 + ee) * 32 + g * 8);
  }
  f32x4 fin[3];
  exchange_halves(lds, acc, fin, lane, w);
#pragma unroll
  for (int e = 0; e < 3; ++e) {
    if (e < 2 || kh == 0)
      *(LAS v2u*)(sx + e * 32) = v2u{pk2(fin[e][0] + bflo(bv[e].x) + bflo(hv[e].x), fin[e][1] + bfhi(bv[e].x) + bfhi(hv[e].x)),
                                     pk2(fin[e][2] + bflo(bv[e].y) + bflo(hv[e].y), fin[e][3] + bfhi(bv[e].y) + bfhi(hv[e].y))};
  }
  lbar();
  {
    const int q0 = w * 64 + lane, q1 = q0 + 512;
    const v4u o0 = *(LAS v4u*)(lds + L_STASH + q0 * 16);
    v4u o1 = o0; if (w < 2) o1 = *(LAS v4u*)(lds + L_STASH + q1 * 16);
    const int r0 = q0 / 10, r1 = q1 / 10;
    rsrc_t outr = mk_rsrc(p.out, T_ * 1280);
    const unsigned ob = (unsigned)(128 * ti + 64 * hb) * 1280 + 160 * j;
    __builtin_amdgcn_raw_buffer_store_b128(o0, outr, ob + r0 * 1280 + (q0 - r0 * 10) * 16, 0, 0);
    if (w < 2) __builtin_amdgcn_raw_buffer_store_b128(o1, outr, ob + r1 * 1280 + (q1 - r1 * 10) * 16, 0, 0);
  }
  STAMP(16);
}

DEVI void loader(const Params& p, LAS char* lds, int lane, int lw, u64 tag STAMP_ARG) {
  const int x8 = blockIdx.x & 7, r = blockIdx.x >> 3;
  const int t2d = 4 * (x8 >> 1) + (r >> 3), c2d = 8 * (x8 & 1) + (r & 7);
  const int tl = 2 * x8 + (r >> 4), hb = (r >> 3) & 1, j = r & 7;
  rsrc_t ws = mk_rsrc(p.ws, WS_END);
  const v4u srd = mk_srd(p.ws, WS_END);
  const unsigned l16 = lane * 16;
  {
    if (lw == 0) {
      __builtin_amdgcn_s_sleep(P0_POLL_DELAY);
      int bq = (t2d > 0 ? 8 * t2d - 1 : 0) + (lane >> 2); if (t2d == 0 && lane >= 32) bq = 7;
      wait_flags2(p.ws, WS_F0X + (bq * 4 + (lane & 3)) * 8, lane < 36, WS_F0Q + (c2d * 64 + lane) * 8, lane < 40, tag);
      if (lane == 0) __hip_atomic_store((LAS unsigned*)(lds + L_GO), 1u, __ATOMIC_RELAXED, __HIP_MEMORY_SCOPE_WORKGROUP);
    } else {
      for (unsigned sp = 0; sp < (1u << 22); ++sp) {
        if (__builtin_amdgcn_readfirstlane(__hip_atomic_load((LAS unsigned*)(lds + L_GO), __ATOMIC_RELAXED, __HIP_MEMORY_SCOPE_WORKGROUP))) break;
        __builtin_amdgcn_s_sleep(1);
      }
      asm volatile("" ::: "memory");
    }
  }
  STAMP(2);
  unsigned sb[9], si[9];
#pragma unroll
  for (int i = 0; i < 9; ++i) {
    const int n = (lw + 4 * i) < 34 ? (lw + 4 * i) : 33, e = n >= 17 ? 1 : 0, f = n - 17 * e;
    int blk = 8 * t2d - 1 + f; if (blk < 0) blk = 0;
    sb[i] = f < 9 ? WS_XN + (unsigned)blk * 20480 + e * 1024 : WS_WQ + (unsigned)c2d * 163840 + (e * 8 + (f - 9)) * 1024;
    si[i] = f < 9 ? 2048u : 16384u;
  }
#define P1_STG(s) do { if (lw < 2) dma16<16>(ws, lds + ((s) & 3) * P1_STAGE + (lw + 32) * 1024, l16, sb[8] + (s) * si[8]); \
    _Pragma("unroll") for (int i = 0; i < 8; ++i) dma16<16>(ws, lds + ((s) & 3) * P1_STAGE + (lw + 4 * i) * 1024, l16, sb[i] + (s) * si[i]); } while (0)
  P1_STG(0); P1_STG(1);
  WAITV(8); rbar();
  P1_STG(2); P1_STG(3);
  v2u f1, f2;
  const unsigned fo1 = lane < 20 ? WS_F0O + (j * 32 + lane) * 8 : WS_F0A + (c2d * 32 + (lane < 40 ? lane - 20 : 0)) * 8;
  const unsigned fo2 = WS_F0B + (j * 64 + (lane < 48 ? lane : 0)) * 8;
  const unsigned wo = WS_WO + (unsigned)j * 102400, w1 = WS_W1 + (unsigned)c2d * 122880, w2 = WS_W2 + (unsigned)j * 245760;
#pragma unroll
  for (int q = 1; q <= 9; ++q) {
    if (q <= 8) WAITV(16); else WAITV(8);
    if (q == 7) asm volatile("" : "+v"(f1), "+v"(f2));
    rbar();
    if (q + 3 <= 9) P1_STG(q + 3);
    if (q == 4) {
      asm volatile("buffer_load_dwordx2 %0, %1, %2, 0 offen sc1" : "=v"(f1) : "v"(fo1), "s"(srd) : "memory");
      asm volatile("buffer_load_dwordx2 %0, %1, %2, 0 offen sc1" : "=v"(f2) : "v"(fo2), "s"(srd) : "memory");
    }
    if (q == 7) {
      const bool ok = (((u64)f1.y << 32) | f1.x) == tag && (((u64)f2.y << 32) | f2.x) == tag;
      if (!__all(ok)) wait_flags2(p.ws, fo1, true, fo2, true, tag);
#pragma unroll
      for (int i = 0; i < 8; ++i) { const int f = 68 + lw + 4 * i; dma16<16>(ws, lds + f * 1024, l16, wo + f * 1024); }
    }
  }
#undef P1_STG
  STAMP(3);
  rbar();
#pragma unroll
  for (int i = 0; i < 17; ++i) { const int f = lw + 4 * i; dma16<16>(ws, lds + f * 1024, l16, wo + f * 1024); }
  {
    rsrc_t xs = mk_rsrc(p.x, T_ * 1280);
#pragma unroll
    for (int m = 0; m < 3; ++m) {
      const int n = lw + 4 * m;
      if (n < 10) { const int q = n * 64 + lane, row = q / 10, ch = q - row * 10;
        dma16<0>(xs, lds + L_STASH + n * 1024, (unsigned)(128 * tl + 64 * hb + row) * 1280 + 160 * j + 16 * ch, 0); }
    }
  }
  STAMP(4);
  rbar();
  STAMP(5);
  if (lw == 0) wait_flags(p.ws, WS_F1 + (tl * 16 + lane) * 8, lane < 16, tag);
  WAITV(0); rbar();
  STAMP(6);
  if (lw == 0) { rsrc_t bs = mk_rsrc(p.b1, F_ * 2); dma16<0>(bs, lds + L_P3_B1, lane < 12 ? 192 * c2d + lane * 16 : 0x7fffffffu, 0); }
#pragma unroll
  for (int i = 0; i < 5; ++i) { const int f = 100 + lw + 4 * i; dma16<16>(ws, lds + f * 1024, l16, w1 + f * 1024); }
  rbar();
#pragma unroll
  for (int i = 0; i < 25; ++i) { const int f = lw + 4 * i; dma16<16>(ws, lds + f * 1024, l16, w1 + f * 1024); }
  STAMP(8);
  rbar();
  STAMP(9);
  WAITV(0); rbar();
  if (lw == 0) wait_flags(p.ws, WS_F2 + (t2d * 16 + lane) * 8, lane < 16, tag);
  rbar();
  STAMP(10);
  rbar();
  STAMP(11);
  if (lw == 0) { rsrc_t bs = mk_rsrc(p.b2, D_ * 2); dma16<0>(bs, lds + L_P4_B2, lane < 10 ? 160 * j + lane * 16 : 0x7fffffffu, 0); }
#pragma unroll
  for (int i = 0; i < 30; ++i) { const int f = lw + 4 * i; dma16<16>(ws, lds + f * 1024, l16, w2 + f * 1024); }
  STAMP(12);
  rbar();
  STAMP(13);
  if (lw == 0) wait_flags(p.ws, WS_F3 + (tl * 16 + lane) * 8, lane < 16, tag);
  WAITV(0); rbar();
  STAMP(14);
#define P4_STG(st) do { _Pragma("unroll") for (int i = 0; i < 10; ++i) { const int f = lw + 4 * i; \
    dma16<16>(ws, lds + ((st) % 3) * 40960 + f * 1024, l16, w2 + (st) * 40960 + f * 1024); } } while (0)
  rbar(); P4_STG(3);
  rbar(); P4_STG(4);
  WAITV(10); rbar(); P4_STG(5);
  if (blockIdx.x == 0 && lw == 1) {
    for (int q = 0; q < 4; ++q) wait_flags(p.ws, WS_F1 + (q * 64 + lane) * 8, true, tag);
    if (lane == 0) publish(p.ws, WS_NONCE, tag);
  }
  WAITV(10); rbar();
  WAITV(0); rbar();
#undef P4_STG
  STAMP(15);
  rbar();
  rbar();
  STAMP(16);
}

DEVI void wg_publish(const Params& p, unsigned off, u64 tag) {
  WAITV(0); lbar();
  if (threadIdx.x == 448) publish(p.ws, off, tag);
}

__global__ __launch_bounds__(768, 3) __attribute__((amdgpu_waves_per_eu(3, 3))) void fused_block(Params p) {
  extern __shared__ __attribute__((aligned(16))) char smem[];
  LAS char* lds = (LAS char*)smem;
  const int lane = threadIdx.x & 63, w = __builtin_amdgcn_readfirstlane(threadIdx.x >> 6);
  const int x8 = blockIdx.x & 7, r = blockIdx.x >> 3;
  u64 tag = 0;
  STAMP(0);
  const u64 tstart = __builtin_amdgcn_s_memrealtime();
  const unsigned pending = phase0(p, lds, lane, w, tag, true STAMP_PASS);
  STAMP(1);
  if (w >= 8) loader(p, lds, lane, w - 8, tag STAMP_PASS);
  else {
    const int t2d = 4 * (x8 >> 1) + (r >> 3), c2d = 8 * (x8 & 1) + (r & 7), tl = 2 * x8 + (r >> 4);
    phase1(p, lds, lane, w, tag, pending STAMP_PASS);
    wg_publish(p, WS_F1 + (t2d * 16 + c2d) * 8, tag);
    STAMP(5);
    phase2(p, lds, lane, w STAMP_PASS);
    wg_publish(p, WS_F2 + (tl * 16 + (r & 15)) * 8, tag);
    STAMP(9);
    phase3(p, lds, lane, w STAMP_PASS);
    wg_publish(p, WS_F3 + (t2d * 16 + c2d) * 8, tag);
    STAMP(13);
    phase4(p, lds, lane, w STAMP_PASS);
  }
  while (__builtin_amdgcn_s_memrealtime() - tstart < (u64)(PADTO) * 100 + (((PADMASK >> (blockIdx.x & 7)) & 1) ? 300 : 0)) __builtin_amdgcn_s_sleep(1);
}

extern "C" void kernel_launch(void* const* d_in, const int* in_sizes, int n_in, void* d_out, int out_size, void* d_ws, size_t ws_size, hipStream_t stream) {
  Params p;
  p.x = (const unsigned short*)d_in[0]; p.wqkv = (const unsigned short*)d_in[1]; p.wo = (const unsigned short*)d_in[2];
  p.w1 = (const unsigned short*)d_in[4]; p.b1 = (const unsigned short*)d_in[5]; p.w2 = (const unsigned short*)d_in[6]; p.b2 = (const unsigned short*)d_in[7];
  p.out = (unsigned short*)d_out; p.ws = (unsigned char*)d_ws;
  hipFuncSetAttribute((const void*)fused_block, hipFuncAttributeMaxDynamicSharedMemorySize, LDS_BYTES);
  hipLaunchKernelGGL(fused_block, dim3(256), dim3(768), LDS_BYTES, stream, p);
}
```

```cpp
#include <hip/hip_runtime.h>
#include <hip/hip_bf16.h>
#include <stdint.h>

#define GAS __attribute__((address_space(1)))
#define LAS __attribute__((address_space(3)))
#define DEVI __device__ __forceinline__
typedef unsigned v4u __attribute__((ext_vector_type(4)));
typedef unsigned v2u __attribute__((ext_vector_type(2)));
typedef float f32x4 __attribute__((ext_vector_type(4)));
typedef short bf16x8 __attribute__((ext_vector_type(8)));
typedef short s16x4 __attribute__((ext_vector_type(4)));
typedef __bf16 bf16x2 __attribute__((ext_vector_type(2)));
typedef unsigned long long u64;
typedef GAS u64 gu64;
#define RLX_AGENT __ATOMIC_RELAXED, __HIP_MEMORY_SCOPE_AGENT

#define NLAUNCH 1
#define OUT_SC1 0
#define WS_AT_END 0
#define TOUCH 0
#define P1_SAME 0
#define POLL_SLEEP 4
#define P0_POLL_DELAY 64
#define CPRIO 2
#define DIAG 0

constexpr int T_ = 2048, D_ = 640, F_ = 1536;
constexpr unsigned WS_NONCE = 0;
constexpr unsigned WS_F0Q = 40960;
constexpr unsigned WS_F0O = 8192;
constexpr unsigned WS_F0A = 12288;
constexpr unsigned WS_F0B = 16384;
constexpr unsigned WS_F0X = 20480;
constexpr unsigned WS_F1 = 24576;
constexpr unsigned WS_F2 = 28672;
constexpr unsigned WS_F3 = 32768;
constexpr unsigned WS_XN = 131072;
constexpr unsigned WS_WQ = WS_XN + 128 * 20480;
constexpr unsigned WS_WO = WS_WQ + 16 * 20 * 8 * 1024;
constexpr unsigned WS_W1 = WS_WO + 8 * 20 * 5 * 1024;
constexpr unsigned WS_W2 = WS_W1 + 16 * 20 * 6 * 1024;
constexpr unsigned WS_O = WS_W2 + 8 * 48 * 5 * 1024;
constexpr unsigned WS_H = WS_O + 128 * 20480;
constexpr unsigned WS_H1 = WS_H + 128 * 20480;
constexpr unsigned WS_ST = WS_H1 + 128 * 49152;
constexpr unsigned WS_END = WS_ST + 128 * 2048;
constexpr unsigned LDS_BYTES = 163840;
constexpr unsigned L_STASH = 153600;
constexpr unsigned L_P0 = 104448;
constexpr unsigned L_TAG = 153600;
constexpr unsigned L_GO = 153616;
#define SHADOW_SLEEP 24
constexpr unsigned P1_STAGE = 34816;
constexpr unsigned L_P1_EX = 102400;
constexpr unsigned P1_RS = 264;
constexpr unsigned L_EXH = 124928;
constexpr unsigned L_P3_B1 = 122880;
constexpr unsigned L_P4_B2 = 145408;
constexpr unsigned L_CNT = 146432;

struct Params {
  const unsigned short* x; const unsigned short* wqkv; const unsigned short* wo; const unsigned short* w1; const unsigned short* b1;
  const unsigned short* w2; const unsigned short* b2; unsigned short* out; unsigned char* ws;
};

typedef __amdgpu_buffer_rsrc_t rsrc_t;
DEVI rsrc_t mk_rsrc(const void* p, unsigned bytes) { return __builtin_amdgcn_make_buffer_rsrc((void*)p, 0, bytes, 0x00020000); }
DEVI v4u mk_srd(const void* p, unsigned bytes) {
  u64 a = (u64)p; v4u r; r.x = (unsigned)a; r.y = (unsigned)(a >> 32) & 0xffffu; r.z = bytes; r.w = 0x00020000u; return r;
}
template <int AUX> DEVI void dma16(rsrc_t rs, LAS char* lds, unsigned voff, unsigned soff) {
  __builtin_amdgcn_raw_ptr_buffer_load_lds(rs, (LAS void*)lds, 16, voff, soff, 0, AUX);
}
DEVI void aload(v4u& d, v4u srd, unsigned voff, unsigned soff) {
  asm volatile("buffer_load_dwordx4 %0, %1, %2, %3 offen sc1" : "=v"(d) : "v"(voff), "s"(srd), "s"(soff) : "memory");
}
DEVI void st16(rsrc_t rs, v4u v, unsigned voff, unsigned soff) { __builtin_amdgcn_raw_buffer_store_b128(v, rs, voff + soff, 0, 16); }
#define WAITV(n) asm volatile("s_waitcnt vmcnt(%0)" ::"i"(n) : "memory")
#define WAITV1(n, a) asm volatile("s_waitcnt vmcnt(%1)" : "+v"(a) : "i"(n) : "memory")
#define WAITV2(n, a, b) asm volatile("s_waitcnt vmcnt(%2)" : "+v"(a), "+v"(b) : "i"(n) : "memory")
#define WAITV4(n, a, b, c, d) asm volatile("s_waitcnt vmcnt(%4)" : "+v"(a), "+v"(b), "+v"(c), "+v"(d) : "i"(n) : "memory")
#define WAITL0() asm volatile("s_waitcnt lgkmcnt(0)" ::: "memory")
#define BAR() __builtin_amdgcn_s_barrier()
#define SCHED0() __builtin_amdgcn_sched_barrier(0)
DEVI void lbar() { WAITL0(); BAR(); asm volatile("" ::: "memory"); }
DEVI void rbar() { asm volatile("" ::: "memory"); BAR(); asm volatile("" ::: "memory"); }

DEVI unsigned pk2(float a, float b) { bf16x2 p; p[0] = (__bf16)a; p[1] = (__bf16)b; return __builtin_bit_cast(unsigned, p); }
DEVI float bflo(unsigned u) { return __uint_as_float(u << 16); }
DEVI float bfhi(unsigned u) { return __uint_as_float(u & 0xffff0000u); }
DEVI bf16x8 asbf(v4u v) { return __builtin_bit_cast(bf16x8, v); }
DEVI f32x4 mfma(v4u a, v4u b, f32x4 c) { return __builtin_amdgcn_mfma_f32_16x16x32_bf16(asbf(a), asbf(b), c, 0, 0, 0); }
DEVI v4u ldsr16(LAS char* p) { return *(LAS v4u*)p; }
DEVI float fsel(bool c, float a, float b) { asm("" : "+v"(a)); asm("" : "+v"(b)); return c ? a : b; }
DEVI float xrow_max(float v) {
  unsigned u = __float_as_uint(v);
  auto a = __builtin_amdgcn_permlane32_swap(u, u, false, false); v = fmaxf(__uint_as_float(a[0]), __uint_as_float(a[1]));
  u = __float_as_uint(v);
  auto b = __builtin_amdgcn_permlane16_swap(u, u, false, false); return fmaxf(__uint_as_float(b[0]), __uint_as_float(b[1]));
}
DEVI float xrow_sum(float v) {
  unsigned u = __float_as_uint(v);
  auto a = __builtin_amdgcn_permlane32_swap(u, u, false, false); v = __uint_as_float(a[0]) + __uint_as_float(a[1]);
  u = __float_as_uint(v);
  auto b = __builtin_amdgcn_permlane16_swap(u, u, false, false); return __uint_as_float(b[0]) + __uint_as_float(b[1]);
}
DEVI float row16_sum(float v) {
  v += __uint_as_float(__builtin_amdgcn_update_dpp(0u, __float_as_uint(v), 0x128, 0xf, 0xf, false));
  v += __uint_as_float(__builtin_amdgcn_update_dpp(0u, __float_as_uint(v), 0x124, 0xf, 0xf, false));
  v += __uint_as_float(__builtin_amdgcn_update_dpp(0u, __float_as_uint(v), 0x122, 0xf, 0xf, false));
  v += __uint_as_float(__builtin_amdgcn_update_dpp(0u, __float_as_uint(v), 0x121, 0xf, 0xf, false));
  return v;
}

DEVI u64 rfl64(u64 v) { return ((u64)__builtin_amdgcn_readfirstlane((unsigned)(v >> 32)) << 32) | (unsigned)__builtin_amdgcn_readfirstlane((unsigned)v); }
DEVI void publish(unsigned char* ws, unsigned off, u64 tag) { __hip_atomic_store((gu64*)(ws + off), tag, RLX_AGENT); }
DEVI void wait_flags(unsigned char* ws, unsigned laneoff, bool active, u64 tag) {
  for (unsigned sp = 0; sp < (1u << 18); ++sp) {
    u64 v = tag;
    if (active) v = __hip_atomic_load((gu64*)(ws + laneoff), RLX_AGENT);
    if (__all(v == tag)) break;
    __builtin_amdgcn_s_sleep(POLL_SLEEP);
  }
  __builtin_amdgcn_fence(__ATOMIC_ACQUIRE, "wavefront");
  asm volatile("" ::: "memory");
}

DEVI void wait_flags2(unsigned char* ws, unsigned off1, bool act1, unsigned off2, bool act2, u64 tag) {
  for (unsigned sp = 0; sp < (1u << 18); ++sp) {
    u64 v1 = tag, v2 = tag;
    if (act1) v1 = __hip_atomic_load((gu64*)(ws + off1), RLX_AGENT);
    if (act2) v2 = __hip_atomic_load((gu64*)(ws + off2), RLX_AGENT);
    if (__all(v1 == tag && v2 == tag)) break;
    __builtin_amdgcn_s_sleep(POLL_SLEEP);
  }
  __builtin_amdgcn_fence(__ATOMIC_ACQUIRE, "wavefront");
  asm volatile("" ::: "memory");
}

#define STAMP_ARG
#define STAMP_PASS
#define STAMP(id) do {} while (0)

template <int KIND>
DEVI unsigned retile_unit(const Params& p, rsrc_t ws, LAS char* st, int lane, int a, int ks STAMP_ARG) {
  constexpr int NCH = KIND == 2 ? 12 : 10;
  constexpr int NI = 32 * NCH / 64;
  constexpr int NT = KIND == 2 ? 6 : 5;
  constexpr unsigned RS = KIND == 2 ? 3072u : 1280u;
  const void* src = KIND == 1 ? (const void*)p.wo : KIND == 2 ? (const void*)p.w1 : (const void*)p.w2;
  constexpr unsigned SB = KIND == 1 ? 640u * 1280u : KIND == 2 ? 640u * 3072u : 1536u * 1280u;
  rsrc_t rs = mk_rsrc(src, SB);
#pragma unroll
  for (int n = 0; n < NI; ++n) {
    int q = n * 64 + lane;
    int rho = q / NCH, ch = q - rho * NCH;
    int row; unsigned col;
    const int g = rho >> 3, t = (rho >> 2) & 1, e = rho & 3;
    if (KIND == 1) { row = ks < 16 ? (2 * ks + t) * 20 + 4 * g + e : (2 * (4 * (ks - 16) + g) + t) * 20 + 16 + e; col = 160 * a + 16 * ch; }
    else if (KIND == 2) { row = 32 * ks + rho; col = 192 * a + 16 * ch; }
    else { row = 32 * ks + 16 * t + 4 * g + e; col = 160 * a + 16 * ch; }
    dma16<0>(rs, st + n * 1024, (unsigned)row * RS + col, 0);
  }
  WAITV(0);
  STAMP(18);
  unsigned dst;
  if (KIND == 1) dst = WS_WO + (unsigned)(a * 20 + ks) * 5120;
  else if (KIND == 2) dst = WS_W1 + (unsigned)(a * 20 + ks) * 6144;
  else { int kh = ks / 24, s = ks - kh * 24; dst = WS_W2 + (unsigned)a * 245760 + (s >> 2) * 40960 + kh * 20480 + (s & 3) * 5120; }
  const int g = lane >> 4, q4 = (lane & 15) >> 2, p4 = lane & 3;
  constexpr int RB = NCH * 16;
  LAS char* base = st + (8 * g + q4) * RB + 8 * p4;
#pragma unroll
  for (int t = 0; t < NT; ++t) {
    s16x4 lo = __builtin_amdgcn_ds_read_tr16_b64_v4i16((LAS s16x4*)(base + 32 * t));
    s16x4 hi = __builtin_amdgcn_ds_read_tr16_b64_v4i16((LAS s16x4*)(base + 4 * RB + 32 * t));
    v2u l2 = __builtin_bit_cast(v2u, lo), h2 = __builtin_bit_cast(v2u, hi);
    st16(ws, v4u{l2.x, l2.y, h2.x, h2.y}, lane * 16, dst + t * 1024);
  }
  STAMP(19);
  return KIND == 1 ? WS_F0O + (a * 32 + ks) * 8 : KIND == 2 ? WS_F0A + (a * 32 + ks) * 8 : WS_F0B + (a * 64 + ks) * 8;
}

DEVI void wqkv_unit(const Params& p, rsrc_t ws, LAS char* lds, int lane, int w, int u, u64 nonce, u64& tag, bool flags STAMP_ARG) {
  const int ks = u >> 2, hf = (u >> 1) & 1, rh = u & 1;
  rsrc_t rs = mk_rsrc(p.wqkv, 640u * 3840u);
#pragma unroll
  for (int m = 0; m < 4; ++m) {
    const int n = w + 8 * m;
    if (n < 30) {
      const int q = n * 64 + lane, row = q / 120, ch = q - row * 120, sec = ch / 40;
      dma16<0>(rs, lds + L_P0 + n * 1024, (unsigned)(32 * ks + 16 * rh + row) * 3840u + sec * 1280 + 640 * hf + 16 * (ch - sec * 40), 0);
    }
  }
  STAMP(17);
  WAITV(0);
  if (flags && w == 0) { asm volatile("" : "+v"(nonce)); if (lane == 0) *(LAS u64*)(lds + L_TAG) = nonce * 6364136223846793005ull + 1442695040888963407ull; }
  lbar();
  STAMP(18);
  if (flags) tag = rfl64(*(LAS u64*)(lds + L_TAG));
  const int gl = (lane >> 4) & 1, th = lane >> 5, q4 = (lane & 15) >> 2, p4 = lane & 3;
  LAS char* base = lds + L_P0 + (8 * gl + q4) * 1920 + 80 * w;
  const unsigned dst = WS_WQ + (unsigned)((8 * hf + w) * 20 + ks) * 8192;
  const unsigned vo = th * 1024 + (2 * rh + gl) * 256 + (lane & 15) * 16;
#pragma unroll
  for (int m = 0; m < 4; ++m) {
    int cb;
    if (m == 0) cb = 640 + 40 * th + 8 * p4;
    else if (m == 1) cb = th == 0 ? (p4 < 2 ? 640 : 0) + 32 + 40 * (p4 & 1) : 8 * p4;
    else if (m == 2) cb = th == 0 ? 40 + 8 * p4 : 1280 + 8 * p4;
    else cb = th == 0 ? 1280 + 40 + 8 * p4 : 1280 + 32 + 40 * (p4 & 1);
    s16x4 lo = __builtin_amdgcn_ds_read_tr16_b64_v4i16((LAS s16x4*)(base + cb));
    s16x4 hi = __builtin_amdgcn_ds_read_tr16_b64_v4i16((LAS s16x4*)(base + 4 * 1920 + cb));
    v2u l2 = __builtin_bit_cast(v2u, lo), h2 = __builtin_bit_cast(v2u, hi);
    v4u v = {l2.x, l2.y, h2.x, h2.y};
    if (m == 3 && th == 1 && (lane & 15) >= 8) v = v4u{0u, 0u, 0u, 0u};
    st16(ws, v, vo, dst + m * 2048);
  }
  STAMP(19);
  if (flags) { WAITV(0); STAMP(20); if (lane == 0) publish(p.ws, WS_F0Q + ((8 * hf + w) * 64 + ks * 2 + rh) * 8, tag); }
}

DEVI void ln_unit(const Params& p, rsrc_t ws, LAS char* lds, int lane, int w, int tb, u64 nonce, u64& tag, bool flags STAMP_ARG) {
  const int t = lane >> 4, i = lane & 15, c = 4 * w + t;
  const GAS v4u* src = (const GAS v4u*)((const GAS char*)p.x + (size_t)(tb * 16 + c) * 1280) + i;
  v4u r[5];
#pragma unroll
  for (int n = 0; n < 5; ++n) r[n] = src[16 * n];
  STAMP(17);
  float v[40];
#pragma unroll
  for (int n = 0; n < 5; ++n)
#pragma unroll
    for (int e = 0; e < 4; ++e) { v[n * 8 + 2 * e] = bflo(r[n][e]); v[n * 8 + 2 * e + 1] = bfhi(r[n][e]); }
  float s4[4] = {0.f, 0.f, 0.f, 0.f};
#pragma unroll
  for (int e = 0; e < 40; ++e) s4[e & 3] += v[e];
  const float mean = row16_sum((s4[0] + s4[1]) + (s4[2] + s4[3])) * (1.f / 640.f);
  float q4[4] = {0.f, 0.f, 0.f, 0.f};
#pragma unroll
  for (int e = 0; e < 40; ++e) { v[e] -= mean; q4[e & 3] = fmaf(v[e], v[e], q4[e & 3]); }
  const float rstd = __builtin_amdgcn_rsqf(row16_sum((q4[0] + q4[1]) + (q4[2] + q4[3])) * (1.f / 640.f) + 1e-5f);
  LAS char* st = lds + L_P0;
#pragma unroll
  for (int n = 0; n < 5; ++n) {
    v4u o;
#pragma unroll
    for (int e = 0; e < 4; ++e) o[e] = pk2(v[n * 8 + 2 * e] * rstd, v[n * 8 + 2 * e + 1] * rstd);
    *(LAS v4u*)(st + (i + 16 * n) * 272 + c * 16) = o;
  }
  if (flags && w == 0) { if (lane == 0) *(LAS u64*)(lds + L_TAG) = nonce * 6364136223846793005ull + 1442695040888963407ull; }
  STAMP(18);
  lbar();
  if (flags) tag = rfl64(*(LAS u64*)(lds + L_TAG));
#pragma unroll
  for (int n = 0; n < 5; ++n) {
    const int ks = w * 5 + n;
    v4u o = ldsr16(st + (4 * ks + (lane >> 4)) * 272 + (lane & 15) * 16);
    st16(ws, o, lane * 16, WS_XN + (unsigned)tb * 20480 + ks * 1024);
  }
  STAMP(19);
  if (flags) { WAITV(0); STAMP(20); if (lane == 0) publish(p.ws, WS_F0X + (tb * 4 + w) * 8, tag); }
}

DEVI unsigned phase0(const Params& p, LAS char* lds, int lane, int w, u64& tag, bool flags STAMP_ARG) {
  rsrc_t ws = mk_rsrc(p.ws, WS_END);
  const int cu = blockIdx.x;
  u64 nonce = 0;
  if (flags && w == 0) nonce = __hip_atomic_load((gu64*)(p.ws + WS_NONCE), RLX_AGENT);
  if (w >= 8) { if (w == 8 && lane == 0) *(LAS unsigned*)(lds + L_GO) = 0u; lbar(); tag = rfl64(*(LAS u64*)(lds + L_TAG)); return 0; }
  if (cu < 128 && w < 4) { ln_unit(p, ws, lds, lane, w, cu, nonce, tag, flags STAMP_PASS); return 0; }
  if (cu >= 128 && cu < 208) { wqkv_unit(p, ws, lds, lane, w, cu - 128, nonce, tag, flags STAMP_PASS); return 0; }
  int slot;
  if (cu < 128) slot = cu * 4 + (w - 4);
  else {
    slot = 512 + (cu - 208) * 8 + w;
    if (flags && w == 0 && lane == 0) *(LAS u64*)(lds + L_TAG) = nonce * 6364136223846793005ull + 1442695040888963407ull;
  }
  lbar();
  if (flags) tag = rfl64(*(LAS u64*)(lds + L_TAG));
  if (cu >= 208) __builtin_amdgcn_s_sleep(SHADOW_SLEEP);
  STAMP(17);
  LAS char* st = lds + L_P0 + (cu < 128 ? 21760 + (w - 4) * 6144 : w * 6144);
  if (slot < 160) return retile_unit<1>(p, ws, st, lane, slot & 7, slot >> 3 STAMP_PASS);
  if (slot < 480) { int u = slot - 160; return retile_unit<2>(p, ws, st, lane, u & 15, u >> 4 STAMP_PASS); }
  if (slot < 864) { int u = slot - 480; return retile_unit<3>(p, ws, st, lane, u & 7, u >> 3 STAMP_PASS); }
  return 0;
}

template <int NB>
DEVI void p1_loop(LAS char* lds, unsigned t0o, unsigned t1o, unsigned xo, f32x4 (&acc)[2][5]) {
  v4u W[2][2], X[2][NB];
#define P1_RO(ks) ((((ks) >> 1) & 3) * P1_STAGE + ((ks) & 1) * 17408)
#define P1_READ(ks) do { W[(ks) & 1][0] = ldsr16(lds + P1_RO(ks) + t0o); W[(ks) & 1][1] = ldsr16(lds + P1_RO(ks) + t1o); \
    _Pragma("unroll") for (int b = 0; b < NB; ++b) X[(ks) & 1][b] = ldsr16(lds + P1_RO(ks) + xo + b * 1024); } while (0)
  rbar();
  P1_READ(0);
  SCHED0();
#pragma unroll
  for (int ks = 0; ks < 20; ++ks) {
    if ((ks & 1) && ks < 19) lbar();
    if (ks + 1 < 20) P1_READ(ks + 1);
#pragma unroll
    for (int b = 0; b < NB; ++b) { acc[0][b] = mfma(W[ks & 1][0], X[ks & 1][b], acc[0][b]); acc[1][b] = mfma(W[ks & 1][1], X[ks & 1][b], acc[1][b]); }
    if (ks & 1) {
      __builtin_amdgcn_sched_group_barrier(0x100, 2, 0);
#pragma unroll
      for (int b = 0; b < NB; ++b) { __builtin_amdgcn_sched_group_barrier(0x8, 2, 0); __builtin_amdgcn_sched_group_barrier(0x100, 1, 0); }
    } else {
      __builtin_amdgcn_sched_group_barrier(0x100, 2, 0); __builtin_amdgcn_sched_group_barrier(0x8, 1, 0);
      __builtin_amdgcn_sched_group_barrier(0x100, 2, 0); __builtin_amdgcn_sched_group_barrier(0x8, 1, 0);
      __builtin_amdgcn_sched_group_barrier(0x100, 2, 0); __builtin_amdgcn_sched_group_barrier(0x8, 1, 0);
      __builtin_amdgcn_sched_group_barrier(0x100, NB - 4, 0); __builtin_amdgcn_sched_group_barrier(0x8, 2 * NB - 3, 0);
    }
    SCHED0();
  }
#undef P1_RO
#undef P1_READ
}

DEVI void phase1(const Params& p, LAS char* lds, int lane, int w, u64 tag, unsigned pending STAMP_ARG) {
  const int x8 = blockIdx.x & 7, r = blockIdx.x >> 3;
  const int ti = 4 * (x8 >> 1) + (r >> 3), pr = 8 * (x8 & 1) + (r & 7);
  rsrc_t ws = mk_rsrc(p.ws, WS_END);
  const unsigned l16 = lane * 16;
  if (pending) { WAITV(0); if (lane == 0) publish(p.ws, pending, tag); }
  STAMP(2);
  const int np = w & 3, rg = w >> 2;
  const int T0 = np == 0 ? 0 : np == 1 ? 2 : np == 2 ? 5 : 3, T1 = np == 0 ? 1 : np == 1 ? 7 : np == 2 ? 6 : 4;
  f32x4 acc[2][5];
#pragma unroll
  for (int e = 0; e < 2; ++e)
#pragma unroll
    for (int b = 0; b < 5; ++b) acc[e][b] = f32x4{0.f, 0.f, 0.f, 0.f};
  const unsigned t0o = (9 + T0) * 1024 + l16, t1o = (9 + T1) * 1024 + l16;
  if (rg == 0) p1_loop<5>(lds, t0o, t1o, l16, acc);
  else p1_loop<4>(lds, t0o, t1o, 5 * 1024 + l16, acc);
  STAMP(3);
  const int g = lane >> 4, c = lane & 15;
  {
    LAS char* d = lds + L_P1_EX + (rg * 80 + c) * P1_RS + g * 8;
#pragma unroll
    for (int b = 0; b < 5; ++b) {
      if (b < 4 || rg == 0) {
        *(LAS v2u*)(d + b * 16 * P1_RS + T0 * 32) = v2u{pk2(acc[0][b][0], acc[0][b][1]), pk2(acc[0][b][2], acc[0][b][3])};
        *(LAS v2u*)(d + b * 16 * P1_RS + T1 * 32) = v2u{pk2(acc[1][b][0], acc[1][b][1]), pk2(acc[1][b][2], acc[1][b][3])};
      }
    }
  }
  STAMP(22);
  lbar();
  STAMP(23);
  const int blk = 8 * ti + w;
  LAS char* own = lds + L_P1_EX + ((w + 1) * 16 + c) * P1_RS + g * 8;
  LAS char* trp = lds + L_P1_EX + ((w + 1) * 16 + 4 * g + (c >> 2)) * P1_RS + (c & 3) * 8;
  v2u ka[2], kp[2], qa[2], qr[2], va[2], vp[2];
  const v2u kr = *(LAS v2u*)(own + 64), krp = *(LAS v2u*)(own + 64 - 16 * P1_RS);
#pragma unroll
  for (int hh = 0; hh < 2; ++hh) {
    ka[hh] = *(LAS v2u*)(own + hh * 32); kp[hh] = *(LAS v2u*)(own + hh * 32 - 16 * P1_RS);
    qa[hh] = *(LAS v2u*)(own + (3 + hh) * 32);
    qr[hh] = *(LAS v2u*)(own - g * 8 + 64 + 16 + hh * 8);
    va[hh] = __builtin_bit_cast(v2u, __builtin_amdgcn_ds_read_tr16_b64_v4i16((LAS s16x4*)(trp + (5 + hh) * 32)));
    vp[hh] = __builtin_bit_cast(v2u, __builtin_amdgcn_ds_read_tr16_b64_v4i16((LAS s16x4*)(trp + (5 + hh) * 32 - 16 * P1_RS)));
  }
  const v2u vr = __builtin_bit_cast(v2u, __builtin_amdgcn_ds_read_tr16_b64_v4i16((LAS s16x4*)(trp + 7 * 32)));
  const v2u vrp = __builtin_bit_cast(v2u, __builtin_amdgcn_ds_read_tr16_b64_v4i16((LAS s16x4*)(trp + 7 * 32 - 16 * P1_RS)));
  STAMP(24);
  SCHED0();
  const int odd = c & 1, ce = c - odd;
  const bool havePrev = (ti > 0) || (w > 0);
  const bool v1a = havePrev && (4 * g >= ce + 2), v1b = havePrev && (4 * g >= ce), v2a = ce >= 4 * g, v2b = ce - 2 >= 4 * g;
  const float c2 = 0.22360679774997896f * 1.4426950408889634f, NEG = -3.0e38f;
  const f32x4 z = {0.f, 0.f, 0.f, 0.f};
  f32x4 S1[2], S2[2];
#pragma unroll
  for (int hh = 0; hh < 2; ++hh) {
    const bool mine = g == hh;
    const v4u Q = {qa[hh].x, qa[hh].y, mine ? qr[hh].x : 0u, mine ? qr[hh].y : 0u};
    S1[hh] = mfma(v4u{kp[hh].x, kp[hh].y, krp.x, krp.y}, Q, z);
    S2[hh] = mfma(v4u{ka[hh].x, ka[hh].y, kr.x, kr.y}, Q, z);
  }
  float s1a[2], s1b[2], s2a[2], s2b[2], m[2], inv[2];
#pragma unroll
  for (int hh = 0; hh < 2; ++hh) {
    s1a[hh] = fsel(v1a, fsel(odd, S1[hh][1], S1[hh][0]), NEG); s1b[hh] = fsel(v1b, fsel(odd, S1[hh][3], S1[hh][2]), NEG);
    s2a[hh] = fsel(v2a, fsel(odd, S2[hh][1], S2[hh][0]), NEG); s2b[hh] = fsel(v2b, fsel(odd, S2[hh][3], S2[hh][2]), NEG);
    m[hh] = fmaxf(fmaxf(s1a[hh], s1b[hh]), fmaxf(s2a[hh], s2b[hh]));
  }
  {
    auto a0 = __builtin_amdgcn_permlane32_swap(__float_as_uint(m[0]), __float_as_uint(m[0]), false, false);
    auto a1 = __builtin_amdgcn_permlane32_swap(__float_as_uint(m[1]), __float_as_uint(m[1]), false, false);
    m[0] = fmaxf(__uint_as_float(a0[0]), __uint_as_float(a0[1])); m[1] = fmaxf(__uint_as_float(a1[0]), __uint_as_float(a1[1]));
    auto b0 = __builtin_amdgcn_permlane16_swap(__float_as_uint(m[0]), __float_as_uint(m[0]), false, false);
    auto b1 = __builtin_amdgcn_permlane16_swap(__float_as_uint(m[1]), __float_as_uint(m[1]), false, false);
    m[0] = fmaxf(__uint_as_float(b0[0]), __uint_as_float(b0[1])); m[1] = fmaxf(__uint_as_float(b1[0]), __uint_as_float(b1[1]));
  }
#pragma unroll
  for (int hh = 0; hh < 2; ++hh) {
    s1a[hh] = __builtin_amdgcn_exp2f((s1a[hh] - m[hh]) * c2); s1b[hh] = __builtin_amdgcn_exp2f((s1b[hh] - m[hh]) * c2);
    s2a[hh] = __builtin_amdgcn_exp2f((s2a[hh] - m[hh]) * c2); s2b[hh] = __builtin_amdgcn_exp2f((s2b[hh] - m[hh]) * c2);
    inv[hh] = (s1a[hh] + s1b[hh]) + (s2a[hh] + s2b[hh]);
  }
  {
    auto a0 = __builtin_amdgcn_permlane32_swap(__float_as_uint(inv[0]), __float_as_uint(inv[0]), false, false);
    auto a1 = __builtin_amdgcn_permlane32_swap(__float_as_uint(inv[1]), __float_as_uint(inv[1]), false, false);
    inv[0] = __uint_as_float(a0[0]) + __uint_as_float(a0[1]); inv[1] = __uint_as_float(a1[0]) + __uint_as_float(a1[1]);
    auto b0 = __builtin_amdgcn_permlane16_swap(__float_as_uint(inv[0]), __float_as_uint(inv[0]), false, false);
    auto b1 = __builtin_amdgcn_permlane16_swap(__float_as_uint(inv[1]), __float_as_uint(inv[1]), false, false);
    inv[0] = __builtin_amdgcn_rcpf(__uint_as_float(b0[0]) + __uint_as_float(b0[1])); inv[1] = __builtin_amdgcn_rcpf(__uint_as_float(b1[0]) + __uint_as_float(b1[1]));
  }
  STAMP(25);
  f32x4 OA[2], OR[2];
  const unsigned sh = 16 * odd;
#pragma unroll
  for (int hh = 0; hh < 2; ++hh) {
    const v4u P = {pk2(s1a[hh] * inv[hh], 0.f) << sh, pk2(s1b[hh] * inv[hh], 0.f) << sh, pk2(s2a[hh] * inv[hh], 0.f) << sh, pk2(s2b[hh] * inv[hh], 0.f) << sh};
    OA[hh] = mfma(v4u{vp[hh].x, vp[hh].y, va[hh].x, va[hh].y}, P, z);
    OR[hh] = mfma(v4u{vrp.x, vrp.y, vr.x, vr.y}, P, z);
  }
  {
    v4u om = {pk2(OA[0][0], OA[0][1]), pk2(OA[0][2], OA[0][3]), pk2(OA[1][0], OA[1][1]), pk2(OA[1][2], OA[1][3])};
    st16(ws, om, l16, WS_O + (unsigned)blk * 20480 + pr * 1024);
    unsigned r0x = pk2(OR[0][0], OR[0][1]), r0y = pk2(OR[0][2], OR[0][3]), r1x = pk2(OR[1][0], OR[1][1]), r1y = pk2(OR[1][2], OR[1][3]);
    auto t0 = __builtin_amdgcn_permlane16_swap(r1x, r1x, false, false); auto t1 = __builtin_amdgcn_permlane16_swap(r1y, r1y, false, false);
    v4u orr = {r0x, r0y, t0[1], t1[1]};
    if (lane < 16) st16(ws, orr, l16, WS_O + (unsigned)blk * 20480 + (16 + (pr >> 2)) * 1024 + (pr & 3) * 256);
  }
  STAMP(4);
}

DEVI void exchange_halves(LAS char* lds, const f32x4 (&acc)[5], f32x4 (&fin)[3], int lane, int w) {
  LAS char* d = lds + L_EXH + (w & 3) * 5120 + lane * 16;
  if (w < 4) { *(LAS f32x4*)(d + 3072) = acc[3]; *(LAS f32x4*)(d + 4096) = acc[4]; }
  else { *(LAS f32x4*)(d) = acc[0]; *(LAS f32x4*)(d + 1024) = acc[1]; *(LAS f32x4*)(d + 2048) = acc[2]; }
  lbar();
  if (w < 4) { f32x4 u0 = *(LAS f32x4*)(d), u1 = *(LAS f32x4*)(d + 1024), u2 = *(LAS f32x4*)(d + 2048); fin[0] = acc[0] + u0; fin[1] = acc[1] + u1; fin[2] = acc[2] + u2; }
  else { f32x4 u0 = *(LAS f32x4*)(d + 3072), u1 = *(LAS f32x4*)(d + 4096); fin[0] = acc[3] + u0; fin[1] = acc[4] + u1; fin[2] = f32x4{0.f, 0.f, 0.f, 0.f}; }
}

DEVI void phase2(const Params& p, LAS char* lds, int lane, int w STAMP_ARG) {
  const int x8 = blockIdx.x & 7, r = blockIdx.x >> 3;
  const int ti = 2 * x8 + (r >> 4), hb = (r >> 3) & 1, j = r & 7;
  rsrc_t ws = mk_rsrc(p.ws, WS_END);
  const v4u srd = mk_srd(p.ws, WS_END);
  const unsigned l16 = lane * 16;
  const int b = w & 3, kh = w >> 2, blk = 8 * ti + 4 * hb + b;
  rbar();
  STAMP(6);
  v4u a[10];
  const unsigned oa = WS_O + (unsigned)blk * 20480 + 10 * kh * 1024;
#pragma unroll
  for (int s = 0; s < 10; ++s) aload(a[s], srd, l16, oa + s * 1024);
  f32x4 acc[5];
#pragma unroll
  for (int t = 0; t < 5; ++t) acc[t] = f32x4{0.f, 0.f, 0.f, 0.f};
  v4u B[2][5];
  LAS char* bb = lds + kh * 51200 + l16;
#pragma unroll
  for (int t = 0; t < 5; ++t) B[0][t] = ldsr16(bb + t * 1024);
  SCHED0();
#pragma unroll
  for (int s = 0; s < 10; ++s) {
    if (s == 5) lbar();
    asm volatile("s_waitcnt vmcnt(%1)" : "+v"(a[s]) : "i"(9 - s) : "memory");
    if (s + 1 < 10) {
#pragma unroll
      for (int t = 0; t < 5; ++t) B[(s + 1) & 1][t] = ldsr16(bb + (s + 1) * 5120 + t * 1024);
    }
#pragma unroll
    for (int t = 0; t < 5; ++t) acc[t] = mfma(B[s & 1][t], a[s], acc[t]);
#pragma unroll
    for (int t = 0; t < 5; ++t) { __builtin_amdgcn_sched_group_barrier(0x100, 1, 0); __builtin_amdgcn_sched_group_barrier(0x8, 1, 0); }
    SCHED0();
  }
  STAMP(7);
  const int g = lane >> 4, c = lane & 15, row = 16 * b + c, mt0 = 3 * kh;
  LAS char* sx = lds + L_STASH + row * 160 + mt0 * 32 + g * 8;
  v2u xv[3];
#pragma unroll
  for (int e = 0; e < 3; ++e) xv[e] = *(LAS v2u*)(sx + (e < 2 || kh == 0 ? e : 0) * 32);
  f32x4 fin[3];
  exchange_halves(lds, acc, fin, lane, w);
  v2u hv[3];
  float ssum = 0.f, ssq = 0.f;
#pragma unroll
  for (int e = 0; e < 3; ++e) {
    hv[e] = v2u{pk2(fin[e][0] + bflo(xv[e].x), fin[e][1] + bfhi(xv[e].x)), pk2(fin[e][2] + bflo(xv[e].y), fin[e][3] + bfhi(xv[e].y))};
    if (e < 2 || kh == 0) {
      *(LAS v2u*)(sx + e * 32) = hv[e];
      const float h0 = bflo(hv[e].x), h1 = bfhi(hv[e].x), h2 = bflo(hv[e].y), h3 = bfhi(hv[e].y);
      ssum += (h0 + h1) + (h2 + h3); ssq += fmaf(h0, h0, h1 * h1) + fmaf(h2, h2, h3 * h3);
    }
  }
  {
    auto sx0 = __builtin_amdgcn_permlane16_swap(hv[0].x, hv[1].x, false, false); auto sy0 = __builtin_amdgcn_permlane16_swap(hv[0].y, hv[1].y, false, false);
    const int G = 2 * (mt0 + (g & 1)) + (g >> 1);
    st16(ws, v4u{sx0[0], sy0[0], sx0[1], sy0[1]}, c * 16 + (10 * j + G) * 256, WS_H + (unsigned)blk * 20480);
    if (kh == 0) __builtin_amdgcn_raw_buffer_store_b64(hv[2], ws, WS_H + (unsigned)blk * 20480 + (10 * j + 4 + (g >> 1)) * 256 + c * 16 + (g & 1) * 8, 0, 16);
  }
  ssum = xrow_sum(ssum); ssq = xrow_sum(ssq);
  {
    const int idx = 2 * j + kh;
    const int e = idx & 3;
    if (lane < 16) {
      v2u sv = {__float_as_uint(ssum), __float_as_uint(ssq)};
      __builtin_amdgcn_raw_buffer_store_b64(sv, ws, WS_ST + (unsigned)blk * 2048 + (e >> 1) * 1024 + (idx >> 2) * 256 + c * 16 + (e & 1) * 8, 0, 16);
    }
  }
  STAMP(8);
}

#define P3D 8
DEVI void phase3(const Params& p, LAS char* lds, int lane, int w STAMP_ARG) {
  const int x8 = blockIdx.x & 7, r = blockIdx.x >> 3;
  const int ti = 4 * (x8 >> 1) + (r >> 3), jj = 8 * (x8 & 1) + (r & 7);
  rsrc_t ws = mk_rsrc(p.ws, WS_END);
  const v4u srd = mk_srd(p.ws, WS_END);
  const unsigned l16 = lane * 16;
  const int blk = 8 * ti + w;
  rbar();
  if (w < 6) {
    v4u cf[20];
#pragma unroll
    for (int ks = 0; ks < 20; ++ks) cf[ks] = ldsr16(lds + (ks * 6 + w) * 1024 + l16);
    SCHED0();
    f32x4 cs0 = {0.f, 0.f, 0.f, 0.f}, cs1 = cs0;
    const v4u ones = {0x3f803f80u, 0x3f803f80u, 0x3f803f80u, 0x3f803f80u};
#pragma unroll
    for (int ks = 0; ks < 20; ks += 2) { cs0 = mfma(cf[ks], ones, cs0); cs1 = mfma(cf[ks + 1], ones, cs1); }
    if ((lane & 15) == 0) *(LAS f32x4*)(lds + L_P3_B1 + 1024 + (w * 16 + (lane >> 4) * 4) * 4) = cs0 + cs1;
  }
  lbar();
  STAMP(10);
  v4u a[20], st0, st1;
  const unsigned ha = WS_H + (unsigned)blk * 20480;
#pragma unroll
  for (int s = 0; s < P3D; ++s) aload(a[s], srd, l16, ha + s * 1024);
  f32x4 acc[6];
#pragma unroll
  for (int t = 0; t < 6; ++t) acc[t] = f32x4{0.f, 0.f, 0.f, 0.f};
  v4u B[2][6];
  LAS char* bb = lds + l16;
#pragma unroll
  for (int t = 0; t < 6; ++t) B[0][t] = ldsr16(bb + t * 1024);
  SCHED0();
#pragma unroll
  for (int s = 0; s < 20; ++s) {
    if (s == 6 || s == 13) lbar();
    asm volatile("s_waitcnt vmcnt(%1)" : "+v"(a[s]) : "i"(s + P3D - 1 < 22 ? P3D - 1 : 21 - s) : "memory");
    if (s + P3D < 20) aload(a[s + P3D], srd, l16, ha + (s + P3D) * 1024);
    else if (s + P3D == 20) aload(st0, srd, l16, WS_ST + (unsigned)blk * 2048);
    else if (s + P3D == 21) aload(st1, srd, l16, WS_ST + (unsigned)blk * 2048 + 1024);
    if (s + 1 < 20) {
#pragma unroll
      for (int t = 0; t < 6; ++t) B[(s + 1) & 1][t] = ldsr16(bb + (s + 1) * 6144 + t * 1024);
    }
#pragma unroll
    for (int t = 0; t < 6; ++t) acc[t] = mfma(B[s & 1][t], a[s], acc[t]);
#pragma unroll
    for (int t = 0; t < 6; ++t) { __builtin_amdgcn_sched_group_barrier(0x100, 1, 0); __builtin_amdgcn_sched_group_barrier(0x8, 1, 0); }
    SCHED0();
  }
  lbar();
  STAMP(11);
  const int g = lane >> 4;
  f32x4 cs[6]; v2u bb1[6];
#pragma unroll
  for (int t = 0; t < 6; ++t) { cs[t] = *(LAS f32x4*)(lds + L_P3_B1 + 1024 + (t * 16 + g * 4) * 4); bb1[t] = *(LAS v2u*)(lds + L_P3_B1 + (t * 16 + g * 4) * 2); }
  WAITV2(0, st0, st1);
  SCHED0();
  float s1 = (__uint_as_float(st0[0]) + __uint_as_float(st0[2])) + (__uint_as_float(st1[0]) + __uint_as_float(st1[2]));
  float s2 = (__uint_as_float(st0[1]) + __uint_as_float(st0[3])) + (__uint_as_float(st1[1]) + __uint_as_float(st1[3]));
  s1 = xrow_sum(s1); s2 = xrow_sum(s2);
  const float mean = s1 * (1.f / 640.f);
  const float var = fmaxf(s2 * (1.f / 640.f) - mean * mean, 0.f);
  const float rstd = __builtin_amdgcn_rsqf(var + 1e-5f), mr = -mean * rstd;
  unsigned o[12];
#pragma unroll
  for (int t = 0; t < 6; ++t) {
    float y0 = fmaxf(fmaf(rstd, acc[t][0], fmaf(mr, cs[t][0], bflo(bb1[t].x))), 0.f), y1 = fmaxf(fmaf(rstd, acc[t][1], fmaf(mr, cs[t][1], bfhi(bb1[t].x))), 0.f);
    float y2 = fmaxf(fmaf(rstd, acc[t][2], fmaf(mr, cs[t][2], bflo(bb1[t].y))), 0.f), y3 = fmaxf(fmaf(rstd, acc[t][3], fmaf(mr, cs[t][3], bfhi(bb1[t].y))), 0.f);
    o[2 * t] = pk2(y0 * y0, y1 * y1); o[2 * t + 1] = pk2(y2 * y2, y3 * y3);
  }
#pragma unroll
  for (int q = 0; q < 3; ++q) st16(ws, v4u{o[4 * q], o[4 * q + 1], o[4 * q + 2], o[4 * q + 3]}, l16, WS_H1 + (unsigned)blk * 49152 + (3 * jj + q) * 1024);
  STAMP(12);
}

#define P4D 8
DEVI void phase4(const Params& p, LAS char* lds, int lane, int w STAMP_ARG) {
  const int x8 = blockIdx.x & 7, r = blockIdx.x >> 3;
  const int ti = 2 * x8 + (r >> 4), hb = (r >> 3) & 1, j = r & 7;
  const v4u srd = mk_srd(p.ws, WS_END);
  const unsigned l16 = lane * 16;
  const int b = w & 3, kh = w >> 2, blk = 8 * ti + 4 * hb + b;
  rbar();
  STAMP(14);
  const unsigned ha = WS_H1 + (unsigned)blk * 49152 + kh * 24 * 1024;
  v4u a[24];
#pragma unroll
  for (int s = 0; s < P4D; ++s) aload(a[s], srd, l16, ha + s * 1024);
  f32x4 acc[5];
#pragma unroll
  for (int t = 0; t < 5; ++t) acc[t] = f32x4{0.f, 0.f, 0.f, 0.f};
  v4u B[2][5];
  LAS char* bb = lds + kh * 20480 + l16;
#define P4_READ(s) do { _Pragma("unroll") for (int t = 0; t < 5; ++t) B[(s) & 1][t] = ldsr16(bb + (((s) >> 2) % 3) * 40960 + ((s) & 3) * 5120 + t * 1024); } while (0)
  P4_READ(0);
  SCHED0();
#pragma unroll
  for (int s = 0; s < 24; ++s) {
    if ((s & 3) == 3 && s < 23) lbar();
    asm volatile("s_waitcnt vmcnt(%1)" : "+v"(a[s]) : "i"(s + P4D - 1 < 24 ? P4D - 1 : 23 - s) : "memory");
    if (s + P4D < 24) aload(a[s + P4D], srd, l16, ha + (s + P4D) * 1024);
    if (s + 1 < 24) P4_READ(s + 1);
#pragma unroll
    for (int t = 0; t < 5; ++t) acc[t] = mfma(B[s & 1][t], a[s], acc[t]);
#pragma unroll
    for (int t = 0; t < 5; ++t) { __builtin_amdgcn_sched_group_barrier(0x100, 1, 0); __builtin_amdgcn_sched_group_barrier(0x8, 1, 0); }
    SCHED0();
  }
#undef P4_READ
  STAMP(15);
  const int g = lane >> 4, c = lane & 15, row = 16 * b + c, mt0 = 3 * kh;
  LAS char* sx = lds + L_STASH + row * 160 + mt0 * 32 + g * 8;
  v2u hv[3], bv[3];
#pragma unroll
  for (int e = 0; e < 3; ++e) {
    const int ee = (e < 2 || kh == 0) ? e : 0;
    hv[e] = *(LAS v2u*)(sx + ee * 32);
    bv[e] = *(LAS v2u*)(lds + L_P4_B2 + (mt0 + ee) * 32 + g * 8);
  }
  f32x4 fin[3];
  exchange_halves(lds, acc, fin, lane, w);
#pragma unroll
  for (int e = 0; e < 3; ++e) {
    if (e < 2 || kh == 0)
      *(LAS v2u*)(sx + e * 32) = v2u{pk2(fin[e][0] + bflo(bv[e].x) + bflo(hv[e].x), fin[e][1] + bfhi(bv[e].x) + bfhi(hv[e].x)),
                                     pk2(fin[e][2] + bflo(bv[e].y) + bflo(hv[e].y), fin[e][3] + bfhi(bv[e].y) + bfhi(hv[e].y))};
  }
  lbar();
  {
    const int q0 = w * 64 + lane, q1 = q0 + 512;
    const v4u o0 = *(LAS v4u*)(lds + L_STASH + q0 * 16);
    v4u o1 = o0; if (w < 2) o1 = *(LAS v4u*)(lds + L_STASH + q1 * 16);
    const int r0 = q0 / 10, r1 = q1 / 10;
    rsrc_t outr = mk_rsrc(p.out, T_ * 1280);
    const unsigned ob = (unsigned)(128 * ti + 64 * hb) * 1280 + 160 * j;
    __builtin_amdgcn_raw_buffer_store_b128(o0, outr, ob + r0 * 1280 + (q0 - r0 * 10) * 16, 0, 0);
    if (w < 2) __builtin_amdgcn_raw_buffer_store_b128(o1, outr, ob + r1 * 1280 + (q1 - r1 * 10) * 16, 0, 0);
  }
  STAMP(16);
}

DEVI void loader(const Params& p, LAS char* lds, int lane, int lw, u64 tag STAMP_ARG) {
  const int x8 = blockIdx.x & 7, r = blockIdx.x >> 3;
  const int t2d = 4 * (x8 >> 1) + (r >> 3), c2d = 8 * (x8 & 1) + (r & 7);
  const int tl = 2 * x8 + (r >> 4), hb = (r >> 3) & 1, j = r & 7;
  rsrc_t ws = mk_rsrc(p.ws, WS_END);
  const v4u srd = mk_srd(p.ws, WS_END);
  const unsigned l16 = lane * 16;
  {
    if (lw == 0) {
      __builtin_amdgcn_s_sleep(P0_POLL_DELAY);
      int bq = (t2d > 0 ? 8 * t2d - 1 : 0) + (lane >> 2); if (t2d == 0 && lane >= 32) bq = 7;
      wait_flags2(p.ws, WS_F0X + (bq * 4 + (lane & 3)) * 8, lane < 36, WS_F0Q + (c2d * 64 + lane) * 8, lane < 40, tag);
      if (lane == 0) __hip_atomic_store((LAS unsigned*)(lds + L_GO), 1u, __ATOMIC_RELAXED, __HIP_MEMORY_SCOPE_WORKGROUP);
    } else {
      for (unsigned sp = 0; sp < (1u << 22); ++sp) {
        if (__builtin_amdgcn_readfirstlane(__hip_atomic_load((LAS unsigned*)(lds + L_GO), __ATOMIC_RELAXED, __HIP_MEMORY_SCOPE_WORKGROUP))) break;
        __builtin_amdgcn_s_sleep(1);
      }
      asm volatile("" ::: "memory");
    }
  }
  STAMP(2);
  unsigned sb[9], si[9];
#pragma unroll
  for (int i = 0; i < 9; ++i) {
    const int n = (lw + 4 * i) < 34 ? (lw + 4 * i) : 33, e = n >= 17 ? 1 : 0, f = n - 17 * e;
    int blk = 8 * t2d - 1 + f; if (blk < 0) blk = 0;
    sb[i] = f < 9 ? WS_XN + (unsigned)blk * 20480 + e * 1024 : WS_WQ + (unsigned)c2d * 163840 + (e * 8 + (f - 9)) * 1024;
    si[i] = f < 9 ? 2048u : 16384u;
  }
#define P1_STG(s) do { if (lw < 2) dma16<16>(ws, lds + ((s) & 3) * P1_STAGE + (lw + 32) * 1024, l16, sb[8] + (s) * si[8]); \
    _Pragma("unroll") for (int i = 0; i < 8; ++i) dma16<16>(ws, lds + ((s) & 3) * P1_STAGE + (lw + 4 * i) * 1024, l16, sb[i] + (s) * si[i]); } while (0)
  P1_STG(0); P1_STG(1);
  WAITV(8); rbar();
  if (lw == 0 && lane < 4) *((LAS unsigned*)(lds + L_CNT) + lane) = 0u;
  P1_STG(2); P1_STG(3);
  v2u f1, f2;
  const unsigned fo1 = lane < 20 ? WS_F0O + (j * 32 + lane) * 8 : WS_F0A + (c2d * 32 + (lane < 40 ? lane - 20 : 0)) * 8;
  const unsigned fo2 = WS_F0B + (j * 64 + (lane < 48 ? lane : 0)) * 8;
  const unsigned wo = WS_WO + (unsigned)j * 102400, w1 = WS_W1 + (unsigned)c2d * 122880, w2 = WS_W2 + (unsigned)j * 245760;
#pragma unroll
  for (int q = 1; q <= 9; ++q) {
    if (q == 1) WAITL0();
    if (q <= 8) WAITV(16); else WAITV(8);
    if (q == 7) asm volatile("" : "+v"(f1), "+v"(f2));
    rbar();
    if (q + 3 <= 9) P1_STG(q + 3);
    if (q == 4) {
      asm volatile("buffer_load_dwordx2 %0, %1, %2, 0 offen sc1" : "=v"(f1) : "v"(fo1), "s"(srd) : "memory");
      asm volatile("buffer_load_dwordx2 %0, %1, %2, 0 offen sc1" : "=v"(f2) : "v"(fo2), "s"(srd) : "memory");
    }
    if (q == 7) {
      const bool ok = (((u64)f1.y << 32) | f1.x) == tag && (((u64)f2.y << 32) | f2.x) == tag;
      if (!__all(ok)) wait_flags2(p.ws, fo1, true, fo2, true, tag);
#pragma unroll
      for (int i = 0; i < 8; ++i) { const int f = 68 + lw + 4 * i; dma16<16>(ws, lds + f * 1024, l16, wo + f * 1024); }
    }
  }
#undef P1_STG
  STAMP(3);
  rbar();
#pragma unroll
  for (int i = 0; i < 17; ++i) { const int f = lw + 4 * i; dma16<16>(ws, lds + f * 1024, l16, wo + f * 1024); }
  {
    rsrc_t xs = mk_rsrc(p.x, T_ * 1280);
#pragma unroll
    for (int m = 0; m < 3; ++m) {
      const int n = lw + 4 * m;
      if (n < 10) { const int q = n * 64 + lane, row = q / 10, ch = q - row * 10;
        dma16<0>(xs, lds + L_STASH + n * 1024, (unsigned)(128 * tl + 64 * hb + row) * 1280 + 160 * j + 16 * ch, 0); }
    }
  }
  STAMP(5);
  if (lw == 0) wait_flags(p.ws, WS_F1 + (tl * 16 + lane) * 8, lane < 16, tag);
  WAITV(0); rbar();
  STAMP(6);
  if (lw == 0) { rsrc_t bs = mk_rsrc(p.b1, F_ * 2); dma16<0>(bs, lds + L_P3_B1, lane < 12 ? 192 * c2d + lane * 16 : 0x7fffffffu, 0); }
#pragma unroll
  for (int i = 0; i < 5; ++i) { const int f = 100 + lw + 4 * i; dma16<16>(ws, lds + f * 1024, l16, w1 + f * 1024); }
  rbar();
#pragma unroll
  for (int i = 0; i < 15; ++i) { const int q = lw + 4 * i, f = q < 30 ? q : q + 20; dma16<16>(ws, lds + f * 1024, l16, w1 + f * 1024); }
  rbar();
#pragma unroll
  for (int i = 0; i < 10; ++i) { const int q = lw + 4 * i, f = q < 20 ? q + 30 : q + 60; dma16<16>(ws, lds + f * 1024, l16, w1 + f * 1024); }
  STAMP(9);
  WAITV(0); rbar();
  if (lw == 0) wait_flags(p.ws, WS_F2 + (t2d * 16 + lane) * 8, lane < 16, tag);
  rbar();
  STAMP(10);
  if (lw == 0) { rsrc_t bs = mk_rsrc(p.b2, D_ * 2); dma16<0>(bs, lds + L_P4_B2, lane < 10 ? 160 * j + lane * 16 : 0x7fffffffu, 0); }
  rbar();
#pragma unroll
  for (int i = 0; i < 10; ++i) { const int f = lw + 4 * i; dma16<16>(ws, lds + f * 1024, l16, w2 + f * 1024); }
  rbar();
#pragma unroll
  for (int i = 0; i < 10; ++i) { const int f = 40 + lw + 4 * i; dma16<16>(ws, lds + f * 1024, l16, w2 + f * 1024); }
  rbar();
  STAMP(11);
#pragma unroll
  for (int i = 0; i < 10; ++i) { const int f = 80 + lw + 4 * i; dma16<16>(ws, lds + f * 1024, l16, w2 + f * 1024); }
  STAMP(13);
  if (lw == 0) wait_flags(p.ws, WS_F3 + (tl * 16 + lane) * 8, lane < 16, tag);
  WAITV(0); rbar();
  STAMP(14);
#define P4_STG(st) do { _Pragma("unroll") for (int i = 0; i < 10; ++i) { const int f = lw + 4 * i; \
    dma16<16>(ws, lds + ((st) % 3) * 40960 + f * 1024, l16, w2 + (st) * 40960 + f * 1024); } } while (0)
  rbar(); P4_STG(3);
  rbar(); P4_STG(4);
  WAITV(10); rbar(); P4_STG(5);
  if (blockIdx.x == 0 && lw == 1) {
    for (int q = 0; q < 4; ++q) wait_flags(p.ws, WS_F1 + (q * 64 + lane) * 8, true, tag);
    if (lane == 0) publish(p.ws, WS_NONCE, tag);
  }
  WAITV(10); rbar();
  WAITV(0); rbar();
#undef P4_STG
  STAMP(15);
  rbar();
  rbar();
  STAMP(16);
}

DEVI void wg_publish(const Params& p, LAS char* lds, int k, int lane, unsigned off, u64 tag) {
  WAITV(0);
  if (lane == 0) {
    const unsigned old = __hip_atomic_fetch_add((LAS unsigned*)(lds + L_CNT) + k, 1u, __ATOMIC_RELAXED, __HIP_MEMORY_SCOPE_WORKGROUP);
    if (old == 7u) publish(p.ws, off, tag);
  }
}

__global__ __launch_bounds__(768, 3) __attribute__((amdgpu_waves_per_eu(3, 3))) void fused_block(Params p) {
  extern __shared__ __attribute__((aligned(16))) char smem[];
  LAS char* lds = (LAS char*)smem;
  const int lane = threadIdx.x & 63, w = __builtin_amdgcn_readfirstlane(threadIdx.x >> 6);
  const int x8 = blockIdx.x & 7, r = blockIdx.x >> 3;
  u64 tag = 0;
  STAMP(0);
  const unsigned pending = phase0(p, lds, lane, w, tag, true STAMP_PASS);
  STAMP(1);
  if (w >= 8) loader(p, lds, lane, w - 8, tag STAMP_PASS);
  else {
    if (CPRIO) __builtin_amdgcn_s_setprio(CPRIO);
    const int t2d = 4 * (x8 >> 1) + (r >> 3), c2d = 8 * (x8 & 1) + (r & 7), tl = 2 * x8 + (r >> 4);
    phase1(p, lds, lane, w, tag, pending STAMP_PASS);
    wg_publish(p, lds, 0, lane, WS_F1 + (t2d * 16 + c2d) * 8, tag);
    STAMP(5);
    phase2(p, lds, lane, w STAMP_PASS);
    wg_publish(p, lds, 1, lane, WS_F2 + (tl * 16 + (r & 15)) * 8, tag);
    STAMP(9);
    phase3(p, lds, lane, w STAMP_PASS);
    wg_publish(p, lds, 2, lane, WS_F3 + (t2d * 16 + c2d) * 8, tag);
    STAMP(13);
    phase4(p, lds, lane, w STAMP_PASS);
  }
}

extern "C" void kernel_launch(void* const* d_in, const int* in_sizes, int n_in, void* d_out, int out_size, void* d_ws, size_t ws_size, hipStream_t stream) {
  Params p;
  p.x = (const unsigned short*)d_in[0]; p.wqkv = (const unsigned short*)d_in[1]; p.wo = (const unsigned short*)d_in[2];
  p.w1 = (const unsigned short*)d_in[4]; p.b1 = (const unsigned short*)d_in[5]; p.w2 = (const unsigned short*)d_in[6]; p.b2 = (const unsigned short*)d_in[7];
  p.out = (unsigned short*)d_out; p.ws = (unsigned char*)d_ws;
  hipFuncSetAttribute((const void*)fused_block, hipFuncAttributeMaxDynamicSharedMemorySize, LDS_BYTES);
  hipLaunchKernelGGL(fused_block, dim3(256), dim3(768), LDS_BYTES, stream, p);
}
```

```cpp
#include <hip/hip_runtime.h>
#include <hip/hip_bf16.h>
#include <stdint.h>

#define GAS __attribute__((address_space(1)))
#define LAS __attribute__((address_space(3)))
#define DEVI __device__ __forceinline__
typedef unsigned v4u __attribute__((ext_vector_type(4)));
typedef unsigned v2u __attribute__((ext_vector_type(2)));
typedef float f32x4 __attribute__((ext_vector_type(4)));
typedef short bf16x8 __attribute__((ext_vector_type(8)));
typedef short s16x4 __attribute__((ext_vector_type(4)));
typedef __bf16 bf16x2 __attribute__((ext_vector_type(2)));
typedef unsigned long long u64;
typedef GAS u64 gu64;
#define RLX_AGENT __ATOMIC_RELAXED, __HIP_MEMORY_SCOPE_AGENT

#define NLAUNCH 1
#define OUT_SC1 0
#define WS_AT_END 0
#define TOUCH 0
#define P1_SAME 0
#define POLL_SLEEP 4
#define P0_POLL_DELAY 64
#define CPRIO 2
#define WARM 1
#define RT_STORE 11
#define RT_PUB 18
#define TOUCH_SLEEP 2
#define WT0 3
#define EARLY_W2 2
#define RT_ISSUE 1
#define TOUCH2_SLEEP 36
#define PRO 0
#define DIAG 0

constexpr int T_ = 2048, D_ = 640, F_ = 1536;
constexpr unsigned WS_NONCE = 0;
constexpr unsigned WS_F0Q = 40960;
constexpr unsigned WS_F0O = 8192;
constexpr unsigned WS_F0A = 12288;
constexpr unsigned WS_F0B = 16384;
constexpr unsigned WS_F0X = 20480;
constexpr unsigned WS_F1 = 24576;
constexpr unsigned WS_F2 = 28672;
constexpr unsigned WS_F3 = 32768;
constexpr unsigned WS_XN = 131072;
constexpr unsigned WS_WQ = WS_XN + 128 * 20480;
constexpr unsigned WS_WO = WS_WQ + 16 * 20 * 8 * 1024;
constexpr unsigned WS_W1 = WS_WO + 8 * 20 * 5 * 1024;
constexpr unsigned WS_W2 = WS_W1 + 16 * 20 * 6 * 1024;
constexpr unsigned WS_O = WS_W2 + 8 * 48 * 5 * 1024;
constexpr unsigned WS_H = WS_O + 128 * 20480;
constexpr unsigned WS_H1 = WS_H + 128 * 20480;
constexpr unsigned WS_ST = WS_H1 + 128 * 49152;
constexpr unsigned WS_END = WS_ST + 128 * 2048;
constexpr unsigned LDS_BYTES = 163840;
constexpr unsigned L_STASH = 153600;
constexpr unsigned L_P0 = 122880;
constexpr unsigned L_TAG = 153600;
constexpr unsigned L_GO = 153616;
#define SHADOW_SLEEP 40
constexpr unsigned P1_STAGE = 40960;
constexpr unsigned L_P1_EX = 102400;
constexpr unsigned P1_RS = 520;
constexpr unsigned L_P1_ST = 144000;
constexpr unsigned L_EXH = 124928;
constexpr unsigned L_P3_B1 = 122880;
constexpr unsigned L_P4_B2 = 145408;
constexpr unsigned L_CNT = 147456;

struct Params {
  const unsigned short* x; const unsigned short* wqkv; const unsigned short* wo; const unsigned short* w1; const unsigned short* b1;
  const unsigned short* w2; const unsigned short* b2; unsigned short* out; unsigned char* ws;
};

typedef __amdgpu_buffer_rsrc_t rsrc_t;
DEVI rsrc_t mk_rsrc(const void* p, unsigned bytes) { return __builtin_amdgcn_make_buffer_rsrc((void*)p, 0, bytes, 0x00020000); }
DEVI v4u mk_srd(const void* p, unsigned bytes) {
  u64 a = (u64)p; v4u r; r.x = (unsigned)a; r.y = (unsigned)(a >> 32) & 0xffffu; r.z = bytes; r.w = 0x00020000u; return r;
}
template <int AUX> DEVI void dma16(rsrc_t rs, LAS char* lds, unsigned voff, unsigned soff) {
  __builtin_amdgcn_raw_ptr_buffer_load_lds(rs, (LAS void*)lds, 16, voff, soff, 0, AUX);
}
DEVI void dma16_asm(v4u srd, unsigned ldsbase, unsigned voff) {
  unsigned keep;
  asm volatile("s_mov_b32 %0, m0\n\ts_mov_b32 m0, %3\n\ts_nop 0\n\tbuffer_load_dwordx4 %1, %2, 0 offen lds\n\ts_mov_b32 m0, %0"
               : "=&s"(keep) : "v"(voff), "s"(srd), "s"(ldsbase) : "memory");
}
DEVI void aload(v4u& d, v4u srd, unsigned voff, unsigned soff) {
  asm volatile("buffer_load_dwordx4 %0, %1, %2, %3 offen sc1" : "=v"(d) : "v"(voff), "s"(srd), "s"(soff) : "memory");
}
DEVI void st16(rsrc_t rs, v4u v, unsigned voff, unsigned soff) { __builtin_amdgcn_raw_buffer_store_b128(v, rs, voff + soff, 0, 16); }
#define WAITV(n) asm volatile("s_waitcnt vmcnt(%0)" ::"i"(n) : "memory")
#define WAITV1(n, a) asm volatile("s_waitcnt vmcnt(%1)" : "+v"(a) : "i"(n) : "memory")
#define WAITV2(n, a, b) asm volatile("s_waitcnt vmcnt(%2)" : "+v"(a), "+v"(b) : "i"(n) : "memory")
#define WAITV4(n, a, b, c, d) asm volatile("s_waitcnt vmcnt(%4)" : "+v"(a), "+v"(b), "+v"(c), "+v"(d) : "i"(n) : "memory")
#define WAITL0() asm volatile("s_waitcnt lgkmcnt(0)" ::: "memory")
#define BAR() __builtin_amdgcn_s_barrier()
#define SCHED0() __builtin_amdgcn_sched_barrier(0)
DEVI void lbar() { WAITL0(); BAR(); asm volatile("" ::: "memory"); }
DEVI void rbar() { asm volatile("" ::: "memory"); BAR(); asm volatile("" ::: "memory"); }

DEVI unsigned pk2(float a, float b) { bf16x2 p; p[0] = (__bf16)a; p[1] = (__bf16)b; return __builtin_bit_cast(unsigned, p); }
DEVI float bflo(unsigned u) { return __uint_as_float(u << 16); }
DEVI float bfhi(unsigned u) { return __uint_as_float(u & 0xffff0000u); }
DEVI bf16x8 asbf(v4u v) { return __builtin_bit_cast(bf16x8, v); }
DEVI f32x4 mfma(v4u a, v4u b, f32x4 c) { return __builtin_amdgcn_mfma_f32_16x16x32_bf16(asbf(a), asbf(b), c, 0, 0, 0); }
DEVI v4u ldsr16(LAS char* p) { return *(LAS v4u*)p; }
DEVI int fresh_lane() { int v; asm volatile("v_mbcnt_lo_u32_b32 %0, -1, 0\n\tv_mbcnt_hi_u32_b32 %0, -1, %0" : "=v"(v)); return v; }
DEVI float fsel(bool c, float a, float b) { asm("" : "+v"(a)); asm("" : "+v"(b)); return c ? a : b; }
DEVI float xrow_max(float v) {
  unsigned u = __float_as_uint(v);
  auto a = __builtin_amdgcn_permlane32_swap(u, u, false, false); v = fmaxf(__uint_as_float(a[0]), __uint_as_float(a[1]));
  u = __float_as_uint(v);
  auto b = __builtin_amdgcn_permlane16_swap(u, u, false, false); return fmaxf(__uint_as_float(b[0]), __uint_as_float(b[1]));
}
DEVI float xrow_sum(float v) {
  unsigned u = __float_as_uint(v);
  auto a = __builtin_amdgcn_permlane32_swap(u, u, false, false); v = __uint_as_float(a[0]) + __uint_as_float(a[1]);
  u = __float_as_uint(v);
  auto b = __builtin_amdgcn_permlane16_swap(u, u, false, false); return __uint_as_float(b[0]) + __uint_as_float(b[1]);
}
DEVI float row16_sum(float v) {
  v += __uint_as_float(__builtin_amdgcn_update_dpp(0u, __float_as_uint(v), 0x128, 0xf, 0xf, false));
  v += __uint_as_float(__builtin_amdgcn_update_dpp(0u, __float_as_uint(v), 0x124, 0xf, 0xf, false));
  v += __uint_as_float(__builtin_amdgcn_update_dpp(0u, __float_as_uint(v), 0x122, 0xf, 0xf, false));
  v += __uint_as_float(__builtin_amdgcn_update_dpp(0u, __float_as_uint(v), 0x121, 0xf, 0xf, false));
  return v;
}

DEVI u64 rfl64(u64 v) { return ((u64)__builtin_amdgcn_readfirstlane((unsigned)(v >> 32)) << 32) | (unsigned)__builtin_amdgcn_readfirstlane((unsigned)v); }
DEVI void publish(unsigned char* ws, unsigned off, u64 tag) { __hip_atomic_store((gu64*)(ws + off), tag, RLX_AGENT); }
DEVI void wait_flags(unsigned char* ws, unsigned laneoff, bool active, u64 tag) {
  for (unsigned sp = 0; sp < (1u << 18); ++sp) {
    u64 v = tag;
    if (active) v = __hip_atomic_load((gu64*)(ws + laneoff), RLX_AGENT);
    if (__all(v == tag)) break;
    __builtin_amdgcn_s_sleep(POLL_SLEEP);
  }
  __builtin_amdgcn_fence(__ATOMIC_ACQUIRE, "wavefront");
  asm volatile("" ::: "memory");
}

DEVI void wait_flags2(unsigned char* ws, unsigned off1, bool act1, unsigned off2, bool act2, u64 tag) {
  for (unsigned sp = 0; sp < (1u << 18); ++sp) {
    u64 v1 = tag, v2 = tag;
    if (act1) v1 = __hip_atomic_load((gu64*)(ws + off1), RLX_AGENT);
    if (act2) v2 = __hip_atomic_load((gu64*)(ws + off2), RLX_AGENT);
    if (__all(v1 == tag && v2 == tag)) break;
    __builtin_amdgcn_s_sleep(POLL_SLEEP);
  }
  __builtin_amdgcn_fence(__ATOMIC_ACQUIRE, "wavefront");
  asm volatile("" ::: "memory");
}

#define STAMP_ARG
#define STAMP_PASS
#define STAMP(id) do {} while (0)

DEVI void w2_unit_issue(const Params& p, LAS char* st, int lane, int a, int ks) {
  const v4u rs = mk_srd(p.w2, 1536u * 1280u);
  const unsigned sb = __builtin_amdgcn_readfirstlane((unsigned)(uintptr_t)st);
#pragma unroll
  for (int n = 0; n < 5; ++n) {
    const int q = n * 64 + lane, rho = (q * 6554) >> 16, ch = q - rho * 10, g = rho >> 3, t = (rho >> 2) & 1, e = rho & 3;
    dma16_asm(rs, sb + n * 1024, (unsigned)(32 * ks + 16 * t + 4 * g + e) * 1280u + 160 * a + 16 * ch);
  }
}
DEVI void w2_unit_store(const Params& p, LAS char* st, int lane, int a, int ks) {
  rsrc_t ws = mk_rsrc(p.ws, WS_END);
  const int kh = ks / 24, s = ks - kh * 24;
  const unsigned dst = WS_W2 + (unsigned)a * 245760 + (s >> 2) * 40960 + kh * 20480 + (s & 3) * 5120;
  const unsigned lo = (unsigned)(uintptr_t)st + (8 * (lane >> 4) + ((lane & 15) >> 2)) * 160 + 8 * (lane & 3);
  v2u a0, a1, a2, a3, a4, b0, b1, b2, b3, b4;
  asm volatile("ds_read_b64_tr_b16 %0, %10\n\tds_read_b64_tr_b16 %5, %10 offset:640\n\tds_read_b64_tr_b16 %1, %10 offset:32\n\tds_read_b64_tr_b16 %6, %10 offset:672\n\t"
               "ds_read_b64_tr_b16 %2, %10 offset:64\n\tds_read_b64_tr_b16 %7, %10 offset:704\n\tds_read_b64_tr_b16 %3, %10 offset:96\n\tds_read_b64_tr_b16 %8, %10 offset:736\n\t"
               "ds_read_b64_tr_b16 %4, %10 offset:128\n\tds_read_b64_tr_b16 %9, %10 offset:768\n\ts_waitcnt lgkmcnt(0)"
               : "=&v"(a0), "=&v"(a1), "=&v"(a2), "=&v"(a3), "=&v"(a4), "=&v"(b0), "=&v"(b1), "=&v"(b2), "=&v"(b3), "=&v"(b4) : "v"(lo) : "memory");
  st16(ws, v4u{a0.x, a0.y, b0.x, b0.y}, lane * 16, dst);
  st16(ws, v4u{a1.x, a1.y, b1.x, b1.y}, lane * 16, dst + 1024);
  st16(ws, v4u{a2.x, a2.y, b2.x, b2.y}, lane * 16, dst + 2048);
  st16(ws, v4u{a3.x, a3.y, b3.x, b3.y}, lane * 16, dst + 3072);
  st16(ws, v4u{a4.x, a4.y, b4.x, b4.y}, lane * 16, dst + 4096);
}
constexpr unsigned L_JUNK = 147520;
DEVI void touch_line(LAS char* lds, const GAS char* a) {
  unsigned keep;
  asm volatile("s_mov_b32 %0, m0\n\ts_mov_b32 m0, %2\n\ts_nop 0\n\tglobal_load_lds_dword %1, off\n\ts_mov_b32 m0, %0" : "=&s"(keep) : "v"(a), "s"((unsigned)(uintptr_t)(lds + L_JUNK)) : "memory");
}
DEVI const GAS char* proj_touch(const Params& p, int n, int lane) {
  int idx = n * 64 + lane; if (idx > 39679) idx = 39679;
  const int s = idx / 3968, rem = idx - s * 3968;
  if (rem < 2048) return (const GAS char*)p.x + (unsigned)rem * 1280u + s * 128;
  const int q = rem - 2048, row = q / 30;
  return (const GAS char*)p.wqkv + (unsigned)(64 * s + row) * 3840u + (q - row * 30) * 128;
}
DEVI const GAS char* weight_touch(const Params& p, int n, int lane) {
  const int idx = n * 64 + lane;
  if (idx < 6400) return (const GAS char*)p.wo + (unsigned)idx * 128u;
  if (idx < 21760) return (const GAS char*)p.w2 + (unsigned)(idx - 6400) * 128u;
  return (const GAS char*)p.w1 + (unsigned)(idx - 21760) * 128u;
}
struct StageTouch { const GAS char* a; unsigned stride; };
DEVI StageTouch stage_touch(const Params& p, int lane) {
  const int x8 = blockIdx.x & 7, r = blockIdx.x >> 3, idx = r * 47 + lane;
  StageTouch t;
  if (lane < 47 && idx < 960) { const int row = (idx * 4370) >> 16, l = idx - row * 15, sec = (l * 52) >> 8;
    t.a = (const GAS char*)p.wqkv + (unsigned)row * 3840u + sec * 1280 + 640 * (x8 & 1) + (l - sec * 5) * 128; t.stride = 64 * 3840; }
  else { int tok = 512 * (x8 >> 1) - 16 + (idx - 960); if (lane >= 47 || idx >= 1488 || tok < 0) tok = 512 * (x8 >> 1);
    t.a = (const GAS char*)p.x + (unsigned)tok * 1280u; t.stride = 128; }
  return t;
}
DEVI v4u trfrag(LAS char* lo, LAS char* hi) {
  const v2u a = __builtin_bit_cast(v2u, __builtin_amdgcn_ds_read_tr16_b64_v4i16((LAS s16x4*)lo));
  const v2u b = __builtin_bit_cast(v2u, __builtin_amdgcn_ds_read_tr16_b64_v4i16((LAS s16x4*)hi));
  return v4u{a.x, a.y, b.x, b.y};
}

DEVI void p1_tail(const Params& p, LAS char* lds, int lane, int w, int tt, int grp, f32x4 (&acc)[2][5], f32x4 st1, f32x4 st2 STAMP_ARG);
DEVI void phase1(const Params& p, LAS char* lds, int lane, int w, u64& tag STAMP_ARG) {
  const int x8 = blockIdx.x & 7, r = blockIdx.x >> 3;
  const int tt = 8 * (x8 >> 1) + (r >> 2), grp = 4 * (x8 & 1) + (r & 3);
  rsrc_t ws = mk_rsrc(p.ws, WS_END);
  const unsigned l16 = lane * 16;
  const int g = lane >> 4, c = lane & 15;
  f32x4 acc[2][5], st1 = {0.f, 0.f, 0.f, 0.f}, st2 = st1;
#pragma unroll
  for (int e = 0; e < 2; ++e)
#pragma unroll
    for (int b = 0; b < 5; ++b) acc[e][b] = f32x4{0.f, 0.f, 0.f, 0.f};
  const unsigned xo0 = c * 128 + ((g ^ (c & 7)) * 16), xo1 = c * 128 + (((4 + g) ^ (c & 7)) * 16);
  const unsigned sblk = (w < 5 ? w : 0) * 2048;
  unsigned wo0, wo1;
  {
    const int p4 = lane & 3, pp = (w >> 2) * 80, m = w & 3;
    wo0 = m == 0 ? 160 + 8 * p4 : m == 1 ? (p4 < 2 ? 160 : 0) + 32 + 40 * (p4 & 1) : m == 2 ? 40 + 8 * p4 : 320 + 40 + 8 * p4;
    wo1 = m == 0 ? 160 + 40 + 8 * p4 : m == 1 ? 8 * p4 : m == 2 ? 320 + 8 * p4 : 320 + 32 + 40 * (p4 & 1);
    const int q4 = (lane & 15) >> 2;
    const unsigned r0 = 10240 + (8 * g + 4 * (g & 1) + q4) * 480 + pp;
    wo0 += r0; wo1 += r0;
  }
  const int hiD = (g & 1) ? -4 * 480 : 4 * 480;
  v4u W[2][2], X[2][6];
#define P1_RO(ks) ((((ks) >> 1) % 3) * P1_STAGE)
#define P1_TR(addr) __builtin_bit_cast(v2u, __builtin_amdgcn_ds_read_tr16_b64_v4i16((LAS s16x4*)(addr)))
#define P1_READ(ks) do { \
    LAS char* wb_ = lds + P1_RO(ks) + ((ks) & 1) * 15360; \
    { v2u l0 = P1_TR(wb_ + wo0), h0 = P1_TR(wb_ + wo0 + hiD), l1 = P1_TR(wb_ + wo1), h1 = P1_TR(wb_ + wo1 + hiD); \
      W[(ks) & 1][0] = v4u{l0.x, l0.y, h0.x, h0.y}; W[(ks) & 1][1] = v4u{l1.x, l1.y, h1.x, h1.y}; } \
    LAS char* xb_ = lds + P1_RO(ks) + (((ks) & 1) ? xo1 : xo0); \
    _Pragma("unroll") for (int b = 0; b < 5; ++b) X[(ks) & 1][b] = ldsr16(xb_ + b * 2048); \
    X[(ks) & 1][5] = ldsr16(xb_ + sblk); } while (0)
  if (WARM && w == 4) { __builtin_amdgcn_s_sleep(TOUCH_SLEEP); touch_line(lds, proj_touch(p, blockIdx.x, lane)); }
  if (WARM && w >= 5) {
    __builtin_amdgcn_s_sleep(TOUCH2_SLEEP);
    if (w == 5) { touch_line(lds, proj_touch(p, blockIdx.x + 256, lane)); if (blockIdx.x + 512 < 620) touch_line(lds, proj_touch(p, blockIdx.x + 512, lane)); }
    else if (w == 6) { touch_line(lds, weight_touch(p, blockIdx.x, lane)); touch_line(lds, weight_touch(p, blockIdx.x + 256, lane)); }
    else if (blockIdx.x + 512 < 580) touch_line(lds, weight_touch(p, blockIdx.x + 512, lane));
  }
  if (w == 3) {
    const u64 nonce = __hip_atomic_load((gu64*)(p.ws + WS_NONCE), RLX_AGENT);
    if (lane == 0) *(LAS u64*)(lds + L_TAG) = nonce * 6364136223846793005ull + 1442695040888963407ull;
  }
  STAMP(2);
  lbar();
  tag = rfl64(*(LAS u64*)(lds + L_TAG));
  P1_READ(0);
  SCHED0();
  const v4u ones = {0x3f803f80u, 0x3f803f80u, 0x3f803f80u, 0x3f803f80u};
#pragma unroll
  for (int ks = 0; ks < 20; ++ks) {
    if ((ks & 1) && ks < 19) lbar();
    if (ks + 1 < 20) P1_READ(ks + 1);
    v4u x0 = X[ks & 1][0];
    if (c == 0) x0 = ones;
#pragma unroll
    for (int b = 0; b < 5; ++b) {
      const v4u xb = b == 0 ? x0 : X[ks & 1][b];
      acc[0][b] = mfma(W[ks & 1][0], xb, acc[0][b]); acc[1][b] = mfma(W[ks & 1][1], xb, acc[1][b]);
    }
    st1 = mfma(ones, X[ks & 1][5], st1); st2 = mfma(X[ks & 1][5], X[ks & 1][5], st2);
    __builtin_amdgcn_sched_group_barrier(0x100, 4, 0);
#pragma unroll
    for (int b = 0; b < 6; ++b) { __builtin_amdgcn_sched_group_barrier(0x8, 2, 0); __builtin_amdgcn_sched_group_barrier(0x100, 1, 0); }
    SCHED0();
  }
#undef P1_RO
#undef P1_TR
#undef P1_READ
  STAMP(3);
  p1_tail(p, lds, fresh_lane(), w, tt, grp, acc, st1, st2 STAMP_PASS);
}
DEVI void p1_tail(const Params& p, LAS char* lds, int lane, int w, int tt, int grp, f32x4 (&acc)[2][5], f32x4 st1, f32x4 st2 STAMP_ARG) {
  rsrc_t ws = mk_rsrc(p.ws, WS_END);
  const unsigned l16 = lane * 16;
  const int g = lane >> 4, c = lane & 15;
  if (w < 5) {
    const float mean = st1[0] * (1.f / 640.f);
    const float e2 = fsel((c & 2) != 0, fsel((c & 1) != 0, st2[3], st2[2]), fsel((c & 1) != 0, st2[1], st2[0])) * (1.f / 640.f);
    const float rstd = __builtin_amdgcn_rsqf(fmaxf(e2 - mean * mean, 0.f) + 1e-5f);
    if (g == (c >> 2)) *(LAS v2u*)(lds + L_P1_ST + (w * 16 + c) * 8) = v2u{__float_as_uint(rstd), __float_as_uint(-mean * rstd)};
  }
  lbar();
  {
    v2u sv[5];
#pragma unroll
    for (int b = 0; b < 5; ++b) sv[b] = *(LAS v2u*)(lds + L_P1_ST + (b * 16 + c) * 8);
    float cs[2][4];
#pragma unroll
    for (int e = 0; e < 2; ++e)
#pragma unroll
      for (int q = 0; q < 4; ++q) cs[e][q] = __uint_as_float(__builtin_amdgcn_update_dpp(0u, __float_as_uint(acc[e][0][q]), 0x150, 0xf, 0xf, false));
    LAS char* d = lds + L_P1_EX + c * P1_RS + w * 64 + g * 8;
#pragma unroll
    for (int b = 0; b < 5; ++b) {
      const float rs = __uint_as_float(sv[b].x), mr = __uint_as_float(sv[b].y);
#pragma unroll
      for (int e = 0; e < 2; ++e)
        *(LAS v2u*)(d + b * 16 * P1_RS + e * 32) = v2u{pk2(fmaf(rs, acc[e][b][0], mr * cs[e][0]), fmaf(rs, acc[e][b][1], mr * cs[e][1])),
                                                       pk2(fmaf(rs, acc[e][b][2], mr * cs[e][2]), fmaf(rs, acc[e][b][3], mr * cs[e][3]))};
    }
  }
  if (DIAG != 4) STAMP(22);
  lbar();
  if (DIAG != 4) STAMP(23);
  const int blk = 4 * tt + (w & 3), pr = 2 * grp + (w >> 2);
  LAS char* own = lds + L_P1_EX + (((w & 3) + 1) * 16 + c) * P1_RS + (w >> 2) * 256 + g * 8;
  LAS char* trp = lds + L_P1_EX + (((w & 3) + 1) * 16 + 4 * g + (c >> 2)) * P1_RS + (w >> 2) * 256 + (c & 3) * 8;
  v2u ka[2], kp[2], qa[2], qr[2], va[2], vp[2];
  const v2u kr = *(LAS v2u*)(own + 64), krp = *(LAS v2u*)(own + 64 - 16 * P1_RS);
#pragma unroll
  for (int hh = 0; hh < 2; ++hh) {
    ka[hh] = *(LAS v2u*)(own + hh * 32); kp[hh] = *(LAS v2u*)(own + hh * 32 - 16 * P1_RS);
    qa[hh] = *(LAS v2u*)(own + (3 + hh) * 32);
    qr[hh] = *(LAS v2u*)(own - g * 8 + 64 + 16 + hh * 8);
    va[hh] = __builtin_bit_cast(v2u, __builtin_amdgcn_ds_read_tr16_b64_v4i16((LAS s16x4*)(trp + (5 + hh) * 32)));
    vp[hh] = __builtin_bit_cast(v2u, __builtin_amdgcn_ds_read_tr16_b64_v4i16((LAS s16x4*)(trp + (5 + hh) * 32 - 16 * P1_RS)));
  }
  const v2u vr = __builtin_bit_cast(v2u, __builtin_amdgcn_ds_read_tr16_b64_v4i16((LAS s16x4*)(trp + 7 * 32)));
  const v2u vrp = __builtin_bit_cast(v2u, __builtin_amdgcn_ds_read_tr16_b64_v4i16((LAS s16x4*)(trp + 7 * 32 - 16 * P1_RS)));
  if (DIAG != 4) STAMP(24);
  SCHED0();
  const int odd = c & 1, ce = c - odd;
  const bool havePrev = (tt > 0) || ((w & 3) > 0);
  const bool v1a = havePrev && (4 * g >= ce + 2), v1b = havePrev && (4 * g >= ce), v2a = ce >= 4 * g, v2b = ce - 2 >= 4 * g;
  const float c2 = 0.22360679774997896f * 1.4426950408889634f, NEG = -3.0e38f;
  const f32x4 z = {0.f, 0.f, 0.f, 0.f};
  f32x4 S1[2], S2[2];
#pragma unroll
  for (int hh = 0; hh < 2; ++hh) {
    const bool mine = g == hh;
    const v4u Q = {qa[hh].x, qa[hh].y, mine ? qr[hh].x : 0u, mine ? qr[hh].y : 0u};
    S1[hh] = mfma(v4u{kp[hh].x, kp[hh].y, krp.x, krp.y}, Q, z);
    S2[hh] = mfma(v4u{ka[hh].x, ka[hh].y, kr.x, kr.y}, Q, z);
  }
  float s1a[2], s1b[2], s2a[2], s2b[2], m[2], inv[2];
#pragma unroll
  for (int hh = 0; hh < 2; ++hh) {
    s1a[hh] = fsel(v1a, fsel(odd, S1[hh][1], S1[hh][0]), NEG); s1b[hh] = fsel(v1b, fsel(odd, S1[hh][3], S1[hh][2]), NEG);
    s2a[hh] = fsel(v2a, fsel(odd, S2[hh][1], S2[hh][0]), NEG); s2b[hh] = fsel(v2b, fsel(odd, S2[hh][3], S2[hh][2]), NEG);
    m[hh] = fmaxf(fmaxf(s1a[hh], s1b[hh]), fmaxf(s2a[hh], s2b[hh]));
  }
  {
    auto a0 = __builtin_amdgcn_permlane32_swap(__float_as_uint(m[0]), __float_as_uint(m[0]), false, false);
    auto a1 = __builtin_amdgcn_permlane32_swap(__float_as_uint(m[1]), __float_as_uint(m[1]), false, false);
    m[0] = fmaxf(__uint_as_float(a0[0]), __uint_as_float(a0[1])); m[1] = fmaxf(__uint_as_float(a1[0]), __uint_as_float(a1[1]));
    auto b0 = __builtin_amdgcn_permlane16_swap(__float_as_uint(m[0]), __float_as_uint(m[0]), false, false);
    auto b1 = __builtin_amdgcn_permlane16_swap(__float_as_uint(m[1]), __float_as_uint(m[1]), false, false);
    m[0] = fmaxf(__uint_as_float(b0[0]), __uint_as_float(b0[1])); m[1] = fmaxf(__uint_as_float(b1[0]), __uint_as_float(b1[1]));
  }
#pragma unroll
  for (int hh = 0; hh < 2; ++hh) {
    s1a[hh] = __builtin_amdgcn_exp2f((s1a[hh] - m[hh]) * c2); s1b[hh] = __builtin_amdgcn_exp2f((s1b[hh] - m[hh]) * c2);
    s2a[hh] = __builtin_amdgcn_exp2f((s2a[hh] - m[hh]) * c2); s2b[hh] = __builtin_amdgcn_exp2f((s2b[hh] - m[hh]) * c2);
    inv[hh] = (s1a[hh] + s1b[hh]) + (s2a[hh] + s2b[hh]);
  }
  {
    auto a0 = __builtin_amdgcn_permlane32_swap(__float_as_uint(inv[0]), __float_as_uint(inv[0]), false, false);
    auto a1 = __builtin_amdgcn_permlane32_swap(__float_as_uint(inv[1]), __float_as_uint(inv[1]), false, false);
    inv[0] = __uint_as_float(a0[0]) + __uint_as_float(a0[1]); inv[1] = __uint_as_float(a1[0]) + __uint_as_float(a1[1]);
    auto b0 = __builtin_amdgcn_permlane16_swap(__float_as_uint(inv[0]), __float_as_uint(inv[0]), false, false);
    auto b1 = __builtin_amdgcn_permlane16_swap(__float_as_uint(inv[1]), __float_as_uint(inv[1]), false, false);
    inv[0] = __builtin_amdgcn_rcpf(__uint_as_float(b0[0]) + __uint_as_float(b0[1])); inv[1] = __builtin_amdgcn_rcpf(__uint_as_float(b1[0]) + __uint_as_float(b1[1]));
  }
  if (DIAG != 4) STAMP(25);
  f32x4 OA[2], OR[2];
  const unsigned sh = 16 * odd;
#pragma unroll
  for (int hh = 0; hh < 2; ++hh) {
    const v4u P = {pk2(s1a[hh] * inv[hh], 0.f) << sh, pk2(s1b[hh] * inv[hh], 0.f) << sh, pk2(s2a[hh] * inv[hh], 0.f) << sh, pk2(s2b[hh] * inv[hh], 0.f) << sh};
    OA[hh] = mfma(v4u{vp[hh].x, vp[hh].y, va[hh].x, va[hh].y}, P, z);
    OR[hh] = mfma(v4u{vrp.x, vrp.y, vr.x, vr.y}, P, z);
  }
  {
    v4u om = {pk2(OA[0][0], OA[0][1]), pk2(OA[0][2], OA[0][3]), pk2(OA[1][0], OA[1][1]), pk2(OA[1][2], OA[1][3])};
    st16(ws, om, l16, WS_O + (unsigned)blk * 20480 + pr * 1024);
    unsigned r0x = pk2(OR[0][0], OR[0][1]), r0y = pk2(OR[0][2], OR[0][3]), r1x = pk2(OR[1][0], OR[1][1]), r1y = pk2(OR[1][2], OR[1][3]);
    auto t0 = __builtin_amdgcn_permlane16_swap(r1x, r1x, false, false); auto t1 = __builtin_amdgcn_permlane16_swap(r1y, r1y, false, false);
    v4u orr = {r0x, r0y, t0[1], t1[1]};
    if (lane < 16) st16(ws, orr, l16, WS_O + (unsigned)blk * 20480 + (16 + (pr >> 2)) * 1024 + (pr & 3) * 256);
  }
  STAMP(4);
}

DEVI void exchange_halves(LAS char* lds, const f32x4 (&acc)[5], f32x4 (&fin)[3], int lane, int w) {
  LAS char* d = lds + L_EXH + (w & 3) * 5120 + lane * 16;
  if (w < 4) { *(LAS f32x4*)(d + 3072) = acc[3]; *(LAS f32x4*)(d + 4096) = acc[4]; }
  else { *(LAS f32x4*)(d) = acc[0]; *(LAS f32x4*)(d + 1024) = acc[1]; *(LAS f32x4*)(d + 2048) = acc[2]; }
  lbar();
  if (w < 4) { f32x4 u0 = *(LAS f32x4*)(d), u1 = *(LAS f32x4*)(d + 1024), u2 = *(LAS f32x4*)(d + 2048); fin[0] = acc[0] + u0; fin[1] = acc[1] + u1; fin[2] = acc[2] + u2; }
  else { f32x4 u0 = *(LAS f32x4*)(d + 3072), u1 = *(LAS f32x4*)(d + 4096); fin[0] = acc[3] + u0; fin[1] = acc[4] + u1; fin[2] = f32x4{0.f, 0.f, 0.f, 0.f}; }
}

DEVI void phase2(const Params& p, LAS char* lds, int lane, int w STAMP_ARG) {
  const int x8 = blockIdx.x & 7, r = blockIdx.x >> 3;
  const int ti = 2 * x8 + (r >> 4), hb = (r >> 3) & 1, j = r & 7;
  rsrc_t ws = mk_rsrc(p.ws, WS_END);
  const v4u srd = mk_srd(p.ws, WS_END);
  const unsigned l16 = lane * 16;
  const int b = w & 3, kh = w >> 2, blk = 8 * ti + 4 * hb + b;
  rbar();
  STAMP(6);
  v4u a[10];
  const unsigned oa = WS_O + (unsigned)blk * 20480 + 10 * kh * 1024;
#pragma unroll
  for (int s = 0; s < 10; ++s) aload(a[s], srd, l16, oa + s * 1024);
  f32x4 acc[5];
#pragma unroll
  for (int t = 0; t < 5; ++t) acc[t] = f32x4{0.f, 0.f, 0.f, 0.f};
  v4u B[2][5];
  LAS char* bb = lds + kh * 51200 + l16;
#pragma unroll
  for (int t = 0; t < 5; ++t) B[0][t] = ldsr16(bb + t * 1024);
  SCHED0();
#pragma unroll
  for (int s = 0; s < 10; ++s) {
    if (s == 5) lbar();
    asm volatile("s_waitcnt vmcnt(%1)" : "+v"(a[s]) : "i"(9 - s) : "memory");
    if (s + 1 < 10) {
#pragma unroll
      for (int t = 0; t < 5; ++t) B[(s + 1) & 1][t] = ldsr16(bb + (s + 1) * 5120 + t * 1024);
    }
#pragma unroll
    for (int t = 0; t < 5; ++t) acc[t] = mfma(B[s & 1][t], a[s], acc[t]);
#pragma unroll
    for (int t = 0; t < 5; ++t) { __builtin_amdgcn_sched_group_barrier(0x100, 1, 0); __builtin_amdgcn_sched_group_barrier(0x8, 1, 0); }
    SCHED0();
  }
  STAMP(7);
  const int g = lane >> 4, c = lane & 15, row = 16 * b + c, mt0 = 3 * kh;
  LAS char* sx = lds + L_STASH + row * 160 + mt0 * 32 + g * 8;
  v2u xv[3];
#pragma unroll
  for (int e = 0; e < 3; ++e) xv[e] = *(LAS v2u*)(sx + (e < 2 || kh == 0 ? e : 0) * 32);
  f32x4 fin[3];
  exchange_halves(lds, acc, fin, lane, w);
  v2u hv[3];
  float ssum = 0.f, ssq = 0.f;
#pragma unroll
  for (int e = 0; e < 3; ++e) {
    hv[e] = v2u{pk2(fin[e][0] + bflo(xv[e].x), fin[e][1] + bfhi(xv[e].x)), pk2(fin[e][2] + bflo(xv[e].y), fin[e][3] + bfhi(xv[e].y))};
    if (e < 2 || kh == 0) {
      *(LAS v2u*)(sx + e * 32) = hv[e];
      const float h0 = bflo(hv[e].x), h1 = bfhi(hv[e].x), h2 = bflo(hv[e].y), h3 = bfhi(hv[e].y);
      ssum += (h0 + h1) + (h2 + h3); ssq += fmaf(h0, h0, h1 * h1) + fmaf(h2, h2, h3 * h3);
    }
  }
  {
    auto sx0 = __builtin_amdgcn_permlane16_swap(hv[0].x, hv[1].x, false, false); auto sy0 = __builtin_amdgcn_permlane16_swap(hv[0].y, hv[1].y, false, false);
    const int G = 2 * (mt0 + (g & 1)) + (g >> 1);
    st16(ws, v4u{sx0[0], sy0[0], sx0[1], sy0[1]}, c * 16 + (10 * j + G) * 256, WS_H + (unsigned)blk * 20480);
    if (kh == 0) __builtin_amdgcn_raw_buffer_store_b64(hv[2], ws, WS_H + (unsigned)blk * 20480 + (10 * j + 4 + (g >> 1)) * 256 + c * 16 + (g & 1) * 8, 0, 16);
  }
  ssum = xrow_sum(ssum); ssq = xrow_sum(ssq);
  {
    const int idx = 2 * j + kh;
    const int e = idx & 3;
    if (lane < 16) {
      v2u sv = {__float_as_uint(ssum), __float_as_uint(ssq)};
      __builtin_amdgcn_raw_buffer_store_b64(sv, ws, WS_ST + (unsigned)blk * 2048 + (e >> 1) * 1024 + (idx >> 2) * 256 + c * 16 + (e & 1) * 8, 0, 16);
    }
  }
  STAMP(8);
}

#define P3D 8
DEVI void phase3(const Params& p, LAS char* lds, int lane, int w STAMP_ARG) {
  const int x8 = blockIdx.x & 7, r = blockIdx.x >> 3;
  const int ti = 4 * (x8 >> 1) + (r >> 3), jj = 8 * (x8 & 1) + (r & 7);
  rsrc_t ws = mk_rsrc(p.ws, WS_END);
  const v4u srd = mk_srd(p.ws, WS_END);
  const unsigned l16 = lane * 16;
  const int blk = 8 * ti + w;
  rbar();
  if (w < 6) {
    v4u cf[20];
#pragma unroll
    for (int ks = 0; ks < 20; ++ks) cf[ks] = ldsr16(lds + (ks * 6 + w) * 1024 + l16);
    SCHED0();
    f32x4 cs0 = {0.f, 0.f, 0.f, 0.f}, cs1 = cs0;
    const v4u ones = {0x3f803f80u, 0x3f803f80u, 0x3f803f80u, 0x3f803f80u};
#pragma unroll
    for (int ks = 0; ks < 20; ks += 2) { cs0 = mfma(cf[ks], ones, cs0); cs1 = mfma(cf[ks + 1], ones, cs1); }
    if ((lane & 15) == 0) *(LAS f32x4*)(lds + L_P3_B1 + 1024 + (w * 16 + (lane >> 4) * 4) * 4) = cs0 + cs1;
  }
  lbar();
  STAMP(10);
  v4u a[20], st0, st1;
  const unsigned ha = WS_H + (unsigned)blk * 20480;
#pragma unroll
  for (int s = 0; s < P3D; ++s) aload(a[s], srd, l16, ha + s * 1024);
  f32x4 acc[6];
#pragma unroll
  for (int t = 0; t < 6; ++t) acc[t] = f32x4{0.f, 0.f, 0.f, 0.f};
  v4u B[2][6];
  LAS char* bb = lds + l16;
#define P3_RD(s, t) ldsr16(bb + (s) * 6144 + (t) * 1024)
#pragma unroll
  for (int t = 0; t < 6; ++t) B[0][t] = P3_RD(0, t);
  SCHED0();
#pragma unroll
  for (int s = 0; s < 20; ++s) {
    if (s == 6 || s == 13) lbar();
    asm volatile("s_waitcnt vmcnt(%1)" : "+v"(a[s]) : "i"(s + P3D - 1 < 22 ? P3D - 1 : 21 - s) : "memory");
    if (s + P3D < 20) aload(a[s + P3D], srd, l16, ha + (s + P3D) * 1024);
    else if (s + P3D == 20) aload(st0, srd, l16, WS_ST + (unsigned)blk * 2048);
    else if (s + P3D == 21) aload(st1, srd, l16, WS_ST + (unsigned)blk * 2048 + 1024);
    if (s + 1 < 20) {
#pragma unroll
      for (int t = 0; t < 6; ++t) B[(s + 1) & 1][t] = P3_RD(s + 1, t);
    }
#pragma unroll
    for (int t = 0; t < 6; ++t) acc[t] = mfma(B[s & 1][t], a[s], acc[t]);
#pragma unroll
    for (int t = 0; t < 6; ++t) { __builtin_amdgcn_sched_group_barrier(0x100, 1, 0); __builtin_amdgcn_sched_group_barrier(0x8, 1, 0); }
    SCHED0();
  }
#undef P3_RD
  lbar();
  STAMP(11);
  const int g = lane >> 4;
  f32x4 cs[6]; v2u bb1[6];
#pragma unroll
  for (int t = 0; t < 6; ++t) { cs[t] = *(LAS f32x4*)(lds + L_P3_B1 + 1024 + (t * 16 + g * 4) * 4); bb1[t] = *(LAS v2u*)(lds + L_P3_B1 + (t * 16 + g * 4) * 2); }
  WAITV2(0, st0, st1);
  SCHED0();
  float s1 = (__uint_as_float(st0[0]) + __uint_as_float(st0[2])) + (__uint_as_float(st1[0]) + __uint_as_float(st1[2]));
  float s2 = (__uint_as_float(st0[1]) + __uint_as_float(st0[3])) + (__uint_as_float(st1[1]) + __uint_as_float(st1[3]));
  s1 = xrow_sum(s1); s2 = xrow_sum(s2);
  const float mean = s1 * (1.f / 640.f);
  const float var = fmaxf(s2 * (1.f / 640.f) - mean * mean, 0.f);
  const float rstd = __builtin_amdgcn_rsqf(var + 1e-5f), mr = -mean * rstd;
  unsigned o[12];
#pragma unroll
  for (int t = 0; t < 6; ++t) {
    float y0 = fmaxf(fmaf(rstd, acc[t][0], fmaf(mr, cs[t][0], bflo(bb1[t].x))), 0.f), y1 = fmaxf(fmaf(rstd, acc[t][1], fmaf(mr, cs[t][1], bfhi(bb1[t].x))), 0.f);
    float y2 = fmaxf(fmaf(rstd, acc[t][2], fmaf(mr, cs[t][2], bflo(bb1[t].y))), 0.f), y3 = fmaxf(fmaf(rstd, acc[t][3], fmaf(mr, cs[t][3], bfhi(bb1[t].y))), 0.f);
    o[2 * t] = pk2(y0 * y0, y1 * y1); o[2 * t + 1] = pk2(y2 * y2, y3 * y3);
  }
#pragma unroll
  for (int q = 0; q < 3; ++q) st16(ws, v4u{o[4 * q], o[4 * q + 1], o[4 * q + 2], o[4 * q + 3]}, l16, WS_H1 + (unsigned)blk * 49152 + (3 * jj + q) * 1024);
  STAMP(12);
}

#define P4D 8
DEVI void phase4(const Params& p, LAS char* lds, int lane, int w STAMP_ARG) {
  const int x8 = blockIdx.x & 7, r = blockIdx.x >> 3;
  const int ti = 2 * x8 + (r >> 4), hb = (r >> 3) & 1, j = r & 7;
  const v4u srd = mk_srd(p.ws, WS_END);
  const unsigned l16 = lane * 16;
  const int b = w & 3, kh = w >> 2, blk = 8 * ti + 4 * hb + b;
  rbar();
  STAMP(14);
  const unsigned ha = WS_H1 + (unsigned)blk * 49152 + kh * 24 * 1024;
  v4u a[24];
#pragma unroll
  for (int s = 0; s < P4D; ++s) aload(a[s], srd, l16, ha + s * 1024);
  f32x4 acc[5];
#pragma unroll
  for (int t = 0; t < 5; ++t) acc[t] = f32x4{0.f, 0.f, 0.f, 0.f};
  v4u B[2][5];
  LAS char* bb = lds + kh * 20480 + l16;
#define P4_READ(s) do { _Pragma("unroll") for (int t = 0; t < 5; ++t) B[(s) & 1][t] = ldsr16(bb + (((s) >> 2) % 3) * 40960 + ((s) & 3) * 5120 + t * 1024); } while (0)
  P4_READ(0);
  SCHED0();
#pragma unroll
  for (int s = 0; s < 24; ++s) {
    if ((s & 3) == 3 && s < 23) lbar();
    asm volatile("s_waitcnt vmcnt(%1)" : "+v"(a[s]) : "i"(s + P4D - 1 < 24 ? P4D - 1 : 23 - s) : "memory");
    if (s + P4D < 24) aload(a[s + P4D], srd, l16, ha + (s + P4D) * 1024);
    if (s + 1 < 24) P4_READ(s + 1);
#pragma unroll
    for (int t = 0; t < 5; ++t) acc[t] = mfma(B[s & 1][t], a[s], acc[t]);
#pragma unroll
    for (int t = 0; t < 5; ++t) { __builtin_amdgcn_sched_group_barrier(0x100, 1, 0); __builtin_amdgcn_sched_group_barrier(0x8, 1, 0); }
    SCHED0();
  }
#undef P4_READ
  STAMP(15);
  const int g = lane >> 4, c = lane & 15, row = 16 * b + c, mt0 = 3 * kh;
  LAS char* sx = lds + L_STASH + row * 160 + mt0 * 32 + g * 8;
  v2u hv[3], bv[3];
#pragma unroll
  for (int e = 0; e < 3; ++e) {
    const int ee = (e < 2 || kh == 0) ? e : 0;
    hv[e] = *(LAS v2u*)(sx + ee * 32);
    bv[e] = *(LAS v2u*)(lds + L_P4_B2 + (mt0 + ee) * 32 + g * 8);
  }
  f32x4 fin[3];
  exchange_halves(lds, acc, fin, lane, w);
#pragma unroll
  for (int e = 0; e < 3; ++e) {
    if (e < 2 || kh == 0)
      *(LAS v2u*)(sx + e * 32) = v2u{pk2(fin[e][0] + bflo(bv[e].x) + bflo(hv[e].x), fin[e][1] + bfhi(bv[e].x) + bfhi(hv[e].x)),
                                     pk2(fin[e][2] + bflo(bv[e].y) + bflo(hv[e].y), fin[e][3] + bfhi(bv[e].y) + bfhi(hv[e].y))};
  }
  lbar();
  {
    const int q0 = w * 64 + lane, q1 = q0 + 512;
    const v4u o0 = *(LAS v4u*)(lds + L_STASH + q0 * 16);
    v4u o1 = o0; if (w < 2) o1 = *(LAS v4u*)(lds + L_STASH + q1 * 16);
    const int r0 = q0 / 10, r1 = q1 / 10;
    rsrc_t outr = mk_rsrc(p.out, T_ * 1280);
    const unsigned ob = (unsigned)(128 * ti + 64 * hb) * 1280 + 160 * j;
    __builtin_amdgcn_raw_buffer_store_b128(o0, outr, ob + r0 * 1280 + (q0 - r0 * 10) * 16, 0, 0);
    if (w < 2) __builtin_amdgcn_raw_buffer_store_b128(o1, outr, ob + r1 * 1280 + (q1 - r1 * 10) * 16, 0, 0);
  }
  STAMP(16);
}

DEVI unsigned ldsaddr(LAS char* p) { return (unsigned)(uintptr_t)p; }
DEVI void convert5(LAS char* u, int lane) {
  const int g = lane >> 4;
  const unsigned lo = ldsaddr(u) + (8 * g + 4 * (g & 1) + ((lane & 15) >> 2)) * 160 + (lane & 3) * 8, hi = lo + ((g & 1) ? -640 : 640), d = ldsaddr(u) + lane * 16;
  v2u a0, a1, a2, a3, a4, b0, b1, b2, b3, b4;
  asm volatile("ds_read_b64_tr_b16 %0, %10\n\tds_read_b64_tr_b16 %5, %11\n\tds_read_b64_tr_b16 %1, %10 offset:32\n\tds_read_b64_tr_b16 %6, %11 offset:32\n\t"
               "ds_read_b64_tr_b16 %2, %10 offset:64\n\tds_read_b64_tr_b16 %7, %11 offset:64\n\tds_read_b64_tr_b16 %3, %10 offset:96\n\tds_read_b64_tr_b16 %8, %11 offset:96\n\t"
               "ds_read_b64_tr_b16 %4, %10 offset:128\n\tds_read_b64_tr_b16 %9, %11 offset:128\n\ts_waitcnt lgkmcnt(0)"
               : "=&v"(a0), "=&v"(a1), "=&v"(a2), "=&v"(a3), "=&v"(a4), "=&v"(b0), "=&v"(b1), "=&v"(b2), "=&v"(b3), "=&v"(b4) : "v"(lo), "v"(hi) : "memory");
  const v4u f0 = {a0.x, a0.y, b0.x, b0.y}, f1 = {a1.x, a1.y, b1.x, b1.y}, f2 = {a2.x, a2.y, b2.x, b2.y}, f3 = {a3.x, a3.y, b3.x, b3.y}, f4 = {a4.x, a4.y, b4.x, b4.y};
  asm volatile("ds_write_b128 %0, %1\n\tds_write_b128 %0, %2 offset:1024\n\tds_write_b128 %0, %3 offset:2048\n\tds_write_b128 %0, %4 offset:3072\n\tds_write_b128 %0, %5 offset:4096\n\ts_waitcnt lgkmcnt(0)"
               ::"v"(d), "v"(f0), "v"(f1), "v"(f2), "v"(f3), "v"(f4) : "memory");
}
DEVI void convert6(LAS char* u, int lane) {
  const int g = lane >> 4, od = g & 1;
  const unsigned row = ldsaddr(u) + (8 * g + ((lane & 15) >> 2)) * 192 + (lane & 3) * 8, lo = row + 32 * od, l5 = row + (od ? 0 : 160), d = ldsaddr(u) + lane * 16;
  v2u a0, a1, a2, a3, a4, a5, b0, b1, b2, b3, b4, b5;
  asm volatile("ds_read_b64_tr_b16 %0, %12\n\tds_read_b64_tr_b16 %6, %12 offset:768\n\tds_read_b64_tr_b16 %1, %12 offset:32\n\tds_read_b64_tr_b16 %7, %12 offset:800\n\t"
               "ds_read_b64_tr_b16 %2, %12 offset:64\n\tds_read_b64_tr_b16 %8, %12 offset:832\n\tds_read_b64_tr_b16 %3, %12 offset:96\n\tds_read_b64_tr_b16 %9, %12 offset:864\n\t"
               "ds_read_b64_tr_b16 %4, %12 offset:128\n\tds_read_b64_tr_b16 %10, %12 offset:896\n\tds_read_b64_tr_b16 %5, %13\n\tds_read_b64_tr_b16 %11, %13 offset:768\n\ts_waitcnt lgkmcnt(0)"
               : "=&v"(a0), "=&v"(a1), "=&v"(a2), "=&v"(a3), "=&v"(a4), "=&v"(a5), "=&v"(b0), "=&v"(b1), "=&v"(b2), "=&v"(b3), "=&v"(b4), "=&v"(b5) : "v"(lo), "v"(l5) : "memory");
  const v4u f0 = {a0.x, a0.y, b0.x, b0.y}, f1 = {a1.x, a1.y, b1.x, b1.y}, f2 = {a2.x, a2.y, b2.x, b2.y}, f3 = {a3.x, a3.y, b3.x, b3.y}, f4 = {a4.x, a4.y, b4.x, b4.y}, f5 = {a5.x, a5.y, b5.x, b5.y};
  asm volatile("ds_write_b128 %0, %1\n\tds_write_b128 %0, %2 offset:1024\n\tds_write_b128 %0, %3 offset:2048\n\tds_write_b128 %0, %4 offset:3072\n\tds_write_b128 %0, %5 offset:4096\n\t"
               "ds_write_b128 %0, %6 offset:5120\n\ts_waitcnt lgkmcnt(0)"
               ::"v"(d), "v"(f0), "v"(f1), "v"(f2), "v"(f3), "v"(f4), "v"(f5) : "memory");
}

DEVI void loader(const Params& p, LAS char* lds, int lane, int lw STAMP_ARG) {
  const int x8 = blockIdx.x & 7, r = blockIdx.x >> 3;
  const int t2d = 4 * (x8 >> 1) + (r >> 3), c2d = 8 * (x8 & 1) + (r & 7);
  const int tl = 2 * x8 + (r >> 4), hb = (r >> 3) & 1, j = r & 7;
  rsrc_t ws = mk_rsrc(p.ws, WS_END);
  const v4u srd = mk_srd(p.ws, WS_END);
  const unsigned l16 = lane * 16;
  const int tt = 8 * (x8 >> 1) + (r >> 2), grp = 4 * (x8 & 1) + (r & 3);
  rsrc_t xs = mk_rsrc(p.x, T_ * 1280), wqs = mk_rsrc(p.wqkv, 640u * 3840u);
  unsigned vx[3], vw[8];
#pragma unroll
  for (int i = 0; i < 3; ++i) {
    const int m = lw + 4 * i, row = 8 * m + (lane >> 3);
    int tok = 64 * tt - 16 + row; if (tok < 0) tok = 0; if (tok > T_ - 1) tok = T_ - 1;
    vx[i] = (unsigned)tok * 1280 + (((lane & 7) ^ (row & 7)) * 16);
  }
  const int wn0 = lw < 2 ? lw * 7 - 1 : 14 + (lw - 2) * 8;
#pragma unroll
  for (int i = 0; i < 8; ++i) {
    int n = wn0 + i; if (n < 0) n = 0;
    const int q = n * 64 + lane, rho = (q * 2185) >> 16, ch = q - rho * 30, sec = (ch * 205) >> 11;
    const int gg = (rho >> 3) & 3, krow = (rho & 32) + 8 * gg + 4 * (((rho >> 2) & 1) ^ (gg & 1)) + (rho & 3);
    vw[i] = (unsigned)krow * 3840u + sec * 1280 + 160 * grp + 16 * (ch - sec * 10);
  }
#define P1_STG(s) do { LAS char* sb_ = lds + ((s) % 3) * P1_STAGE; \
    dma16<0>(xs, sb_ + lw * 1024, vx[0], (s) * 128); dma16<0>(xs, sb_ + (lw + 4) * 1024, vx[1], (s) * 128); \
    if (lw < 2) dma16<0>(xs, sb_ + (lw + 8) * 1024, vx[2], (s) * 128); else dma16<0>(wqs, sb_ + 10240 + wn0 * 1024, vw[0], (s) * 64 * 3840); \
    _Pragma("unroll") for (int i = 1; i < 8; ++i) dma16<0>(wqs, sb_ + 10240 + (wn0 + i) * 1024, vw[i], (s) * 64 * 3840); } while (0)
  STAMP(17);
  P1_STG(0);
  STAMP(18);
  if (PRO == 0) P1_STG(1);
  STAMP(19);
  if (PRO == 0) WAITV(10); else WAITV(0);
  STAMP(20);
  rbar();
  const u64 tag = rfl64(*(LAS u64*)(lds + L_TAG));
  if (lw == 0 && lane < 4) *((LAS unsigned*)(lds + L_CNT) + lane) = 0u;
  STAMP(2);
  if (PRO == 1) P1_STG(1);
  P1_STG(2);
  rsrc_t wos = mk_rsrc(p.wo, 640u * 1280u), w1s = mk_rsrc(p.w1, 640u * 3072u);
  unsigned vA[5], vB[5], vD[6];
#pragma unroll
  for (int i = 0; i < 5; ++i) {
    const int q = 64 * i + lane, rho = (q * 6554) >> 16, cc = q - rho * 10, g = rho >> 3, t = ((rho >> 2) & 1) ^ (g & 1), e = rho & 3;
    const unsigned col = 160 * j + 16 * cc;
    vA[i] = (unsigned)(20 * t + 4 * g + e) * 1280u + col;
    vB[i] = (unsigned)(40 * g + 20 * t + 16 + e) * 1280u + col;
  }
#pragma unroll
  for (int i = 0; i < 6; ++i) {
    const int q = 64 * i + lane, rho = (q * 5462) >> 16, pos = q - rho * 12, cc = ((rho >> 3) & 1) ? (pos < 2 ? pos + 10 : pos - 2) : pos;
    vD[i] = (unsigned)rho * 3072u + 192 * c2d + 16 * cc;
  }
#define WO_KS(ks) do { _Pragma("unroll") for (int i = 0; i < 5; ++i) {     if ((ks) < 16) dma16<0>(wos, lds + (ks) * 5120 + i * 1024, vA[i], (ks) * 40 * 1280); else dma16<0>(wos, lds + (ks) * 5120 + i * 1024, vB[i], ((ks) - 16) * 160 * 1280); } } while (0)
#define W1_KS(ks) do { _Pragma("unroll") for (int i = 0; i < 6; ++i) dma16<0>(w1s, lds + (ks) * 6144 + i * 1024, vD[i], (ks) * 32 * 3072); } while (0)
  const unsigned w2 = WS_W2 + (unsigned)j * 245760;
#define W2_STG(st) do { _Pragma("unroll") for (int i = 0; i < 10; ++i) { const int f = lw + 4 * i; \
    dma16<16>(ws, lds + ((st) % 3) * 40960 + f * 1024, l16, w2 + (st) * 40960 + f * 1024); } } while (0)
  const int uslot = blockIdx.x + 256 * (3 - lw);
  const bool haveu = lw >= 2 && uslot < 384;
  LAS char* ust = lds + L_P0 + (lw & 1) * 5120;
#pragma unroll
  for (int q = 1; q <= 9; ++q) {
    if (q == 1) WAITL0();
    if (q >= 8 && haveu) WAITV(15); else WAITV(10);
    rbar();
    if (q + 2 <= 9) P1_STG(q + 2);
    if (q == 7 && haveu) w2_unit_issue(p, ust, lane, uslot & 7, uslot >> 3);
    if (q == 8) { WO_KS(8 + lw); WO_KS(12 + lw); }
    if (q == 9) WO_KS(16 + lw);
  }
  if (haveu) { WAITV(15); w2_unit_store(p, ust, lane, uslot & 7, uslot >> 3); }
#undef P1_STG
  STAMP(3);
  rbar();
  WO_KS(lw); WO_KS(4 + lw);
  {
#pragma unroll
    for (int m = 0; m < 3; ++m) {
      const int n = lw + 4 * m;
      if (n < 10) { const int q = n * 64 + lane, row = q / 10, ch = q - row * 10;
        dma16<0>(xs, lds + L_STASH + n * 1024, (unsigned)(128 * tl + 64 * hb + row) * 1280 + 160 * j + 16 * ch, 0); }
    }
  }
  STAMP(5);
  rbar();
  WAITV(0);
  if (haveu && lane == 0) publish(p.ws, WS_F0B + ((uslot & 7) * 64 + (uslot >> 3)) * 8, tag);
  convert5(lds + (8 + lw) * 5120, lane); convert5(lds + (12 + lw) * 5120, lane); convert5(lds + (16 + lw) * 5120, lane);
  convert5(lds + lw * 5120, lane); convert5(lds + (4 + lw) * 5120, lane);
  if (lw == 0) wait_flags(p.ws, WS_F1 + (tl * 16 + hb * 8 + lane) * 8, lane < 8, tag);
  lbar();
  STAMP(6);
  if (lw == 0) { rsrc_t bs = mk_rsrc(p.b1, F_ * 2); dma16<0>(bs, lds + L_P3_B1, lane < 12 ? 192 * c2d + lane * 16 : 0x7fffffffu, 0); }
  if (lw < 3) W1_KS(17 + lw);
  rbar();
  W1_KS(lw); W1_KS(9 + lw); if (lw == 3) W1_KS(4);
  rbar();
  W1_KS(5 + lw); W1_KS(13 + lw);
  STAMP(9);
  WAITV(12);
  if (lw < 3) convert6(lds + (17 + lw) * 6144, lane);
  convert6(lds + lw * 6144, lane); convert6(lds + (9 + lw) * 6144, lane); if (lw == 3) convert6(lds + 4 * 6144, lane);
  WAITV(0);
  convert6(lds + (5 + lw) * 6144, lane); convert6(lds + (13 + lw) * 6144, lane);
  lbar();
  if (lw == 0) wait_flags(p.ws, WS_F2 + (t2d * 16 + lane) * 8, lane < 16, tag);
  rbar();
  STAMP(10);
  wait_flags(p.ws, WS_F0B + (j * 64 + lane) * 8, lane < 48, tag);
  if (lw == 0) { rsrc_t bs = mk_rsrc(p.b2, D_ * 2); dma16<0>(bs, lds + L_P4_B2, lane < 10 ? 160 * j + lane * 16 : 0x7fffffffu, 0); }
  rbar();
  W2_STG(0);
  rbar();
  W2_STG(1);
  rbar();
  STAMP(11);
  W2_STG(2);
  STAMP(13);
  if (lw == 0) wait_flags(p.ws, WS_F3 + (tl * 16 + lane) * 8, lane < 16, tag);
  WAITV(0); rbar();
  STAMP(14);
  rbar(); W2_STG(3);
  rbar(); W2_STG(4);
  WAITV(10); rbar(); W2_STG(5);
  if (blockIdx.x == 0 && lw == 1) {
    for (int q = 0; q < 4; ++q) wait_flags(p.ws, WS_F1 + (q * 64 + lane) * 8, true, tag);
    if (lane == 0) publish(p.ws, WS_NONCE, tag);
  }
  WAITV(10); rbar();
  WAITV(0); rbar();
#undef W2_STG
#undef WO_KS
#undef W1_KS
  STAMP(15);
  rbar();
  rbar();
  STAMP(16);
}

DEVI void wg_publish(const Params& p, LAS char* lds, int k, int lane, unsigned off, u64 tag) {
  WAITV(0);
  if (lane == 0) {
    const unsigned old = __hip_atomic_fetch_add((LAS unsigned*)(lds + L_CNT) + k, 1u, __ATOMIC_RELAXED, __HIP_MEMORY_SCOPE_WORKGROUP);
    if (old == 7u) publish(p.ws, off, tag);
  }
}

__global__ __launch_bounds__(768, 3) __attribute__((amdgpu_waves_per_eu(3, 3))) void fused_block(Params p) {
  extern __shared__ __attribute__((aligned(16))) char smem[];
  LAS char* lds = (LAS char*)smem;
  const int lane = threadIdx.x & 63, w = __builtin_amdgcn_readfirstlane(threadIdx.x >> 6);
  const int x8 = blockIdx.x & 7, r = blockIdx.x >> 3;
  u64 tag = 0;
  STAMP(0);
  if (w >= 8) loader(p, lds, lane, w - 8 STAMP_PASS);
  else {

    if (CPRIO) __builtin_amdgcn_s_setprio(CPRIO);
    const int t2d = 4 * (x8 >> 1) + (r >> 3), c2d = 8 * (x8 & 1) + (r & 7), tl = 2 * x8 + (r >> 4);
    phase1(p, lds, lane, w, tag STAMP_PASS);
    { const int tt = 8 * (x8 >> 1) + (r >> 2), grp = 4 * (x8 & 1) + (r & 3);
      wg_publish(p, lds, 0, fresh_lane(), WS_F1 + ((tt >> 1) * 16 + (tt & 1) * 8 + grp) * 8, tag); }
    STAMP(5);
    phase2(p, lds, fresh_lane(), w STAMP_PASS);
    wg_publish(p, lds, 1, fresh_lane(), WS_F2 + (tl * 16 + (r & 15)) * 8, tag);
    STAMP(9);
    phase3(p, lds, fresh_lane(), w STAMP_PASS);
    wg_publish(p, lds, 2, fresh_lane(), WS_F3 + (t2d * 16 + c2d) * 8, tag);
    STAMP(13);
    phase4(p, lds, fresh_lane(), w STAMP_PASS);
  }
}

extern "C" void kernel_launch(void* const* d_in, const int* in_sizes, int n_in, void* d_out, int out_size, void* d_ws, size_t ws_size, hipStream_t stream) {
  Params p;
  p.x = (const unsigned short*)d_in[0]; p.wqkv = (const unsigned short*)d_in[1]; p.wo = (const unsigned short*)d_in[2];
  p.w1 = (const unsigned short*)d_in[4]; p.b1 = (const unsigned short*)d_in[5]; p.w2 = (const unsigned short*)d_in[6]; p.b2 = (const unsigned short*)d_in[7];
  p.out = (unsigned short*)d_out; p.ws = (unsigned char*)d_ws;
  hipFuncSetAttribute((const void*)fused_block, hipFuncAttributeMaxDynamicSharedMemorySize, LDS_BYTES);
  hipLaunchKernelGGL(fused_block, dim3(256), dim3(768), LDS_BYTES, stream, p);
}
```

```cpp
#include <hip/hip_runtime.h>
#include <hip/hip_bf16.h>
#include <stdint.h>

#define GAS __attribute__((address_space(1)))
#define LAS __attribute__((address_space(3)))
#define DEVI __device__ __forceinline__
typedef unsigned v4u __attribute__((ext_vector_type(4)));
typedef unsigned v2u __attribute__((ext_vector_type(2)));
typedef float f32x4 __attribute__((ext_vector_type(4)));
typedef short bf16x8 __attribute__((ext_vector_type(8)));
typedef short s16x4 __attribute__((ext_vector_type(4)));
typedef __bf16 bf16x2 __attribute__((ext_vector_type(2)));
typedef unsigned long long u64;
typedef GAS u64 gu64;
#define RLX_AGENT __ATOMIC_RELAXED, __HIP_MEMORY_SCOPE_AGENT

#define NLAUNCH 1
#define OUT_SC1 0
#define WS_AT_END 0
#define TOUCH 0
#define P1_SAME 0
#define POLL_SLEEP 4
#define P0_POLL_DELAY 64
#define CPRIO 2
#define WARM 1
#define RT_STORE 11
#define RT_PUB 18
#define TOUCH_SLEEP 2
#define WT0 3
#define EARLY_W2 2
#define RT_ISSUE 1
#define TOUCH2_SLEEP 36
#define PRO 0
#define PADTO 45
#define PADMASK 0
#define DIAG 0

constexpr int T_ = 2048, D_ = 640, F_ = 1536;
constexpr unsigned WS_NONCE = 0;
constexpr unsigned WS_F0Q = 40960;
constexpr unsigned WS_F0O = 8192;
constexpr unsigned WS_F0A = 12288;
constexpr unsigned WS_F0B = 16384;
constexpr unsigned WS_F0X = 20480;
constexpr unsigned WS_F1 = 24576;
constexpr unsigned WS_F2 = 28672;
constexpr unsigned WS_F3 = 32768;
constexpr unsigned WS_XN = 131072;
constexpr unsigned WS_WQ = WS_XN + 128 * 20480;
constexpr unsigned WS_WO = WS_WQ + 16 * 20 * 8 * 1024;
constexpr unsigned WS_W1 = WS_WO + 8 * 20 * 5 * 1024;
constexpr unsigned WS_W2 = WS_W1 + 16 * 20 * 6 * 1024;
constexpr unsigned WS_O = WS_W2 + 8 * 48 * 5 * 1024;
constexpr unsigned WS_H = WS_O + 128 * 20480;
constexpr unsigned WS_H1 = WS_H + 128 * 20480;
constexpr unsigned WS_ST = WS_H1 + 128 * 49152;
constexpr unsigned WS_END = WS_ST + 128 * 2048;
constexpr unsigned LDS_BYTES = 163840;
constexpr unsigned L_STASH = 153600;
constexpr unsigned L_P0 = 122880;
constexpr unsigned L_TAG = 153600;
constexpr unsigned L_GO = 153616;
#define SHADOW_SLEEP 40
constexpr unsigned P1_STAGE = 40960;
constexpr unsigned L_P1_EX = 102400;
constexpr unsigned P1_RS = 520;
constexpr unsigned L_P1_ST = 144000;
constexpr unsigned L_EXH = 124928;
constexpr unsigned L_P3_B1 = 122880;
constexpr unsigned L_P4_B2 = 145408;
constexpr unsigned L_CNT = 147456;

struct Params {
  const unsigned short* x; const unsigned short* wqkv; const unsigned short* wo; const unsigned short* w1; const unsigned short* b1;
  const unsigned short* w2; const unsigned short* b2; unsigned short* out; unsigned char* ws;
};

typedef __amdgpu_buffer_rsrc_t rsrc_t;
DEVI rsrc_t mk_rsrc(const void* p, unsigned bytes) { return __builtin_amdgcn_make_buffer_rsrc((void*)p, 0, bytes, 0x00020000); }
DEVI v4u mk_srd(const void* p, unsigned bytes) {
  u64 a = (u64)p; v4u r; r.x = (unsigned)a; r.y = (unsigned)(a >> 32) & 0xffffu; r.z = bytes; r.w = 0x00020000u; return r;
}
template <int AUX> DEVI void dma16(rsrc_t rs, LAS char* lds, unsigned voff, unsigned soff) {
  __builtin_amdgcn_raw_ptr_buffer_load_lds(rs, (LAS void*)lds, 16, voff, soff, 0, AUX);
}
DEVI void dma16_asm(v4u srd, unsigned ldsbase, unsigned voff) {
  unsigned keep;
  asm volatile("s_mov_b32 %0, m0\n\ts_mov_b32 m0, %3\n\ts_nop 0\n\tbuffer_load_dwordx4 %1, %2, 0 offen lds\n\ts_mov_b32 m0, %0"
               : "=&s"(keep) : "v"(voff), "s"(srd), "s"(ldsbase) : "memory");
}
DEVI void aload(v4u& d, v4u srd, unsigned voff, unsigned soff) {
  asm volatile("buffer_load_dwordx4 %0, %1, %2, %3 offen sc1" : "=v"(d) : "v"(voff), "s"(srd), "s"(soff) : "memory");
}
DEVI void st16(rsrc_t rs, v4u v, unsigned voff, unsigned soff) { __builtin_amdgcn_raw_buffer_store_b128(v, rs, voff + soff, 0, 16); }
#define WAITV(n) asm volatile("s_waitcnt vmcnt(%0)" ::"i"(n) : "memory")
#define WAITV1(n, a) asm volatile("s_waitcnt vmcnt(%1)" : "+v"(a) : "i"(n) : "memory")
#define WAITV2(n, a, b) asm volatile("s_waitcnt vmcnt(%2)" : "+v"(a), "+v"(b) : "i"(n) : "memory")
#define WAITV4(n, a, b, c, d) asm volatile("s_waitcnt vmcnt(%4)" : "+v"(a), "+v"(b), "+v"(c), "+v"(d) : "i"(n) : "memory")
#define WAITL0() asm volatile("s_waitcnt lgkmcnt(0)" ::: "memory")
#define BAR() __builtin_amdgcn_s_barrier()
#define SCHED0() __builtin_amdgcn_sched_barrier(0)
DEVI void lbar() { WAITL0(); BAR(); asm volatile("" ::: "memory"); }
DEVI void rbar() { asm volatile("" ::: "memory"); BAR(); asm volatile("" ::: "memory"); }

DEVI unsigned pk2(float a, float b) { bf16x2 p; p[0] = (__bf16)a; p[1] = (__bf16)b; return __builtin_bit_cast(unsigned, p); }
DEVI float bflo(unsigned u) { return __uint_as_float(u << 16); }
DEVI float bfhi(unsigned u) { return __uint_as_float(u & 0xffff0000u); }
DEVI bf16x8 asbf(v4u v) { return __builtin_bit_cast(bf16x8, v); }
DEVI f32x4 mfma(v4u a, v4u b, f32x4 c) { return __builtin_amdgcn_mfma_f32_16x16x32_bf16(asbf(a), asbf(b), c, 0, 0, 0); }
DEVI v4u ldsr16(LAS char* p) { return *(LAS v4u*)p; }
DEVI int fresh_lane() { int v; asm volatile("v_mbcnt_lo_u32_b32 %0, -1, 0\n\tv_mbcnt_hi_u32_b32 %0, -1, %0" : "=v"(v)); return v; }
DEVI float fsel(bool c, float a, float b) { asm("" : "+v"(a)); asm("" : "+v"(b)); return c ? a : b; }
DEVI float xrow_max(float v) {
  unsigned u = __float_as_uint(v);
  auto a = __builtin_amdgcn_permlane32_swap(u, u, false, false); v = fmaxf(__uint_as_float(a[0]), __uint_as_float(a[1]));
  u = __float_as_uint(v);
  auto b = __builtin_amdgcn_permlane16_swap(u, u, false, false); return fmaxf(__uint_as_float(b[0]), __uint_as_float(b[1]));
}
DEVI float xrow_sum(float v) {
  unsigned u = __float_as_uint(v);
  auto a = __builtin_amdgcn_permlane32_swap(u, u, false, false); v = __uint_as_float(a[0]) + __uint_as_float(a[1]);
  u = __float_as_uint(v);
  auto b = __builtin_amdgcn_permlane16_swap(u, u, false, false); return __uint_as_float(b[0]) + __uint_as_float(b[1]);
}
DEVI float row16_sum(float v) {
  v += __uint_as_float(__builtin_amdgcn_update_dpp(0u, __float_as_uint(v), 0x128, 0xf, 0xf, false));
  v += __uint_as_float(__builtin_amdgcn_update_dpp(0u, __float_as_uint(v), 0x124, 0xf, 0xf, false));
  v += __uint_as_float(__builtin_amdgcn_update_dpp(0u, __float_as_uint(v), 0x122, 0xf, 0xf, false));
  v += __uint_as_float(__builtin_amdgcn_update_dpp(0u, __float_as_uint(v), 0x121, 0xf, 0xf, false));
  return v;
}

DEVI u64 rfl64(u64 v) { return ((u64)__builtin_amdgcn_readfirstlane((unsigned)(v >> 32)) << 32) | (unsigned)__builtin_amdgcn_readfirstlane((unsigned)v); }
DEVI void publish(unsigned char* ws, unsigned off, u64 tag) { __hip_atomic_store((gu64*)(ws + off), tag, RLX_AGENT); }
DEVI void wait_flags(unsigned char* ws, unsigned laneoff, bool active, u64 tag) {
  for (unsigned sp = 0; sp < (1u << 18); ++sp) {
    u64 v = tag;
    if (active) v = __hip_atomic_load((gu64*)(ws + laneoff), RLX_AGENT);
    if (__all(v == tag)) break;
    __builtin_amdgcn_s_sleep(POLL_SLEEP);
  }
  __builtin_amdgcn_fence(__ATOMIC_ACQUIRE, "wavefront");
  asm volatile("" ::: "memory");
}

DEVI void wait_flags2(unsigned char* ws, unsigned off1, bool act1, unsigned off2, bool act2, u64 tag) {
  for (unsigned sp = 0; sp < (1u << 18); ++sp) {
    u64 v1 = tag, v2 = tag;
    if (act1) v1 = __hip_atomic_load((gu64*)(ws + off1), RLX_AGENT);
    if (act2) v2 = __hip_atomic_load((gu64*)(ws + off2), RLX_AGENT);
    if (__all(v1 == tag && v2 == tag)) break;
    __builtin_amdgcn_s_sleep(POLL_SLEEP);
  }
  __builtin_amdgcn_fence(__ATOMIC_ACQUIRE, "wavefront");
  asm volatile("" ::: "memory");
}

#define STAMP_ARG
#define STAMP_PASS
#define STAMP(id) do {} while (0)

DEVI void w2_unit_issue(const Params& p, LAS char* st, int lane, int a, int ks) {
  const v4u rs = mk_srd(p.w2, 1536u * 1280u);
  const unsigned sb = __builtin_amdgcn_readfirstlane((unsigned)(uintptr_t)st);
#pragma unroll
  for (int n = 0; n < 5; ++n) {
    const int q = n * 64 + lane, rho = (q * 6554) >> 16, ch = q - rho * 10, g = rho >> 3, t = (rho >> 2) & 1, e = rho & 3;
    dma16_asm(rs, sb + n * 1024, (unsigned)(32 * ks + 16 * t + 4 * g + e) * 1280u + 160 * a + 16 * ch);
  }
}
DEVI void w2_unit_store(const Params& p, LAS char* st, int lane, int a, int ks) {
  rsrc_t ws = mk_rsrc(p.ws, WS_END);
  const int kh = ks / 24, s = ks - kh * 24;
  const unsigned dst = WS_W2 + (unsigned)a * 245760 + (s >> 2) * 40960 + kh * 20480 + (s & 3) * 5120;
  const unsigned lo = (unsigned)(uintptr_t)st + (8 * (lane >> 4) + ((lane & 15) >> 2)) * 160 + 8 * (lane & 3);
  v2u a0, a1, a2, a3, a4, b0, b1, b2, b3, b4;
  asm volatile("ds_read_b64_tr_b16 %0, %10\n\tds_read_b64_tr_b16 %5, %10 offset:640\n\tds_read_b64_tr_b16 %1, %10 offset:32\n\tds_read_b64_tr_b16 %6, %10 offset:672\n\t"
               "ds_read_b64_tr_b16 %2, %10 offset:64\n\tds_read_b64_tr_b16 %7, %10 offset:704\n\tds_read_b64_tr_b16 %3, %10 offset:96\n\tds_read_b64_tr_b16 %8, %10 offset:736\n\t"
               "ds_read_b64_tr_b16 %4, %10 offset:128\n\tds_read_b64_tr_b16 %9, %10 offset:768\n\ts_waitcnt lgkmcnt(0)"
               : "=&v"(a0), "=&v"(a1), "=&v"(a2), "=&v"(a3), "=&v"(a4), "=&v"(b0), "=&v"(b1), "=&v"(b2), "=&v"(b3), "=&v"(b4) : "v"(lo) : "memory");
  st16(ws, v4u{a0.x, a0.y, b0.x, b0.y}, lane * 16, dst);
  st16(ws, v4u{a1.x, a1.y, b1.x, b1.y}, lane * 16, dst + 1024);
  st16(ws, v4u{a2.x, a2.y, b2.x, b2.y}, lane * 16, dst + 2048);
  st16(ws, v4u{a3.x, a3.y, b3.x, b3.y}, lane * 16, dst + 3072);
  st16(ws, v4u{a4.x, a4.y, b4.x, b4.y}, lane * 16, dst + 4096);
}
constexpr unsigned L_JUNK = 147520;
DEVI void touch_line(LAS char* lds, const GAS char* a) {
  unsigned keep;
  asm volatile("s_mov_b32 %0, m0\n\ts_mov_b32 m0, %2\n\ts_nop 0\n\tglobal_load_lds_dword %1, off\n\ts_mov_b32 m0, %0" : "=&s"(keep) : "v"(a), "s"((unsigned)(uintptr_t)(lds + L_JUNK)) : "memory");
}
DEVI const GAS char* proj_touch(const Params& p, int n, int lane) {
  int idx = n * 64 + lane; if (idx > 39679) idx = 39679;
  const int s = idx / 3968, rem = idx - s * 3968;
  if (rem < 2048) return (const GAS char*)p.x + (unsigned)rem * 1280u + s * 128;
  const int q = rem - 2048, row = q / 30;
  return (const GAS char*)p.wqkv + (unsigned)(64 * s + row) * 3840u + (q - row * 30) * 128;
}
DEVI const GAS char* weight_touch(const Params& p, int n, int lane) {
  const int idx = n * 64 + lane;
  if (idx < 6400) return (const GAS char*)p.wo + (unsigned)idx * 128u;
  if (idx < 21760) return (const GAS char*)p.w2 + (unsigned)(idx - 6400) * 128u;
  return (const GAS char*)p.w1 + (unsigned)(idx - 21760) * 128u;
}
struct StageTouch { const GAS char* a; unsigned stride; };
DEVI StageTouch stage_touch(const Params& p, int lane) {
  const int x8 = blockIdx.x & 7, r = blockIdx.x >> 3, idx = r * 47 + lane;
  StageTouch t;
  if (lane < 47 && idx < 960) { const int row = (idx * 4370) >> 16, l = idx - row * 15, sec = (l * 52) >> 8;
    t.a = (const GAS char*)p.wqkv + (unsigned)row * 3840u + sec * 1280 + 640 * (x8 & 1) + (l - sec * 5) * 128; t.stride = 64 * 3840; }
  else { int tok = 512 * (x8 >> 1) - 16 + (idx - 960); if (lane >= 47 || idx >= 1488 || tok < 0) tok = 512 * (x8 >> 1);
    t.a = (const GAS char*)p.x + (unsigned)tok * 1280u; t.stride = 128; }
  return t;
}
DEVI v4u trfrag(LAS char* lo, LAS char* hi) {
  const v2u a = __builtin_bit_cast(v2u, __builtin_amdgcn_ds_read_tr16_b64_v4i16((LAS s16x4*)lo));
  const v2u b = __builtin_bit_cast(v2u, __builtin_amdgcn_ds_read_tr16_b64_v4i16((LAS s16x4*)hi));
  return v4u{a.x, a.y, b.x, b.y};
}

DEVI void p1_tail(const Params& p, LAS char* lds, int lane, int w, int tt, int grp, f32x4 (&acc)[2][5], f32x4 st1, f32x4 st2 STAMP_ARG);
DEVI void phase1(const Params& p, LAS char* lds, int lane, int w, u64& tag STAMP_ARG) {
  const int x8 = blockIdx.x & 7, r = blockIdx.x >> 3;
  const int tt = 8 * (x8 >> 1) + (r >> 2), grp = 4 * (x8 & 1) + (r & 3);
  rsrc_t ws = mk_rsrc(p.ws, WS_END);
  const unsigned l16 = lane * 16;
  const int g = lane >> 4, c = lane & 15;
  f32x4 acc[2][5], st1 = {0.f, 0.f, 0.f, 0.f}, st2 = st1;
#pragma unroll
  for (int e = 0; e < 2; ++e)
#pragma unroll
    for (int b = 0; b < 5; ++b) acc[e][b] = f32x4{0.f, 0.f, 0.f, 0.f};
  const unsigned xo0 = c * 128 + ((g ^ (c & 7)) * 16), xo1 = c * 128 + (((4 + g) ^ (c & 7)) * 16);
  const unsigned sblk = (w < 5 ? w : 0) * 2048;
  unsigned wo0, wo1;
  {
    const int p4 = lane & 3, pp = (w >> 2) * 80, m = w & 3;
    wo0 = m == 0 ? 160 + 8 * p4 : m == 1 ? (p4 < 2 ? 160 : 0) + 32 + 40 * (p4 & 1) : m == 2 ? 40 + 8 * p4 : 320 + 40 + 8 * p4;
    wo1 = m == 0 ? 160 + 40 + 8 * p4 : m == 1 ? 8 * p4 : m == 2 ? 320 + 8 * p4 : 320 + 32 + 40 * (p4 & 1);
    const int q4 = (lane & 15) >> 2;
    const unsigned r0 = 10240 + (8 * g + 4 * (g & 1) + q4) * 480 + pp;
    wo0 += r0; wo1 += r0;
  }
  const int hiD = (g & 1) ? -4 * 480 : 4 * 480;
  v4u W[2][2], X[2][6];
#define P1_RO(ks) ((((ks) >> 1) % 3) * P1_STAGE)
#define P1_TR(addr) __builtin_bit_cast(v2u, __builtin_amdgcn_ds_read_tr16_b64_v4i16((LAS s16x4*)(addr)))
#define P1_READ(ks) do { \
    LAS char* wb_ = lds + P1_RO(ks) + ((ks) & 1) * 15360; \
    { v2u l0 = P1_TR(wb_ + wo0), h0 = P1_TR(wb_ + wo0 + hiD), l1 = P1_TR(wb_ + wo1), h1 = P1_TR(wb_ + wo1 + hiD); \
      W[(ks) & 1][0] = v4u{l0.x, l0.y, h0.x, h0.y}; W[(ks) & 1][1] = v4u{l1.x, l1.y, h1.x, h1.y}; } \
    LAS char* xb_ = lds + P1_RO(ks) + (((ks) & 1) ? xo1 : xo0); \
    _Pragma("unroll") for (int b = 0; b < 5; ++b) X[(ks) & 1][b] = ldsr16(xb_ + b * 2048); \
    X[(ks) & 1][5] = ldsr16(xb_ + sblk); } while (0)
  if (WARM && w == 4) { __builtin_amdgcn_s_sleep(TOUCH_SLEEP); touch_line(lds, proj_touch(p, blockIdx.x, lane)); }
  if (WARM && w >= 5) {
    __builtin_amdgcn_s_sleep(TOUCH2_SLEEP);
    if (w == 5) { touch_line(lds, proj_touch(p, blockIdx.x + 256, lane)); if (blockIdx.x + 512 < 620) touch_line(lds, proj_touch(p, blockIdx.x + 512, lane)); }
    else if (w == 6) { touch_line(lds, weight_touch(p, blockIdx.x, lane)); touch_line(lds, weight_touch(p, blockIdx.x + 256, lane)); }
    else if (blockIdx.x + 512 < 580) touch_line(lds, weight_touch(p, blockIdx.x + 512, lane));
  }
  if (w == 3) {
    const u64 nonce = __hip_atomic_load((gu64*)(p.ws + WS_NONCE), RLX_AGENT);
    if (lane == 0) *(LAS u64*)(lds + L_TAG) = nonce * 6364136223846793005ull + 1442695040888963407ull;
  }
  STAMP(2);
  lbar();
  tag = rfl64(*(LAS u64*)(lds + L_TAG));
  P1_READ(0);
  SCHED0();
  const v4u ones = {0x3f803f80u, 0x3f803f80u, 0x3f803f80u, 0x3f803f80u};
#pragma unroll
  for (int ks = 0; ks < 20; ++ks) {
    if ((ks & 1) && ks < 19) lbar();
    if (ks + 1 < 20) P1_READ(ks + 1);
    v4u x0 = X[ks & 1][0];
    if (c == 0) x0 = ones;
#pragma unroll
    for (int b = 0; b < 5; ++b) {
      const v4u xb = b == 0 ? x0 : X[ks & 1][b];
      acc[0][b] = mfma(W[ks & 1][0], xb, acc[0][b]); acc[1][b] = mfma(W[ks & 1][1], xb, acc[1][b]);
    }
    st1 = mfma(ones, X[ks & 1][5], st1); st2 = mfma(X[ks & 1][5], X[ks & 1][5], st2);
    __builtin_amdgcn_sched_group_barrier(0x100, 4, 0);
#pragma unroll
    for (int b = 0; b < 6; ++b) { __builtin_amdgcn_sched_group_barrier(0x8, 2, 0); __builtin_amdgcn_sched_group_barrier(0x100, 1, 0); }
    SCHED0();
  }
#undef P1_RO
#undef P1_TR
#undef P1_READ
  STAMP(3);
  p1_tail(p, lds, fresh_lane(), w, tt, grp, acc, st1, st2 STAMP_PASS);
}
DEVI void p1_tail(const Params& p, LAS char* lds, int lane, int w, int tt, int grp, f32x4 (&acc)[2][5], f32x4 st1, f32x4 st2 STAMP_ARG) {
  rsrc_t ws = mk_rsrc(p.ws, WS_END);
  const unsigned l16 = lane * 16;
  const int g = lane >> 4, c = lane & 15;
  if (w < 5) {
    const float mean = st1[0] * (1.f / 640.f);
    const float e2 = fsel((c & 2) != 0, fsel((c & 1) != 0, st2[3], st2[2]), fsel((c & 1) != 0, st2[1], st2[0])) * (1.f / 640.f);
    const float rstd = __builtin_amdgcn_rsqf(fmaxf(e2 - mean * mean, 0.f) + 1e-5f);
    if (g == (c >> 2)) *(LAS v2u*)(lds + L_P1_ST + (w * 16 + c) * 8) = v2u{__float_as_uint(rstd), __float_as_uint(-mean * rstd)};
  }
  lbar();
  {
    v2u sv[5];
#pragma unroll
    for (int b = 0; b < 5; ++b) sv[b] = *(LAS v2u*)(lds + L_P1_ST + (b * 16 + c) * 8);
    float cs[2][4];
#pragma unroll
    for (int e = 0; e < 2; ++e)
#pragma unroll
      for (int q = 0; q < 4; ++q) cs[e][q] = __uint_as_float(__builtin_amdgcn_update_dpp(0u, __float_as_uint(acc[e][0][q]), 0x150, 0xf, 0xf, false));
    LAS char* d = lds + L_P1_EX + c * P1_RS + w * 64 + g * 8;
#pragma unroll
    for (int b = 0; b < 5; ++b) {
      const float rs = __uint_as_float(sv[b].x), mr = __uint_as_float(sv[b].y);
#pragma unroll
      for (int e = 0; e < 2; ++e)
        *(LAS v2u*)(d + b * 16 * P1_RS + e * 32) = v2u{pk2(fmaf(rs, acc[e][b][0], mr * cs[e][0]), fmaf(rs, acc[e][b][1], mr * cs[e][1])),
                                                       pk2(fmaf(rs, acc[e][b][2], mr * cs[e][2]), fmaf(rs, acc[e][b][3], mr * cs[e][3]))};
    }
  }
  if (DIAG != 4) STAMP(22);
  lbar();
  if (DIAG != 4) STAMP(23);
  const int blk = 4 * tt + (w & 3), pr = 2 * grp + (w >> 2);
  LAS char* own = lds + L_P1_EX + (((w & 3) + 1) * 16 + c) * P1_RS + (w >> 2) * 256 + g * 8;
  LAS char* trp = lds + L_P1_EX + (((w & 3) + 1) * 16 + 4 * g + (c >> 2)) * P1_RS + (w >> 2) * 256 + (c & 3) * 8;
  v2u ka[2], kp[2], qa[2], qr[2], va[2], vp[2];
  const v2u kr = *(LAS v2u*)(own + 64), krp = *(LAS v2u*)(own + 64 - 16 * P1_RS);
#pragma unroll
  for (int hh = 0; hh < 2; ++hh) {
    ka[hh] = *(LAS v2u*)(own + hh * 32); kp[hh] = *(LAS v2u*)(own + hh * 32 - 16 * P1_RS);
    qa[hh] = *(LAS v2u*)(own + (3 + hh) * 32);
    qr[hh] = *(LAS v2u*)(own - g * 8 + 64 + 16 + hh * 8);
    va[hh] = __builtin_bit_cast(v2u, __builtin_amdgcn_ds_read_tr16_b64_v4i16((LAS s16x4*)(trp + (5 + hh) * 32)));
    vp[hh] = __builtin_bit_cast(v2u, __builtin_amdgcn_ds_read_tr16_b64_v4i16((LAS s16x4*)(trp + (5 + hh) * 32 - 16 * P1_RS)));
  }
  const v2u vr = __builtin_bit_cast(v2u, __builtin_amdgcn_ds_read_tr16_b64_v4i16((LAS s16x4*)(trp + 7 * 32)));
  const v2u vrp = __builtin_bit_cast(v2u, __builtin_amdgcn_ds_read_tr16_b64_v4i16((LAS s16x4*)(trp + 7 * 32 - 16 * P1_RS)));
  if (DIAG != 4) STAMP(24);
  SCHED0();
  const int odd = c & 1, ce = c - odd;
  const bool havePrev = (tt > 0) || ((w & 3) > 0);
  const bool v1a = havePrev && (4 * g >= ce + 2), v1b = havePrev && (4 * g >= ce), v2a = ce >= 4 * g, v2b = ce - 2 >= 4 * g;
  const float c2 = 0.22360679774997896f * 1.4426950408889634f, NEG = -3.0e38f;
  const f32x4 z = {0.f, 0.f, 0.f, 0.f};
  f32x4 S1[2], S2[2];
#pragma unroll
  for (int hh = 0; hh < 2; ++hh) {
    const bool mine = g == hh;
    const v4u Q = {qa[hh].x, qa[hh].y, mine ? qr[hh].x : 0u, mine ? qr[hh].y : 0u};
    S1[hh] = mfma(v4u{kp[hh].x, kp[hh].y, krp.x, krp.y}, Q, z);
    S2[hh] = mfma(v4u{ka[hh].x, ka[hh].y, kr.x, kr.y}, Q, z);
  }
  float s1a[2], s1b[2], s2a[2], s2b[2], m[2], inv[2];
#pragma unroll
  for (int hh = 0; hh < 2; ++hh) {
    s1a[hh] = fsel(v1a, fsel(odd, S1[hh][1], S1[hh][0]), NEG); s1b[hh] = fsel(v1b, fsel(odd, S1[hh][3], S1[hh][2]), NEG);
    s2a[hh] = fsel(v2a, fsel(odd, S2[hh][1], S2[hh][0]), NEG); s2b[hh] = fsel(v2b, fsel(odd, S2[hh][3], S2[hh][2]), NEG);
    m[hh] = fmaxf(fmaxf(s1a[hh], s1b[hh]), fmaxf(s2a[hh], s2b[hh]));
  }
  {
    auto a0 = __builtin_amdgcn_permlane32_swap(__float_as_uint(m[0]), __float_as_uint(m[0]), false, false);
    auto a1 = __builtin_amdgcn_permlane32_swap(__float_as_uint(m[1]), __float_as_uint(m[1]), false, false);
    m[0] = fmaxf(__uint_as_float(a0[0]), __uint_as_float(a0[1])); m[1] = fmaxf(__uint_as_float(a1[0]), __uint_as_float(a1[1]));
    auto b0 = __builtin_amdgcn_permlane16_swap(__float_as_uint(m[0]), __float_as_uint(m[0]), false, false);
    auto b1 = __builtin_amdgcn_permlane16_swap(__float_as_uint(m[1]), __float_as_uint(m[1]), false, false);
    m[0] = fmaxf(__uint_as_float(b0[0]), __uint_as_float(b0[1])); m[1] = fmaxf(__uint_as_float(b1[0]), __uint_as_float(b1[1]));
  }
#pragma unroll
  for (int hh = 0; hh < 2; ++hh) {
    s1a[hh] = __builtin_amdgcn_exp2f((s1a[hh] - m[hh]) * c2); s1b[hh] = __builtin_amdgcn_exp2f((s1b[hh] - m[hh]) * c2);
    s2a[hh] = __builtin_amdgcn_exp2f((s2a[hh] - m[hh]) * c2); s2b[hh] = __builtin_amdgcn_exp2f((s2b[hh] - m[hh]) * c2);
    inv[hh] = (s1a[hh] + s1b[hh]) + (s2a[hh] + s2b[hh]);
  }
  {
    auto a0 = __builtin_amdgcn_permlane32_swap(__float_as_uint(inv[0]), __float_as_uint(inv[0]), false, false);
    auto a1 = __builtin_amdgcn_permlane32_swap(__float_as_uint(inv[1]), __float_as_uint(inv[1]), false, false);
    inv[0] = __uint_as_float(a0[0]) + __uint_as_float(a0[1]); inv[1] = __uint_as_float(a1[0]) + __uint_as_float(a1[1]);
    auto b0 = __builtin_amdgcn_permlane16_swap(__float_as_uint(inv[0]), __float_as_uint(inv[0]), false, false);
    auto b1 = __builtin_amdgcn_permlane16_swap(__float_as_uint(inv[1]), __float_as_uint(inv[1]), false, false);
    inv[0] = __builtin_amdgcn_rcpf(__uint_as_float(b0[0]) + __uint_as_float(b0[1])); inv[1] = __builtin_amdgcn_rcpf(__uint_as_float(b1[0]) + __uint_as_float(b1[1]));
  }
  if (DIAG != 4) STAMP(25);
  f32x4 OA[2], OR[2];
  const unsigned sh = 16 * odd;
#pragma unroll
  for (int hh = 0; hh < 2; ++hh) {
    const v4u P = {pk2(s1a[hh] * inv[hh], 0.f) << sh, pk2(s1b[hh] * inv[hh], 0.f) << sh, pk2(s2a[hh] * inv[hh], 0.f) << sh, pk2(s2b[hh] * inv[hh], 0.f) << sh};
    OA[hh] = mfma(v4u{vp[hh].x, vp[hh].y, va[hh].x, va[hh].y}, P, z);
    OR[hh] = mfma(v4u{vrp.x, vrp.y, vr.x, vr.y}, P, z);
  }
  {
    v4u om = {pk2(OA[0][0], OA[0][1]), pk2(OA[0][2], OA[0][3]), pk2(OA[1][0], OA[1][1]), pk2(OA[1][2], OA[1][3])};
    st16(ws, om, l16, WS_O + (unsigned)blk * 20480 + pr * 1024);
    unsigned r0x = pk2(OR[0][0], OR[0][1]), r0y = pk2(OR[0][2], OR[0][3]), r1x = pk2(OR[1][0], OR[1][1]), r1y = pk2(OR[1][2], OR[1][3]);
    auto t0 = __builtin_amdgcn_permlane16_swap(r1x, r1x, false, false); auto t1 = __builtin_amdgcn_permlane16_swap(r1y, r1y, false, false);
    v4u orr = {r0x, r0y, t0[1], t1[1]};
    if (lane < 16) st16(ws, orr, l16, WS_O + (unsigned)blk * 20480 + (16 + (pr >> 2)) * 1024 + (pr & 3) * 256);
  }
  STAMP(4);
}

DEVI void exchange_halves(LAS char* lds, const f32x4 (&acc)[5], f32x4 (&fin)[3], int lane, int w) {
  LAS char* d = lds + L_EXH + (w & 3) * 5120 + lane * 16;
  if (w < 4) { *(LAS f32x4*)(d + 3072) = acc[3]; *(LAS f32x4*)(d + 4096) = acc[4]; }
  else { *(LAS f32x4*)(d) = acc[0]; *(LAS f32x4*)(d + 1024) = acc[1]; *(LAS f32x4*)(d + 2048) = acc[2]; }
  lbar();
  if (w < 4) { f32x4 u0 = *(LAS f32x4*)(d), u1 = *(LAS f32x4*)(d + 1024), u2 = *(LAS f32x4*)(d + 2048); fin[0] = acc[0] + u0; fin[1] = acc[1] + u1; fin[2] = acc[2] + u2; }
  else { f32x4 u0 = *(LAS f32x4*)(d + 3072), u1 = *(LAS f32x4*)(d + 4096); fin[0] = acc[3] + u0; fin[1] = acc[4] + u1; fin[2] = f32x4{0.f, 0.f, 0.f, 0.f}; }
}

DEVI void phase2(const Params& p, LAS char* lds, int lane, int w STAMP_ARG) {
  const int x8 = blockIdx.x & 7, r = blockIdx.x >> 3;
  const int ti = 2 * x8 + (r >> 4), hb = (r >> 3) & 1, j = r & 7;
  rsrc_t ws = mk_rsrc(p.ws, WS_END);
  const v4u srd = mk_srd(p.ws, WS_END);
  const unsigned l16 = lane * 16;
  const int b = w & 3, kh = w >> 2, blk = 8 * ti + 4 * hb + b;
  rbar();
  STAMP(6);
  v4u a[10];
  const unsigned oa = WS_O + (unsigned)blk * 20480 + 10 * kh * 1024;
#pragma unroll
  for (int s = 0; s < 10; ++s) aload(a[s], srd, l16, oa + s * 1024);
  f32x4 acc[5];
#pragma unroll
  for (int t = 0; t < 5; ++t) acc[t] = f32x4{0.f, 0.f, 0.f, 0.f};
  v4u B[2][5];
  LAS char* bb = lds + kh * 51200 + l16;
#pragma unroll
  for (int t = 0; t < 5; ++t) B[0][t] = ldsr16(bb + t * 1024);
  SCHED0();
#pragma unroll
  for (int s = 0; s < 10; ++s) {
    if (s == 5) lbar();
    asm volatile("s_waitcnt vmcnt(%1)" : "+v"(a[s]) : "i"(9 - s) : "memory");
    if (s + 1 < 10) {
#pragma unroll
      for (int t = 0; t < 5; ++t) B[(s + 1) & 1][t] = ldsr16(bb + (s + 1) * 5120 + t * 1024);
    }
#pragma unroll
    for (int t = 0; t < 5; ++t) acc[t] = mfma(B[s & 1][t], a[s], acc[t]);
#pragma unroll
    for (int t = 0; t < 5; ++t) { __builtin_amdgcn_sched_group_barrier(0x100, 1, 0); __builtin_amdgcn_sched_group_barrier(0x8, 1, 0); }
    SCHED0();
  }
  STAMP(7);
  const int g = lane >> 4, c = lane & 15, row = 16 * b + c, mt0 = 3 * kh;
  LAS char* sx = lds + L_STASH + row * 160 + mt0 * 32 + g * 8;
  v2u xv[3];
#pragma unroll
  for (int e = 0; e < 3; ++e) xv[e] = *(LAS v2u*)(sx + (e < 2 || kh == 0 ? e : 0) * 32);
  f32x4 fin[3];
  exchange_halves(lds, acc, fin, lane, w);
  v2u hv[3];
  float ssum = 0.f, ssq = 0.f;
#pragma unroll
  for (int e = 0; e < 3; ++e) {
    hv[e] = v2u{pk2(fin[e][0] + bflo(xv[e].x), fin[e][1] + bfhi(xv[e].x)), pk2(fin[e][2] + bflo(xv[e].y), fin[e][3] + bfhi(xv[e].y))};
    if (e < 2 || kh == 0) {
      *(LAS v2u*)(sx + e * 32) = hv[e];
      const float h0 = bflo(hv[e].x), h1 = bfhi(hv[e].x), h2 = bflo(hv[e].y), h3 = bfhi(hv[e].y);
      ssum += (h0 + h1) + (h2 + h3); ssq += fmaf(h0, h0, h1 * h1) + fmaf(h2, h2, h3 * h3);
    }
  }
  {
    auto sx0 = __builtin_amdgcn_permlane16_swap(hv[0].x, hv[1].x, false, false); auto sy0 = __builtin_amdgcn_permlane16_swap(hv[0].y, hv[1].y, false, false);
    const int G = 2 * (mt0 + (g & 1)) + (g >> 1);
    st16(ws, v4u{sx0[0], sy0[0], sx0[1], sy0[1]}, c * 16 + (10 * j + G) * 256, WS_H + (unsigned)blk * 20480);
    if (kh == 0) __builtin_amdgcn_raw_buffer_store_b64(hv[2], ws, WS_H + (unsigned)blk * 20480 + (10 * j + 4 + (g >> 1)) * 256 + c * 16 + (g & 1) * 8, 0, 16);
  }
  ssum = xrow_sum(ssum); ssq = xrow_sum(ssq);
  {
    const int idx = 2 * j + kh;
    const int e = idx & 3;
    if (lane < 16) {
      v2u sv = {__float_as_uint(ssum), __float_as_uint(ssq)};
      __builtin_amdgcn_raw_buffer_store_b64(sv, ws, WS_ST + (unsigned)blk * 2048 + (e >> 1) * 1024 + (idx >> 2) * 256 + c * 16 + (e & 1) * 8, 0, 16);
    }
  }
  STAMP(8);
}

#define P3D 8
DEVI void phase3(const Params& p, LAS char* lds, int lane, int w STAMP_ARG) {
  const int x8 = blockIdx.x & 7, r = blockIdx.x >> 3;
  const int ti = 4 * (x8 >> 1) + (r >> 3), jj = 8 * (x8 & 1) + (r & 7);
  rsrc_t ws = mk_rsrc(p.ws, WS_END);
  const v4u srd = mk_srd(p.ws, WS_END);
  const unsigned l16 = lane * 16;
  const int blk = 8 * ti + w;
  rbar();
  if (w < 6) {
    v4u cf[20];
#pragma unroll
    for (int ks = 0; ks < 20; ++ks) cf[ks] = ldsr16(lds + (ks * 6 + w) * 1024 + l16);
    SCHED0();
    f32x4 cs0 = {0.f, 0.f, 0.f, 0.f}, cs1 = cs0;
    const v4u ones = {0x3f803f80u, 0x3f803f80u, 0x3f803f80u, 0x3f803f80u};
#pragma unroll
    for (int ks = 0; ks < 20; ks += 2) { cs0 = mfma(cf[ks], ones, cs0); cs1 = mfma(cf[ks + 1], ones, cs1); }
    if ((lane & 15) == 0) *(LAS f32x4*)(lds + L_P3_B1 + 1024 + (w * 16 + (lane >> 4) * 4) * 4) = cs0 + cs1;
  }
  lbar();
  STAMP(10);
  v4u a[20], st0, st1;
  const unsigned ha = WS_H + (unsigned)blk * 20480;
#pragma unroll
  for (int s = 0; s < P3D; ++s) aload(a[s], srd, l16, ha + s * 1024);
  f32x4 acc[6];
#pragma unroll
  for (int t = 0; t < 6; ++t) acc[t] = f32x4{0.f, 0.f, 0.f, 0.f};
  v4u B[2][6];
  LAS char* bb = lds + l16;
#define P3_RD(s, t) ldsr16(bb + (s) * 6144 + (t) * 1024)
#pragma unroll
  for (int t = 0; t < 6; ++t) B[0][t] = P3_RD(0, t);
  SCHED0();
#pragma unroll
  for (int s = 0; s < 20; ++s) {
    if (s == 6 || s == 13) lbar();
    asm volatile("s_waitcnt vmcnt(%1)" : "+v"(a[s]) : "i"(s + P3D - 1 < 22 ? P3D - 1 : 21 - s) : "memory");
    if (s + P3D < 20) aload(a[s + P3D], srd, l16, ha + (s + P3D) * 1024);
    else if (s + P3D == 20) aload(st0, srd, l16, WS_ST + (unsigned)blk * 2048);
    else if (s + P3D == 21) aload(st1, srd, l16, WS_ST + (unsigned)blk * 2048 + 1024);
    if (s + 1 < 20) {
#pragma unroll
      for (int t = 0; t < 6; ++t) B[(s + 1) & 1][t] = P3_RD(s + 1, t);
    }
#pragma unroll
    for (int t = 0; t < 6; ++t) acc[t] = mfma(B[s & 1][t], a[s], acc[t]);
#pragma unroll
    for (int t = 0; t < 6; ++t) { __builtin_amdgcn_sched_group_barrier(0x100, 1, 0); __builtin_amdgcn_sched_group_barrier(0x8, 1, 0); }
    SCHED0();
  }
#undef P3_RD
  lbar();
  STAMP(11);
  const int g = lane >> 4;
  f32x4 cs[6]; v2u bb1[6];
#pragma unroll
  for (int t = 0; t < 6; ++t) { cs[t] = *(LAS f32x4*)(lds + L_P3_B1 + 1024 + (t * 16 + g * 4) * 4); bb1[t] = *(LAS v2u*)(lds + L_P3_B1 + (t * 16 + g * 4) * 2); }
  WAITV2(0, st0, st1);
  SCHED0();
  float s1 = (__uint_as_float(st0[0]) + __uint_as_float(st0[2])) + (__uint_as_float(st1[0]) + __uint_as_float(st1[2]));
  float s2 = (__uint_as_float(st0[1]) + __uint_as_float(st0[3])) + (__uint_as_float(st1[1]) + __uint_as_float(st1[3]));
  s1 = xrow_sum(s1); s2 = xrow_sum(s2);
  const float mean = s1 * (1.f / 640.f);
  const float var = fmaxf(s2 * (1.f / 640.f) - mean * mean, 0.f);
  const float rstd = __builtin_amdgcn_rsqf(var + 1e-5f), mr = -mean * rstd;
  unsigned o[12];
#pragma unroll
  for (int t = 0; t < 6; ++t) {
    float y0 = fmaxf(fmaf(rstd, acc[t][0], fmaf(mr, cs[t][0], bflo(bb1[t].x))), 0.f), y1 = fmaxf(fmaf(rstd, acc[t][1], fmaf(mr, cs[t][1], bfhi(bb1[t].x))), 0.f);
    float y2 = fmaxf(fmaf(rstd, acc[t][2], fmaf(mr, cs[t][2], bflo(bb1[t].y))), 0.f), y3 = fmaxf(fmaf(rstd, acc[t][3], fmaf(mr, cs[t][3], bfhi(bb1[t].y))), 0.f);
    o[2 * t] = pk2(y0 * y0, y1 * y1); o[2 * t + 1] = pk2(y2 * y2, y3 * y3);
  }
#pragma unroll
  for (int q = 0; q < 3; ++q) st16(ws, v4u{o[4 * q], o[4 * q + 1], o[4 * q + 2], o[4 * q + 3]}, l16, WS_H1 + (unsigned)blk * 49152 + (3 * jj + q) * 1024);
  STAMP(12);
}

#define P4D 8
DEVI void phase4(const Params& p, LAS char* lds, int lane, int w STAMP_ARG) {
  const int x8 = blockIdx.x & 7, r = blockIdx.x >> 3;
  const int ti = 2 * x8 + (r >> 4), hb = (r >> 3) & 1, j = r & 7;
  const v4u srd = mk_srd(p.ws, WS_END);
  const unsigned l16 = lane * 16;
  const int b = w & 3, kh = w >> 2, blk = 8 * ti + 4 * hb + b;
  rbar();
  STAMP(14);
  const unsigned ha = WS_H1 + (unsigned)blk * 49152 + kh * 24 * 1024;
  v4u a[24];
#pragma unroll
  for (int s = 0; s < P4D; ++s) aload(a[s], srd, l16, ha + s * 1024);
  f32x4 acc[5];
#pragma unroll
  for (int t = 0; t < 5; ++t) acc[t] = f32x4{0.f, 0.f, 0.f, 0.f};
  v4u B[2][5];
  LAS char* bb = lds + kh * 20480 + l16;
#define P4_READ(s) do { _Pragma("unroll") for (int t = 0; t < 5; ++t) B[(s) & 1][t] = ldsr16(bb + (((s) >> 2) % 3) * 40960 + ((s) & 3) * 5120 + t * 1024); } while (0)
  P4_READ(0);
  SCHED0();
#pragma unroll
  for (int s = 0; s < 24; ++s) {
    if ((s & 3) == 3 && s < 23) lbar();
    asm volatile("s_waitcnt vmcnt(%1)" : "+v"(a[s]) : "i"(s + P4D - 1 < 24 ? P4D - 1 : 23 - s) : "memory");
    if (s + P4D < 24) aload(a[s + P4D], srd, l16, ha + (s + P4D) * 1024);
    if (s + 1 < 24) P4_READ(s + 1);
#pragma unroll
    for (int t = 0; t < 5; ++t) acc[t] = mfma(B[s & 1][t], a[s], acc[t]);
#pragma unroll
    for (int t = 0; t < 5; ++t) { __builtin_amdgcn_sched_group_barrier(0x100, 1, 0); __builtin_amdgcn_sched_group_barrier(0x8, 1, 0); }
    SCHED0();
  }
#undef P4_READ
  STAMP(15);
  const int g = lane >> 4, c = lane & 15, row = 16 * b + c, mt0 = 3 * kh;
  LAS char* sx = lds + L_STASH + row * 160 + mt0 * 32 + g * 8;
  v2u hv[3], bv[3];
#pragma unroll
  for (int e = 0; e < 3; ++e) {
    const int ee = (e < 2 || kh == 0) ? e : 0;
    hv[e] = *(LAS v2u*)(sx + ee * 32);
    bv[e] = *(LAS v2u*)(lds + L_P4_B2 + (mt0 + ee) * 32 + g * 8);
  }
  f32x4 fin[3];
  exchange_halves(lds, acc, fin, lane, w);
#pragma unroll
  for (int e = 0; e < 3; ++e) {
    if (e < 2 || kh == 0)
      *(LAS v2u*)(sx + e * 32) = v2u{pk2(fin[e][0] + bflo(bv[e].x) + bflo(hv[e].x), fin[e][1] + bfhi(bv[e].x) + bfhi(hv[e].x)),
                                     pk2(fin[e][2] + bflo(bv[e].y) + bflo(hv[e].y), fin[e][3] + bfhi(bv[e].y) + bfhi(hv[e].y))};
  }
  lbar();
  {
    const int q0 = w * 64 + lane, q1 = q0 + 512;
    const v4u o0 = *(LAS v4u*)(lds + L_STASH + q0 * 16);
    v4u o1 = o0; if (w < 2) o1 = *(LAS v4u*)(lds + L_STASH + q1 * 16);
    const int r0 = q0 / 10, r1 = q1 / 10;
    rsrc_t outr = mk_rsrc(p.out, T_ * 1280);
    const unsigned ob = (unsigned)(128 * ti + 64 * hb) * 1280 + 160 * j;
    __builtin_amdgcn_raw_buffer_store_b128(o0, outr, ob + r0 * 1280 + (q0 - r0 * 10) * 16, 0, 0);
    if (w < 2) __builtin_amdgcn_raw_buffer_store_b128(o1, outr, ob + r1 * 1280 + (q1 - r1 * 10) * 16, 0, 0);
  }
  STAMP(16);
}

DEVI unsigned ldsaddr(LAS char* p) { return (unsigned)(uintptr_t)p; }
DEVI void convert5(LAS char* u, int lane) {
  const int g = lane >> 4;
  const unsigned lo = ldsaddr(u) + (8 * g + 4 * (g & 1) + ((lane & 15) >> 2)) * 160 + (lane & 3) * 8, hi = lo + ((g & 1) ? -640 : 640), d = ldsaddr(u) + lane * 16;
  v2u a0, a1, a2, a3, a4, b0, b1, b2, b3, b4;
  asm volatile("ds_read_b64_tr_b16 %0, %10\n\tds_read_b64_tr_b16 %5, %11\n\tds_read_b64_tr_b16 %1, %10 offset:32\n\tds_read_b64_tr_b16 %6, %11 offset:32\n\t"
               "ds_read_b64_tr_b16 %2, %10 offset:64\n\tds_read_b64_tr_b16 %7, %11 offset:64\n\tds_read_b64_tr_b16 %3, %10 offset:96\n\tds_read_b64_tr_b16 %8, %11 offset:96\n\t"
               "ds_read_b64_tr_b16 %4, %10 offset:128\n\tds_read_b64_tr_b16 %9, %11 offset:128\n\ts_waitcnt lgkmcnt(0)"
               : "=&v"(a0), "=&v"(a1), "=&v"(a2), "=&v"(a3), "=&v"(a4), "=&v"(b0), "=&v"(b1), "=&v"(b2), "=&v"(b3), "=&v"(b4) : "v"(lo), "v"(hi) : "memory");
  const v4u f0 = {a0.x, a0.y, b0.x, b0.y}, f1 = {a1.x, a1.y, b1.x, b1.y}, f2 = {a2.x, a2.y, b2.x, b2.y}, f3 = {a3.x, a3.y, b3.x, b3.y}, f4 = {a4.x, a4.y, b4.x, b4.y};
  asm volatile("ds_write_b128 %0, %1\n\tds_write_b128 %0, %2 offset:1024\n\tds_write_b128 %0, %3 offset:2048\n\tds_write_b128 %0, %4 offset:3072\n\tds_write_b128 %0, %5 offset:4096\n\ts_waitcnt lgkmcnt(0)"
               ::"v"(d), "v"(f0), "v"(f1), "v"(f2), "v"(f3), "v"(f4) : "memory");
}
DEVI void convert6(LAS char* u, int lane) {
  const int g = lane >> 4, od = g & 1;
  const unsigned row = ldsaddr(u) + (8 * g + ((lane & 15) >> 2)) * 192 + (lane & 3) * 8, lo = row + 32 * od, l5 = row + (od ? 0 : 160), d = ldsaddr(u) + lane * 16;
  v2u a0, a1, a2, a3, a4, a5, b0, b1, b2, b3, b4, b5;
  asm volatile("ds_read_b64_tr_b16 %0, %12\n\tds_read_b64_tr_b16 %6, %12 offset:768\n\tds_read_b64_tr_b16 %1, %12 offset:32\n\tds_read_b64_tr_b16 %7, %12 offset:800\n\t"
               "ds_read_b64_tr_b16 %2, %12 offset:64\n\tds_read_b64_tr_b16 %8, %12 offset:832\n\tds_read_b64_tr_b16 %3, %12 offset:96\n\tds_read_b64_tr_b16 %9, %12 offset:864\n\t"
               "ds_read_b64_tr_b16 %4, %12 offset:128\n\tds_read_b64_tr_b16 %10, %12 offset:896\n\tds_read_b64_tr_b16 %5, %13\n\tds_read_b64_tr_b16 %11, %13 offset:768\n\ts_waitcnt lgkmcnt(0)"
               : "=&v"(a0), "=&v"(a1), "=&v"(a2), "=&v"(a3), "=&v"(a4), "=&v"(a5), "=&v"(b0), "=&v"(b1), "=&v"(b2), "=&v"(b3), "=&v"(b4), "=&v"(b5) : "v"(lo), "v"(l5) : "memory");
  const v4u f0 = {a0.x, a0.y, b0.x, b0.y}, f1 = {a1.x, a1.y, b1.x, b1.y}, f2 = {a2.x, a2.y, b2.x, b2.y}, f3 = {a3.x, a3.y, b3.x, b3.y}, f4 = {a4.x, a4.y, b4.x, b4.y}, f5 = {a5.x, a5.y, b5.x, b5.y};
  asm volatile("ds_write_b128 %0, %1\n\tds_write_b128 %0, %2 offset:1024\n\tds_write_b128 %0, %3 offset:2048\n\tds_write_b128 %0, %4 offset:3072\n\tds_write_b128 %0, %5 offset:4096\n\t"
               "ds_write_b128 %0, %6 offset:5120\n\ts_waitcnt lgkmcnt(0)"
               ::"v"(d), "v"(f0), "v"(f1), "v"(f2), "v"(f3), "v"(f4), "v"(f5) : "memory");
}

DEVI void loader(const Params& p, LAS char* lds, int lane, int lw STAMP_ARG) {
  const int x8 = blockIdx.x & 7, r = blockIdx.x >> 3;
  const int t2d = 4 * (x8 >> 1) + (r >> 3), c2d = 8 * (x8 & 1) + (r & 7);
  const int tl = 2 * x8 + (r >> 4), hb = (r >> 3) & 1, j = r & 7;
  rsrc_t ws = mk_rsrc(p.ws, WS_END);
  const v4u srd = mk_srd(p.ws, WS_END);
  const unsigned l16 = lane * 16;
  const int tt = 8 * (x8 >> 1) + (r >> 2), grp = 4 * (x8 & 1) + (r & 3);
  rsrc_t xs = mk_rsrc(p.x, T_ * 1280), wqs = mk_rsrc(p.wqkv, 640u * 3840u);
  unsigned vx[3], vw[8];
#pragma unroll
  for (int i = 0; i < 3; ++i) {
    const int m = lw + 4 * i, row = 8 * m + (lane >> 3);
    int tok = 64 * tt - 16 + row; if (tok < 0) tok = 0; if (tok > T_ - 1) tok = T_ - 1;
    vx[i] = (unsigned)tok * 1280 + (((lane & 7) ^ (row & 7)) * 16);
  }
  const int wn0 = lw < 2 ? lw * 7 - 1 : 14 + (lw - 2) * 8;
#pragma unroll
  for (int i = 0; i < 8; ++i) {
    int n = wn0 + i; if (n < 0) n = 0;
    const int q = n * 64 + lane, rho = (q * 2185) >> 16, ch = q - rho * 30, sec = (ch * 205) >> 11;
    const int gg = (rho >> 3) & 3, krow = (rho & 32) + 8 * gg + 4 * (((rho >> 2) & 1) ^ (gg & 1)) + (rho & 3);
    vw[i] = (unsigned)krow * 3840u + sec * 1280 + 160 * grp + 16 * (ch - sec * 10);
  }
#define P1_STG(s) do { LAS char* sb_ = lds + ((s) % 3) * P1_STAGE; \
    dma16<0>(xs, sb_ + lw * 1024, vx[0], (s) * 128); dma16<0>(xs, sb_ + (lw + 4) * 1024, vx[1], (s) * 128); \
    if (lw < 2) dma16<0>(xs, sb_ + (lw + 8) * 1024, vx[2], (s) * 128); else dma16<0>(wqs, sb_ + 10240 + wn0 * 1024, vw[0], (s) * 64 * 3840); \
    _Pragma("unroll") for (int i = 1; i < 8; ++i) dma16<0>(wqs, sb_ + 10240 + (wn0 + i) * 1024, vw[i], (s) * 64 * 3840); } while (0)
  STAMP(17);
  P1_STG(0);
  STAMP(18);
  if (PRO == 0) P1_STG(1);
  STAMP(19);
  if (PRO == 0) WAITV(10); else WAITV(0);
  STAMP(20);
  rbar();
  const u64 tag = rfl64(*(LAS u64*)(lds + L_TAG));
  if (lw == 0 && lane < 4) *((LAS unsigned*)(lds + L_CNT) + lane) = 0u;
  STAMP(2);
  if (PRO == 1) P1_STG(1);
  P1_STG(2);
  rsrc_t wos = mk_rsrc(p.wo, 640u * 1280u), w1s = mk_rsrc(p.w1, 640u * 3072u);
  unsigned vA[5], vB[5], vD[6];
#pragma unroll
  for (int i = 0; i < 5; ++i) {
    const int q = 64 * i + lane, rho = (q * 6554) >> 16, cc = q - rho * 10, g = rho >> 3, t = ((rho >> 2) & 1) ^ (g & 1), e = rho & 3;
    const unsigned col = 160 * j + 16 * cc;
    vA[i] = (unsigned)(20 * t + 4 * g + e) * 1280u + col;
    vB[i] = (unsigned)(40 * g + 20 * t + 16 + e) * 1280u + col;
  }
#pragma unroll
  for (int i = 0; i < 6; ++i) {
    const int q = 64 * i + lane, rho = (q * 5462) >> 16, pos = q - rho * 12, cc = ((rho >> 3) & 1) ? (pos < 2 ? pos + 10 : pos - 2) : pos;
    vD[i] = (unsigned)rho * 3072u + 192 * c2d + 16 * cc;
  }
#define WO_KS(ks) do { _Pragma("unroll") for (int i = 0; i < 5; ++i) {     if ((ks) < 16) dma16<0>(wos, lds + (ks) * 5120 + i * 1024, vA[i], (ks) * 40 * 1280); else dma16<0>(wos, lds + (ks) * 5120 + i * 1024, vB[i], ((ks) - 16) * 160 * 1280); } } while (0)
#define W1_KS(ks) do { _Pragma("unroll") for (int i = 0; i < 6; ++i) dma16<0>(w1s, lds + (ks) * 6144 + i * 1024, vD[i], (ks) * 32 * 3072); } while (0)
  const unsigned w2 = WS_W2 + (unsigned)j * 245760;
#define W2_STG(st) do { _Pragma("unroll") for (int i = 0; i < 10; ++i) { const int f = lw + 4 * i; \
    dma16<16>(ws, lds + ((st) % 3) * 40960 + f * 1024, l16, w2 + (st) * 40960 + f * 1024); } } while (0)
  const int uslot = blockIdx.x + 256 * (3 - lw);
  const bool haveu = lw >= 2 && uslot < 384;
  LAS char* ust = lds + L_P0 + (lw & 1) * 5120;
#pragma unroll
  for (int q = 1; q <= 9; ++q) {
    if (q == 1) WAITL0();
    if (q >= 8 && haveu) WAITV(15); else WAITV(10);
    rbar();
    if (q + 2 <= 9) P1_STG(q + 2);
    if (q == 7 && haveu) w2_unit_issue(p, ust, lane, uslot & 7, uslot >> 3);
    if (q == 8) { WO_KS(8 + lw); WO_KS(12 + lw); }
    if (q == 9) WO_KS(16 + lw);
  }
  if (haveu) { WAITV(15); w2_unit_store(p, ust, lane, uslot & 7, uslot >> 3); }
#undef P1_STG
  STAMP(3);
  rbar();
  WO_KS(lw); WO_KS(4 + lw);
  {
#pragma unroll
    for (int m = 0; m < 3; ++m) {
      const int n = lw + 4 * m;
      if (n < 10) { const int q = n * 64 + lane, row = q / 10, ch = q - row * 10;
        dma16<0>(xs, lds + L_STASH + n * 1024, (unsigned)(128 * tl + 64 * hb + row) * 1280 + 160 * j + 16 * ch, 0); }
    }
  }
  STAMP(5);
  rbar();
  WAITV(0);
  if (haveu && lane == 0) publish(p.ws, WS_F0B + ((uslot & 7) * 64 + (uslot >> 3)) * 8, tag);
  convert5(lds + (8 + lw) * 5120, lane); convert5(lds + (12 + lw) * 5120, lane); convert5(lds + (16 + lw) * 5120, lane);
  convert5(lds + lw * 5120, lane); convert5(lds + (4 + lw) * 5120, lane);
  if (lw == 0) wait_flags(p.ws, WS_F1 + (tl * 16 + hb * 8 + lane) * 8, lane < 8, tag);
  lbar();
  STAMP(6);
  if (lw == 0) { rsrc_t bs = mk_rsrc(p.b1, F_ * 2); dma16<0>(bs, lds + L_P3_B1, lane < 12 ? 192 * c2d + lane * 16 : 0x7fffffffu, 0); }
  if (lw < 3) W1_KS(17 + lw);
  rbar();
  W1_KS(lw); W1_KS(9 + lw); if (lw == 3) W1_KS(4);
  rbar();
  W1_KS(5 + lw); W1_KS(13 + lw);
  STAMP(9);
  WAITV(12);
  if (lw < 3) convert6(lds + (17 + lw) * 6144, lane);
  convert6(lds + lw * 6144, lane); convert6(lds + (9 + lw) * 6144, lane); if (lw == 3) convert6(lds + 4 * 6144, lane);
  WAITV(0);
  convert6(lds + (5 + lw) * 6144, lane); convert6(lds + (13 + lw) * 6144, lane);
  lbar();
  if (lw == 0) wait_flags(p.ws, WS_F2 + (t2d * 16 + lane) * 8, lane < 16, tag);
  rbar();
  STAMP(10);
  wait_flags(p.ws, WS_F0B + (j * 64 + lane) * 8, lane < 48, tag);
  if (lw == 0) { rsrc_t bs = mk_rsrc(p.b2, D_ * 2); dma16<0>(bs, lds + L_P4_B2, lane < 10 ? 160 * j + lane * 16 : 0x7fffffffu, 0); }
  rbar();
  W2_STG(0);
  rbar();
  W2_STG(1);
  rbar();
  STAMP(11);
  W2_STG(2);
  STAMP(13);
  if (lw == 0) wait_flags(p.ws, WS_F3 + (tl * 16 + lane) * 8, lane < 16, tag);
  WAITV(0); rbar();
  STAMP(14);
  rbar(); W2_STG(3);
  rbar(); W2_STG(4);
  WAITV(10); rbar(); W2_STG(5);
  if (blockIdx.x == 0 && lw == 1) {
    for (int q = 0; q < 4; ++q) wait_flags(p.ws, WS_F1 + (q * 64 + lane) * 8, true, tag);
    if (lane == 0) publish(p.ws, WS_NONCE, tag);
  }
  WAITV(10); rbar();
  WAITV(0); rbar();
#undef W2_STG
#undef WO_KS
#undef W1_KS
  STAMP(15);
  rbar();
  rbar();
  STAMP(16);
}

DEVI void wg_publish(const Params& p, LAS char* lds, int k, int lane, unsigned off, u64 tag) {
  WAITV(0);
  if (lane == 0) {
    const unsigned old = __hip_atomic_fetch_add((LAS unsigned*)(lds + L_CNT) + k, 1u, __ATOMIC_RELAXED, __HIP_MEMORY_SCOPE_WORKGROUP);
    if (old == 7u) publish(p.ws, off, tag);
  }
}

__global__ __launch_bounds__(768, 3) __attribute__((amdgpu_waves_per_eu(3, 3))) void fused_block(Params p) {
  extern __shared__ __attribute__((aligned(16))) char smem[];
  LAS char* lds = (LAS char*)smem;
  const int lane = threadIdx.x & 63, w = __builtin_amdgcn_readfirstlane(threadIdx.x >> 6);
  const int x8 = blockIdx.x & 7, r = blockIdx.x >> 3;
  u64 tag = 0;
  STAMP(0);
  const u64 tstart = __builtin_amdgcn_s_memrealtime();
  if (w >= 8) loader(p, lds, lane, w - 8 STAMP_PASS);
  else {

    if (CPRIO) __builtin_amdgcn_s_setprio(CPRIO);
    const int t2d = 4 * (x8 >> 1) + (r >> 3), c2d = 8 * (x8 & 1) + (r & 7), tl = 2 * x8 + (r >> 4);
    phase1(p, lds, lane, w, tag STAMP_PASS);
    { const int tt = 8 * (x8 >> 1) + (r >> 2), grp = 4 * (x8 & 1) + (r & 3);
      wg_publish(p, lds, 0, fresh_lane(), WS_F1 + ((tt >> 1) * 16 + (tt & 1) * 8 + grp) * 8, tag); }
    STAMP(5);
    phase2(p, lds, fresh_lane(), w STAMP_PASS);
    wg_publish(p, lds, 1, fresh_lane(), WS_F2 + (tl * 16 + (r & 15)) * 8, tag);
    STAMP(9);
    phase3(p, lds, fresh_lane(), w STAMP_PASS);
    wg_publish(p, lds, 2, fresh_lane(), WS_F3 + (t2d * 16 + c2d) * 8, tag);
    STAMP(13);
    phase4(p, lds, fresh_lane(), w STAMP_PASS);
  }
  while (__builtin_amdgcn_s_memrealtime() - tstart < (u64)(PADTO) * 100 + (((PADMASK >> (blockIdx.x & 7)) & 1) ? 300 : 0)) __builtin_amdgcn_s_sleep(1);
}

extern "C" void kernel_launch(void* const* d_in, const int* in_sizes, int n_in, void* d_out, int out_size, void* d_ws, size_t ws_size, hipStream_t stream) {
  Params p;
  p.x = (const unsigned short*)d_in[0]; p.wqkv = (const unsigned short*)d_in[1]; p.wo = (const unsigned short*)d_in[2];
  p.w1 = (const unsigned short*)d_in[4]; p.b1 = (const unsigned short*)d_in[5]; p.w2 = (const unsigned short*)d_in[6]; p.b2 = (const unsigned short*)d_in[7];
  p.out = (unsigned short*)d_out; p.ws = (unsigned char*)d_ws;
  hipFuncSetAttribute((const void*)fused_block, hipFuncAttributeMaxDynamicSharedMemorySize, LDS_BYTES);
  hipLaunchKernelGGL(fused_block, dim3(256), dim3(768), LDS_BYTES, stream, p);
}
```

```cpp
#include <hip/hip_runtime.h>
#include <hip/hip_bf16.h>
#include <stdint.h>

#define GAS __attribute__((address_space(1)))
#define LAS __attribute__((address_space(3)))
#define DEVI __device__ __forceinline__
typedef unsigned v4u __attribute__((ext_vector_type(4)));
typedef unsigned v2u __attribute__((ext_vector_type(2)));
typedef float f32x4 __attribute__((ext_vector_type(4)));
typedef short bf16x8 __attribute__((ext_vector_type(8)));
typedef short s16x4 __attribute__((ext_vector_type(4)));
typedef __bf16 bf16x2 __attribute__((ext_vector_type(2)));
typedef unsigned long long u64;
typedef GAS u64 gu64;
#define RLX_AGENT __ATOMIC_RELAXED, __HIP_MEMORY_SCOPE_AGENT

#define NLAUNCH 1
#define OUT_SC1 0
#define WS_AT_END 0
#define TOUCH 0
#define P1_SAME 0
#define POLL_SLEEP 4
#define P0_POLL_DELAY 64
#define CPRIO 2
#define WARM 1
#define RT_STORE 11
#define RT_PUB 18
#define TOUCH_SLEEP 2
#define WT0 3
#define EARLY_W2 2
#define RT_ISSUE 1
#define TOUCH2_SLEEP 10
#define PRO 0
#define WT_SPREAD 5
#define DIAG 0

constexpr int T_ = 2048, D_ = 640, F_ = 1536;
constexpr unsigned WS_NONCE = 0;
constexpr unsigned WS_F0Q = 40960;
constexpr unsigned WS_F0O = 8192;
constexpr unsigned WS_F0A = 12288;
constexpr unsigned WS_F0B = 16384;
constexpr unsigned WS_F0X = 20480;
constexpr unsigned WS_F1 = 24576;
constexpr unsigned WS_F2 = 28672;
constexpr unsigned WS_F3 = 32768;
constexpr unsigned WS_XN = 131072;
constexpr unsigned WS_WQ = WS_XN + 128 * 20480;
constexpr unsigned WS_WO = WS_WQ + 16 * 20 * 8 * 1024;
constexpr unsigned WS_W1 = WS_WO + 8 * 20 * 5 * 1024;
constexpr unsigned WS_W2 = WS_W1 + 16 * 20 * 6 * 1024;
constexpr unsigned WS_O = WS_W2 + 8 * 48 * 5 * 1024;
constexpr unsigned WS_H = WS_O + 128 * 20480;
constexpr unsigned WS_H1 = WS_H + 128 * 20480;
constexpr unsigned WS_ST = WS_H1 + 128 * 49152;
constexpr unsigned WS_END = WS_ST + 128 * 2048;
constexpr unsigned LDS_BYTES = 163840;
constexpr unsigned L_STASH = 153600;
constexpr unsigned L_P0 = 122880;
constexpr unsigned L_TAG = 153600;
constexpr unsigned L_GO = 153616;
#define SHADOW_SLEEP 40
constexpr unsigned P1_STAGE = 40960;
constexpr unsigned L_P1_EX = 102400;
constexpr unsigned P1_RS = 520;
constexpr unsigned L_P1_ST = 144000;
constexpr unsigned L_EXH = 124928;
constexpr unsigned L_P3_B1 = 122880;
constexpr unsigned L_P4_B2 = 145408;
constexpr unsigned L_CNT = 147456;

struct Params {
  const unsigned short* x; const unsigned short* wqkv; const unsigned short* wo; const unsigned short* w1; const unsigned short* b1;
  const unsigned short* w2; const unsigned short* b2; unsigned short* out; unsigned char* ws;
};

typedef __amdgpu_buffer_rsrc_t rsrc_t;
DEVI rsrc_t mk_rsrc(const void* p, unsigned bytes) { return __builtin_amdgcn_make_buffer_rsrc((void*)p, 0, bytes, 0x00020000); }
DEVI v4u mk_srd(const void* p, unsigned bytes) {
  u64 a = (u64)p; v4u r; r.x = (unsigned)a; r.y = (unsigned)(a >> 32) & 0xffffu; r.z = bytes; r.w = 0x00020000u; return r;
}
template <int AUX> DEVI void dma16(rsrc_t rs, LAS char* lds, unsigned voff, unsigned soff) {
  __builtin_amdgcn_raw_ptr_buffer_load_lds(rs, (LAS void*)lds, 16, voff, soff, 0, AUX);
}
DEVI void dma16_asm(v4u srd, unsigned ldsbase, unsigned voff) {
  unsigned keep;
  asm volatile("s_mov_b32 %0, m0\n\ts_mov_b32 m0, %3\n\ts_nop 0\n\tbuffer_load_dwordx4 %1, %2, 0 offen lds\n\ts_mov_b32 m0, %0"
               : "=&s"(keep) : "v"(voff), "s"(srd), "s"(ldsbase) : "memory");
}
DEVI void aload(v4u& d, v4u srd, unsigned voff, unsigned soff) {
  asm volatile("buffer_load_dwordx4 %0, %1, %2, %3 offen sc1" : "=v"(d) : "v"(voff), "s"(srd), "s"(soff) : "memory");
}
DEVI void st16(rsrc_t rs, v4u v, unsigned voff, unsigned soff) { __builtin_amdgcn_raw_buffer_store_b128(v, rs, voff + soff, 0, 16); }
#define WAITV(n) asm volatile("s_waitcnt vmcnt(%0)" ::"i"(n) : "memory")
#define WAITV1(n, a) asm volatile("s_waitcnt vmcnt(%1)" : "+v"(a) : "i"(n) : "memory")
#define WAITV2(n, a, b) asm volatile("s_waitcnt vmcnt(%2)" : "+v"(a), "+v"(b) : "i"(n) : "memory")
#define WAITV4(n, a, b, c, d) asm volatile("s_waitcnt vmcnt(%4)" : "+v"(a), "+v"(b), "+v"(c), "+v"(d) : "i"(n) : "memory")
#define WAITL0() asm volatile("s_waitcnt lgkmcnt(0)" ::: "memory")
#define BAR() __builtin_amdgcn_s_barrier()
#define SCHED0() __builtin_amdgcn_sched_barrier(0)
DEVI void lbar() { WAITL0(); BAR(); asm volatile("" ::: "memory"); }
DEVI void rbar() { asm volatile("" ::: "memory"); BAR(); asm volatile("" ::: "memory"); }

DEVI unsigned pk2(float a, float b) { bf16x2 p; p[0] = (__bf16)a; p[1] = (__bf16)b; return __builtin_bit_cast(unsigned, p); }
DEVI float bflo(unsigned u) { return __uint_as_float(u << 16); }
DEVI float bfhi(unsigned u) { return __uint_as_float(u & 0xffff0000u); }
DEVI bf16x8 asbf(v4u v) { return __builtin_bit_cast(bf16x8, v); }
DEVI f32x4 mfma(v4u a, v4u b, f32x4 c) { return __builtin_amdgcn_mfma_f32_16x16x32_bf16(asbf(a), asbf(b), c, 0, 0, 0); }
DEVI v4u ldsr16(LAS char* p) { return *(LAS v4u*)p; }
DEVI int fresh_lane() { int v; asm volatile("v_mbcnt_lo_u32_b32 %0, -1, 0\n\tv_mbcnt_hi_u32_b32 %0, -1, %0" : "=v"(v)); return v; }
DEVI float fsel(bool c, float a, float b) { asm("" : "+v"(a)); asm("" : "+v"(b)); return c ? a : b; }
DEVI float xrow_max(float v) {
  unsigned u = __float_as_uint(v);
  auto a = __builtin_amdgcn_permlane32_swap(u, u, false, false); v = fmaxf(__uint_as_float(a[0]), __uint_as_float(a[1]));
  u = __float_as_uint(v);
  auto b = __builtin_amdgcn_permlane16_swap(u, u, false, false); return fmaxf(__uint_as_float(b[0]), __uint_as_float(b[1]));
}
DEVI float xrow_sum(float v) {
  unsigned u = __float_as_uint(v);
  auto a = __builtin_amdgcn_permlane32_swap(u, u, false, false); v = __uint_as_float(a[0]) + __uint_as_float(a[1]);
  u = __float_as_uint(v);
  auto b = __builtin_amdgcn_permlane16_swap(u, u, false, false); return __uint_as_float(b[0]) + __uint_as_float(b[1]);
}
DEVI float row16_sum(float v) {
  v += __uint_as_float(__builtin_amdgcn_update_dpp(0u, __float_as_uint(v), 0x128, 0xf, 0xf, false));
  v += __uint_as_float(__builtin_amdgcn_update_dpp(0u, __float_as_uint(v), 0x124, 0xf, 0xf, false));
  v += __uint_as_float(__builtin_amdgcn_update_dpp(0u, __float_as_uint(v), 0x122, 0xf, 0xf, false));
  v += __uint_as_float(__builtin_amdgcn_update_dpp(0u, __float_as_uint(v), 0x121, 0xf, 0xf, false));
  return v;
}

DEVI u64 rfl64(u64 v) { return ((u64)__builtin_amdgcn_readfirstlane((unsigned)(v >> 32)) << 32) | (unsigned)__builtin_amdgcn_readfirstlane((unsigned)v); }
DEVI void publish(unsigned char* ws, unsigned off, u64 tag) { __hip_atomic_store((gu64*)(ws + off), tag, RLX_AGENT); }
DEVI void wait_flags(unsigned char* ws, unsigned laneoff, bool active, u64 tag) {
  for (unsigned sp = 0; sp < (1u << 18); ++sp) {
    u64 v = tag;
    if (active) v = __hip_atomic_load((gu64*)(ws + laneoff), RLX_AGENT);
    if (__all(v == tag)) break;
    __builtin_amdgcn_s_sleep(POLL_SLEEP);
  }
  __builtin_amdgcn_fence(__ATOMIC_ACQUIRE, "wavefront");
  asm volatile("" ::: "memory");
}

DEVI void wait_flags2(unsigned char* ws, unsigned off1, bool act1, unsigned off2, bool act2, u64 tag) {
  for (unsigned sp = 0; sp < (1u << 18); ++sp) {
    u64 v1 = tag, v2 = tag;
    if (act1) v1 = __hip_atomic_load((gu64*)(ws + off1), RLX_AGENT);
    if (act2) v2 = __hip_atomic_load((gu64*)(ws + off2), RLX_AGENT);
    if (__all(v1 == tag && v2 == tag)) break;
    __builtin_amdgcn_s_sleep(POLL_SLEEP);
  }
  __builtin_amdgcn_fence(__ATOMIC_ACQUIRE, "wavefront");
  asm volatile("" ::: "memory");
}

#define STAMP_ARG
#define STAMP_PASS
#define STAMP(id) do {} while (0)

DEVI void w2_unit_issue(const Params& p, LAS char* st, int lane, int a, int ks) {
  const v4u rs = mk_srd(p.w2, 1536u * 1280u);
  const unsigned sb = __builtin_amdgcn_readfirstlane((unsigned)(uintptr_t)st);
#pragma unroll
  for (int n = 0; n < 5; ++n) {
    const int q = n * 64 + lane, rho = (q * 6554) >> 16, ch = q - rho * 10, g = rho >> 3, t = (rho >> 2) & 1, e = rho & 3;
    dma16_asm(rs, sb + n * 1024, (unsigned)(32 * ks + 16 * t + 4 * g + e) * 1280u + 160 * a + 16 * ch);
  }
}
DEVI void w2_unit_store(const Params& p, LAS char* st, int lane, int a, int ks) {
  rsrc_t ws = mk_rsrc(p.ws, WS_END);
  const int kh = ks / 24, s = ks - kh * 24;
  const unsigned dst = WS_W2 + (unsigned)a * 245760 + (s >> 2) * 40960 + kh * 20480 + (s & 3) * 5120;
  const unsigned lo = (unsigned)(uintptr_t)st + (8 * (lane >> 4) + ((lane & 15) >> 2)) * 160 + 8 * (lane & 3);
  v2u a0, a1, a2, a3, a4, b0, b1, b2, b3, b4;
  asm volatile("ds_read_b64_tr_b16 %0, %10\n\tds_read_b64_tr_b16 %5, %10 offset:640\n\tds_read_b64_tr_b16 %1, %10 offset:32\n\tds_read_b64_tr_b16 %6, %10 offset:672\n\t"
               "ds_read_b64_tr_b16 %2, %10 offset:64\n\tds_read_b64_tr_b16 %7, %10 offset:704\n\tds_read_b64_tr_b16 %3, %10 offset:96\n\tds_read_b64_tr_b16 %8, %10 offset:736\n\t"
               "ds_read_b64_tr_b16 %4, %10 offset:128\n\tds_read_b64_tr_b16 %9, %10 offset:768\n\ts_waitcnt lgkmcnt(0)"
               : "=&v"(a0), "=&v"(a1), "=&v"(a2), "=&v"(a3), "=&v"(a4), "=&v"(b0), "=&v"(b1), "=&v"(b2), "=&v"(b3), "=&v"(b4) : "v"(lo) : "memory");
  st16(ws, v4u{a0.x, a0.y, b0.x, b0.y}, lane * 16, dst);
  st16(ws, v4u{a1.x, a1.y, b1.x, b1.y}, lane * 16, dst + 1024);
  st16(ws, v4u{a2.x, a2.y, b2.x, b2.y}, lane * 16, dst + 2048);
  st16(ws, v4u{a3.x, a3.y, b3.x, b3.y}, lane * 16, dst + 3072);
  st16(ws, v4u{a4.x, a4.y, b4.x, b4.y}, lane * 16, dst + 4096);
}
constexpr unsigned L_JUNK = 147520;
DEVI void touch_line(LAS char* lds, const GAS char* a) {
  unsigned keep;
  asm volatile("s_mov_b32 %0, m0\n\ts_mov_b32 m0, %2\n\ts_nop 0\n\tglobal_load_lds_dword %1, off\n\ts_mov_b32 m0, %0" : "=&s"(keep) : "v"(a), "s"((unsigned)(uintptr_t)(lds + L_JUNK)) : "memory");
}
DEVI const GAS char* proj_touch(const Params& p, int n, int lane) {
  int idx = n * 64 + lane; if (idx > 39679) idx = 39679;
  const int s = idx / 3968, rem = idx - s * 3968;
  if (rem < 2048) return (const GAS char*)p.x + (unsigned)rem * 1280u + s * 128;
  const int q = rem - 2048, row = q / 30;
  return (const GAS char*)p.wqkv + (unsigned)(64 * s + row) * 3840u + (q - row * 30) * 128;
}
DEVI const GAS char* weight_touch(const Params& p, int n, int lane) {
  const int idx = n * 64 + lane;
  if (idx < 6400) return (const GAS char*)p.wo + (unsigned)idx * 128u;
  if (idx < 21760) return (const GAS char*)p.w2 + (unsigned)(idx - 6400) * 128u;
  return (const GAS char*)p.w1 + (unsigned)(idx - 21760) * 128u;
}
struct StageTouch { const GAS char* a; unsigned stride; };
DEVI StageTouch stage_touch(const Params& p, int lane) {
  const int x8 = blockIdx.x & 7, r = blockIdx.x >> 3, idx = r * 47 + lane;
  StageTouch t;
  if (lane < 47 && idx < 960) { const int row = (idx * 4370) >> 16, l = idx - row * 15, sec = (l * 52) >> 8;
    t.a = (const GAS char*)p.wqkv + (unsigned)row * 3840u + sec * 1280 + 640 * (x8 & 1) + (l - sec * 5) * 128; t.stride = 64 * 3840; }
  else { int tok = 512 * (x8 >> 1) - 16 + (idx - 960); if (lane >= 47 || idx >= 1488 || tok < 0) tok = 512 * (x8 >> 1);
    t.a = (const GAS char*)p.x + (unsigned)tok * 1280u; t.stride = 128; }
  return t;
}
DEVI v4u trfrag(LAS char* lo, LAS char* hi) {
  const v2u a = __builtin_bit_cast(v2u, __builtin_amdgcn_ds_read_tr16_b64_v4i16((LAS s16x4*)lo));
  const v2u b = __builtin_bit_cast(v2u, __builtin_amdgcn_ds_read_tr16_b64_v4i16((LAS s16x4*)hi));
  return v4u{a.x, a.y, b.x, b.y};
}

DEVI void p1_tail(const Params& p, LAS char* lds, int lane, int w, int tt, int grp, f32x4 (&acc)[2][5], f32x4 st1, f32x4 st2 STAMP_ARG);
DEVI void phase1(const Params& p, LAS char* lds, int lane, int w, u64& tag STAMP_ARG) {
  const int x8 = blockIdx.x & 7, r = blockIdx.x >> 3;
  const int tt = 8 * (x8 >> 1) + (r >> 2), grp = 4 * (x8 & 1) + (r & 3);
  rsrc_t ws = mk_rsrc(p.ws, WS_END);
  const unsigned l16 = lane * 16;
  const int g = lane >> 4, c = lane & 15;
  f32x4 acc[2][5], st1 = {0.f, 0.f, 0.f, 0.f}, st2 = st1;
#pragma unroll
  for (int e = 0; e < 2; ++e)
#pragma unroll
    for (int b = 0; b < 5; ++b) acc[e][b] = f32x4{0.f, 0.f, 0.f, 0.f};
  const unsigned xo0 = c * 128 + ((g ^ (c & 7)) * 16), xo1 = c * 128 + (((4 + g) ^ (c & 7)) * 16);
  const unsigned sblk = (w < 5 ? w : 0) * 2048;
  unsigned wo0, wo1;
  {
    const int p4 = lane & 3, pp = (w >> 2) * 80, m = w & 3;
    wo0 = m == 0 ? 160 + 8 * p4 : m == 1 ? (p4 < 2 ? 160 : 0) + 32 + 40 * (p4 & 1) : m == 2 ? 40 + 8 * p4 : 320 + 40 + 8 * p4;
    wo1 = m == 0 ? 160 + 40 + 8 * p4 : m == 1 ? 8 * p4 : m == 2 ? 320 + 8 * p4 : 320 + 32 + 40 * (p4 & 1);
    const int q4 = (lane & 15) >> 2;
    const unsigned r0 = 10240 + (8 * g + 4 * (g & 1) + q4) * 480 + pp;
    wo0 += r0; wo1 += r0;
  }
  const int hiD = (g & 1) ? -4 * 480 : 4 * 480;
  v4u W[2][2], X[2][6];
#define P1_RO(ks) ((((ks) >> 1) % 3) * P1_STAGE)
#define P1_TR(addr) __builtin_bit_cast(v2u, __builtin_amdgcn_ds_read_tr16_b64_v4i16((LAS s16x4*)(addr)))
#define P1_READ(ks) do { \
    LAS char* wb_ = lds + P1_RO(ks) + ((ks) & 1) * 15360; \
    { v2u l0 = P1_TR(wb_ + wo0), h0 = P1_TR(wb_ + wo0 + hiD), l1 = P1_TR(wb_ + wo1), h1 = P1_TR(wb_ + wo1 + hiD); \
      W[(ks) & 1][0] = v4u{l0.x, l0.y, h0.x, h0.y}; W[(ks) & 1][1] = v4u{l1.x, l1.y, h1.x, h1.y}; } \
    LAS char* xb_ = lds + P1_RO(ks) + (((ks) & 1) ? xo1 : xo0); \
    _Pragma("unroll") for (int b = 0; b < 5; ++b) X[(ks) & 1][b] = ldsr16(xb_ + b * 2048); \
    X[(ks) & 1][5] = ldsr16(xb_ + sblk); } while (0)
  if (WARM && w == 4) { __builtin_amdgcn_s_sleep(TOUCH_SLEEP); touch_line(lds, proj_touch(p, blockIdx.x, lane)); }
  if (WARM && w == 5) { __builtin_amdgcn_s_sleep(TOUCH2_SLEEP); touch_line(lds, proj_touch(p, blockIdx.x + 256, lane)); }
  const int wstage = 1 + (blockIdx.x >> 3) % WT_SPREAD;
  if (w == 3) {
    const u64 nonce = __hip_atomic_load((gu64*)(p.ws + WS_NONCE), RLX_AGENT);
    if (lane == 0) *(LAS u64*)(lds + L_TAG) = nonce * 6364136223846793005ull + 1442695040888963407ull;
  }
  STAMP(2);
  lbar();
  tag = rfl64(*(LAS u64*)(lds + L_TAG));
  P1_READ(0);
  SCHED0();
  const v4u ones = {0x3f803f80u, 0x3f803f80u, 0x3f803f80u, 0x3f803f80u};
#pragma unroll
  for (int ks = 0; ks < 20; ++ks) {
    if ((ks & 1) && ks < 19) lbar();
    if (WARM) {
      if (ks == 0 && w == 5 && blockIdx.x + 512 < 620) touch_line(lds, proj_touch(p, blockIdx.x + 512, fresh_lane()));
      if ((ks & 1) && ks < 2 * WT_SPREAD + 1 && w == 6 && wstage == (ks + 1) / 2) {
        const int ln = fresh_lane();
        touch_line(lds, weight_touch(p, blockIdx.x, ln)); touch_line(lds, weight_touch(p, blockIdx.x + 256, ln));
        if (blockIdx.x + 512 < 580) touch_line(lds, weight_touch(p, blockIdx.x + 512, ln));
      }
    }
    if (ks + 1 < 20) P1_READ(ks + 1);
    v4u x0 = X[ks & 1][0];
    if (c == 0) x0 = ones;
#pragma unroll
    for (int b = 0; b < 5; ++b) {
      const v4u xb = b == 0 ? x0 : X[ks & 1][b];
      acc[0][b] = mfma(W[ks & 1][0], xb, acc[0][b]); acc[1][b] = mfma(W[ks & 1][1], xb, acc[1][b]);
    }
    st1 = mfma(ones, X[ks & 1][5], st1); st2 = mfma(X[ks & 1][5], X[ks & 1][5], st2);
    __builtin_amdgcn_sched_group_barrier(0x100, 4, 0);
#pragma unroll
    for (int b = 0; b < 6; ++b) { __builtin_amdgcn_sched_group_barrier(0x8, 2, 0); __builtin_amdgcn_sched_group_barrier(0x100, 1, 0); }
    SCHED0();
  }
#undef P1_RO
#undef P1_TR
#undef P1_READ
  STAMP(3);
  p1_tail(p, lds, fresh_lane(), w, tt, grp, acc, st1, st2 STAMP_PASS);
}
DEVI void p1_tail(const Params& p, LAS char* lds, int lane, int w, int tt, int grp, f32x4 (&acc)[2][5], f32x4 st1, f32x4 st2 STAMP_ARG) {
  rsrc_t ws = mk_rsrc(p.ws, WS_END);
  const unsigned l16 = lane * 16;
  const int g = lane >> 4, c = lane & 15;
  if (w < 5) {
    const float mean = st1[0] * (1.f / 640.f);
    const float e2 = fsel((c & 2) != 0, fsel((c & 1) != 0, st2[3], st2[2]), fsel((c & 1) != 0, st2[1], st2[0])) * (1.f / 640.f);
    const float rstd = __builtin_amdgcn_rsqf(fmaxf(e2 - mean * mean, 0.f) + 1e-5f);
    if (g == (c >> 2)) *(LAS v2u*)(lds + L_P1_ST + (w * 16 + c) * 8) = v2u{__float_as_uint(rstd), __float_as_uint(-mean * rstd)};
  }
  lbar();
  {
    v2u sv[5];
#pragma unroll
    for (int b = 0; b < 5; ++b) sv[b] = *(LAS v2u*)(lds + L_P1_ST + (b * 16 + c) * 8);
    float cs[2][4];
#pragma unroll
    for (int e = 0; e < 2; ++e)
#pragma unroll
      for (int q = 0; q < 4; ++q) cs[e][q] = __uint_as_float(__builtin_amdgcn_update_dpp(0u, __float_as_uint(acc[e][0][q]), 0x150, 0xf, 0xf, false));
    LAS char* d = lds + L_P1_EX + c * P1_RS + w * 64 + g * 8;
#pragma unroll
    for (int b = 0; b < 5; ++b) {
      const float rs = __uint_as_float(sv[b].x), mr = __uint_as_float(sv[b].y);
#pragma unroll
      for (int e = 0; e < 2; ++e)
        *(LAS v2u*)(d + b * 16 * P1_RS + e * 32) = v2u{pk2(fmaf(rs, acc[e][b][0], mr * cs[e][0]), fmaf(rs, acc[e][b][1], mr * cs[e][1])),
                                                       pk2(fmaf(rs, acc[e][b][2], mr * cs[e][2]), fmaf(rs, acc[e][b][3], mr * cs[e][3]))};
    }
  }
  if (DIAG != 4) STAMP(22);
  lbar();
  if (DIAG != 4) STAMP(23);
  const int blk = 4 * tt + (w & 3), pr = 2 * grp + (w >> 2);
  LAS char* own = lds + L_P1_EX + (((w & 3) + 1) * 16 + c) * P1_RS + (w >> 2) * 256 + g * 8;
  LAS char* trp = lds + L_P1_EX + (((w & 3) + 1) * 16 + 4 * g + (c >> 2)) * P1_RS + (w >> 2) * 256 + (c & 3) * 8;
  v2u ka[2], kp[2], qa[2], qr[2], va[2], vp[2];
  const v2u kr = *(LAS v2u*)(own + 64), krp = *(LAS v2u*)(own + 64 - 16 * P1_RS);
#pragma unroll
  for (int hh = 0; hh < 2; ++hh) {
    ka[hh] = *(LAS v2u*)(own + hh * 32); kp[hh] = *(LAS v2u*)(own + hh * 32 - 16 * P1_RS);
    qa[hh] = *(LAS v2u*)(own + (3 + hh) * 32);
    qr[hh] = *(LAS v2u*)(own - g * 8 + 64 + 16 + hh * 8);
    va[hh] = __builtin_bit_cast(v2u, __builtin_amdgcn_ds_read_tr16_b64_v4i16((LAS s16x4*)(trp + (5 + hh) * 32)));
    vp[hh] = __builtin_bit_cast(v2u, __builtin_amdgcn_ds_read_tr16_b64_v4i16((LAS s16x4*)(trp + (5 + hh) * 32 - 16 * P1_RS)));
  }
  const v2u vr = __builtin_bit_cast(v2u, __builtin_amdgcn_ds_read_tr16_b64_v4i16((LAS s16x4*)(trp + 7 * 32)));
  const v2u vrp = __builtin_bit_cast(v2u, __builtin_amdgcn_ds_read_tr16_b64_v4i16((LAS s16x4*)(trp + 7 * 32 - 16 * P1_RS)));
  if (DIAG != 4) STAMP(24);
  SCHED0();
  const int odd = c & 1, ce = c - odd;
  const bool havePrev = (tt > 0) || ((w & 3) > 0);
  const bool v1a = havePrev && (4 * g >= ce + 2), v1b = havePrev && (4 * g >= ce), v2a = ce >= 4 * g, v2b = ce - 2 >= 4 * g;
  const float c2 = 0.22360679774997896f * 1.4426950408889634f, NEG = -3.0e38f;
  const f32x4 z = {0.f, 0.f, 0.f, 0.f};
  f32x4 S1[2], S2[2];
#pragma unroll
  for (int hh = 0; hh < 2; ++hh) {
    const bool mine = g == hh;
    const v4u Q = {qa[hh].x, qa[hh].y, mine ? qr[hh].x : 0u, mine ? qr[hh].y : 0u};
    S1[hh] = mfma(v4u{kp[hh].x, kp[hh].y, krp.x, krp.y}, Q, z);
    S2[hh] = mfma(v4u{ka[hh].x, ka[hh].y, kr.x, kr.y}, Q, z);
  }
  float s1a[2], s1b[2], s2a[2], s2b[2], m[2], inv[2];
#pragma unroll
  for (int hh = 0; hh < 2; ++hh) {
    s1a[hh] = fsel(v1a, fsel(odd, S1[hh][1], S1[hh][0]), NEG); s1b[hh] = fsel(v1b, fsel(odd, S1[hh][3], S1[hh][2]), NEG);
    s2a[hh] = fsel(v2a, fsel(odd, S2[hh][1], S2[hh][0]), NEG); s2b[hh] = fsel(v2b, fsel(odd, S2[hh][3], S2[hh][2]), NEG);
    m[hh] = fmaxf(fmaxf(s1a[hh], s1b[hh]), fmaxf(s2a[hh], s2b[hh]));
  }
  {
    auto a0 = __builtin_amdgcn_permlane32_swap(__float_as_uint(m[0]), __float_as_uint(m[0]), false, false);
    auto a1 = __builtin_amdgcn_permlane32_swap(__float_as_uint(m[1]), __float_as_uint(m[1]), false, false);
    m[0] = fmaxf(__uint_as_float(a0[0]), __uint_as_float(a0[1])); m[1] = fmaxf(__uint_as_float(a1[0]), __uint_as_float(a1[1]));
    auto b0 = __builtin_amdgcn_permlane16_swap(__float_as_uint(m[0]), __float_as_uint(m[0]), false, false);
    auto b1 = __builtin_amdgcn_permlane16_swap(__float_as_uint(m[1]), __float_as_uint(m[1]), false, false);
    m[0] = fmaxf(__uint_as_float(b0[0]), __uint_as_float(b0[1])); m[1] = fmaxf(__uint_as_float(b1[0]), __uint_as_float(b1[1]));
  }
#pragma unroll
  for (int hh = 0; hh < 2; ++hh) {
    s1a[hh] = __builtin_amdgcn_exp2f((s1a[hh] - m[hh]) * c2); s1b[hh] = __builtin_amdgcn_exp2f((s1b[hh] - m[hh]) * c2);
    s2a[hh] = __builtin_amdgcn_exp2f((s2a[hh] - m[hh]) * c2); s2b[hh] = __builtin_amdgcn_exp2f((s2b[hh] - m[hh]) * c2);
    inv[hh] = (s1a[hh] + s1b[hh]) + (s2a[hh] + s2b[hh]);
  }
  {
    auto a0 = __builtin_amdgcn_permlane32_swap(__float_as_uint(inv[0]), __float_as_uint(inv[0]), false, false);
    auto a1 = __builtin_amdgcn_permlane32_swap(__float_as_uint(inv[1]), __float_as_uint(inv[1]), false, false);
    inv[0] = __uint_as_float(a0[0]) + __uint_as_float(a0[1]); inv[1] = __uint_as_float(a1[0]) + __uint_as_float(a1[1]);
    auto b0 = __builtin_amdgcn_permlane16_swap(__float_as_uint(inv[0]), __float_as_uint(inv[0]), false, false);
    auto b1 = __builtin_amdgcn_permlane16_swap(__float_as_uint(inv[1]), __float_as_uint(inv[1]), false, false);
    inv[0] = __builtin_amdgcn_rcpf(__uint_as_float(b0[0]) + __uint_as_float(b0[1])); inv[1] = __builtin_amdgcn_rcpf(__uint_as_float(b1[0]) + __uint_as_float(b1[1]));
  }
  if (DIAG != 4) STAMP(25);
  f32x4 OA[2], OR[2];
  const unsigned sh = 16 * odd;
#pragma unroll
  for (int hh = 0; hh < 2; ++hh) {
    const v4u P = {pk2(s1a[hh] * inv[hh], 0.f) << sh, pk2(s1b[hh] * inv[hh], 0.f) << sh, pk2(s2a[hh] * inv[hh], 0.f) << sh, pk2(s2b[hh] * inv[hh], 0.f) << sh};
    OA[hh] = mfma(v4u{vp[hh].x, vp[hh].y, va[hh].x, va[hh].y}, P, z);
    OR[hh] = mfma(v4u{vrp.x, vrp.y, vr.x, vr.y}, P, z);
  }
  {
    v4u om = {pk2(OA[0][0], OA[0][1]), pk2(OA[0][2], OA[0][3]), pk2(OA[1][0], OA[1][1]), pk2(OA[1][2], OA[1][3])};
    st16(ws, om, l16, WS_O + (unsigned)blk * 20480 + pr * 1024);
    unsigned r0x = pk2(OR[0][0], OR[0][1]), r0y = pk2(OR[0][2], OR[0][3]), r1x = pk2(OR[1][0], OR[1][1]), r1y = pk2(OR[1][2], OR[1][3]);
    auto t0 = __builtin_amdgcn_permlane16_swap(r1x, r1x, false, false); auto t1 = __builtin_amdgcn_permlane16_swap(r1y, r1y, false, false);
    v4u orr = {r0x, r0y, t0[1], t1[1]};
    if (lane < 16) st16(ws, orr, l16, WS_O + (unsigned)blk * 20480 + (16 + (pr >> 2)) * 1024 + (pr & 3) * 256);
  }
  STAMP(4);
}

DEVI void exchange_halves(LAS char* lds, const f32x4 (&acc)[5], f32x4 (&fin)[3], int lane, int w) {
  LAS char* d = lds + L_EXH + (w & 3) * 5120 + lane * 16;
  if (w < 4) { *(LAS f32x4*)(d + 3072) = acc[3]; *(LAS f32x4*)(d + 4096) = acc[4]; }
  else { *(LAS f32x4*)(d) = acc[0]; *(LAS f32x4*)(d + 1024) = acc[1]; *(LAS f32x4*)(d + 2048) = acc[2]; }
  lbar();
  if (w < 4) { f32x4 u0 = *(LAS f32x4*)(d), u1 = *(LAS f32x4*)(d + 1024), u2 = *(LAS f32x4*)(d + 2048); fin[0] = acc[0] + u0; fin[1] = acc[1] + u1; fin[2] = acc[2] + u2; }
  else { f32x4 u0 = *(LAS f32x4*)(d + 3072), u1 = *(LAS f32x4*)(d + 4096); fin[0] = acc[3] + u0; fin[1] = acc[4] + u1; fin[2] = f32x4{0.f, 0.f, 0.f, 0.f}; }
}

DEVI void phase2(const Params& p, LAS char* lds, int lane, int w STAMP_ARG) {
  const int x8 = blockIdx.x & 7, r = blockIdx.x >> 3;
  const int ti = 2 * x8 + (r >> 4), hb = (r >> 3) & 1, j = r & 7;
  rsrc_t ws = mk_rsrc(p.ws, WS_END);
  const v4u srd = mk_srd(p.ws, WS_END);
  const unsigned l16 = lane * 16;
  const int b = w & 3, kh = w >> 2, blk = 8 * ti + 4 * hb + b;
  rbar();
  STAMP(6);
  v4u a[10];
  const unsigned oa = WS_O + (unsigned)blk * 20480 + 10 * kh * 1024;
#pragma unroll
  for (int s = 0; s < 10; ++s) aload(a[s], srd, l16, oa + s * 1024);
  f32x4 acc[5];
#pragma unroll
  for (int t = 0; t < 5; ++t) acc[t] = f32x4{0.f, 0.f, 0.f, 0.f};
  v4u B[2][5];
  LAS char* bb = lds + kh * 51200 + l16;
#pragma unroll
  for (int t = 0; t < 5; ++t) B[0][t] = ldsr16(bb + t * 1024);
  SCHED0();
#pragma unroll
  for (int s = 0; s < 10; ++s) {
    if (s == 5) lbar();
    asm volatile("s_waitcnt vmcnt(%1)" : "+v"(a[s]) : "i"(9 - s) : "memory");
    if (s + 1 < 10) {
#pragma unroll
      for (int t = 0; t < 5; ++t) B[(s + 1) & 1][t] = ldsr16(bb + (s + 1) * 5120 + t * 1024);
    }
#pragma unroll
    for (int t = 0; t < 5; ++t) acc[t] = mfma(B[s & 1][t], a[s], acc[t]);
#pragma unroll
    for (int t = 0; t < 5; ++t) { __builtin_amdgcn_sched_group_barrier(0x100, 1, 0); __builtin_amdgcn_sched_group_barrier(0x8, 1, 0); }
    SCHED0();
  }
  STAMP(7);
  const int g = lane >> 4, c = lane & 15, row = 16 * b + c, mt0 = 3 * kh;
  LAS char* sx = lds + L_STASH + row * 160 + mt0 * 32 + g * 8;
  v2u xv[3];
#pragma unroll
  for (int e = 0; e < 3; ++e) xv[e] = *(LAS v2u*)(sx + (e < 2 || kh == 0 ? e : 0) * 32);
  f32x4 fin[3];
  exchange_halves(lds, acc, fin, lane, w);
  v2u hv[3];
  float ssum = 0.f, ssq = 0.f;
#pragma unroll
  for (int e = 0; e < 3; ++e) {
    hv[e] = v2u{pk2(fin[e][0] + bflo(xv[e].x), fin[e][1] + bfhi(xv[e].x)), pk2(fin[e][2] + bflo(xv[e].y), fin[e][3] + bfhi(xv[e].y))};
    if (e < 2 || kh == 0) {
      *(LAS v2u*)(sx + e * 32) = hv[e];
      const float h0 = bflo(hv[e].x), h1 = bfhi(hv[e].x), h2 = bflo(hv[e].y), h3 = bfhi(hv[e].y);
      ssum += (h0 + h1) + (h2 + h3); ssq += fmaf(h0, h0, h1 * h1) + fmaf(h2, h2, h3 * h3);
    }
  }
  {
    auto sx0 = __builtin_amdgcn_permlane16_swap(hv[0].x, hv[1].x, false, false); auto sy0 = __builtin_amdgcn_permlane16_swap(hv[0].y, hv[1].y, false, false);
    const int G = 2 * (mt0 + (g & 1)) + (g >> 1);
    st16(ws, v4u{sx0[0], sy0[0], sx0[1], sy0[1]}, c * 16 + (10 * j + G) * 256, WS_H + (unsigned)blk * 20480);
    if (kh == 0) __builtin_amdgcn_raw_buffer_store_b64(hv[2], ws, WS_H + (unsigned)blk * 20480 + (10 * j + 4 + (g >> 1)) * 256 + c * 16 + (g & 1) * 8, 0, 16);
  }
  ssum = xrow_sum(ssum); ssq = xrow_sum(ssq);
  {
    const int idx = 2 * j + kh;
    const int e = idx & 3;
    if (lane < 16) {
      v2u sv = {__float_as_uint(ssum), __float_as_uint(ssq)};
      __builtin_amdgcn_raw_buffer_store_b64(sv, ws, WS_ST + (unsigned)blk * 2048 + (e >> 1) * 1024 + (idx >> 2) * 256 + c * 16 + (e & 1) * 8, 0, 16);
    }
  }
  STAMP(8);
}

#define P3D 8
DEVI void phase3(const Params& p, LAS char* lds, int lane, int w STAMP_ARG) {
  const int x8 = blockIdx.x & 7, r = blockIdx.x >> 3;
  const int ti = 4 * (x8 >> 1) + (r >> 3), jj = 8 * (x8 & 1) + (r & 7);
  rsrc_t ws = mk_rsrc(p.ws, WS_END);
  const v4u srd = mk_srd(p.ws, WS_END);
  const unsigned l16 = lane * 16;
  const int blk = 8 * ti + w;
  rbar();
  if (w < 6) {
    v4u cf[20];
#pragma unroll
    for (int ks = 0; ks < 20; ++ks) cf[ks] = ldsr16(lds + (ks * 6 + w) * 1024 + l16);
    SCHED0();
    f32x4 cs0 = {0.f, 0.f, 0.f, 0.f}, cs1 = cs0;
    const v4u ones = {0x3f803f80u, 0x3f803f80u, 0x3f803f80u, 0x3f803f80u};
#pragma unroll
    for (int ks = 0; ks < 20; ks += 2) { cs0 = mfma(cf[ks], ones, cs0); cs1 = mfma(cf[ks + 1], ones, cs1); }
    if ((lane & 15) == 0) *(LAS f32x4*)(lds + L_P3_B1 + 1024 + (w * 16 + (lane >> 4) * 4) * 4) = cs0 + cs1;
  }
  lbar();
  STAMP(10);
  v4u a[20], st0, st1;
  const unsigned ha = WS_H + (unsigned)blk * 20480;
#pragma unroll
  for (int s = 0; s < P3D; ++s) aload(a[s], srd, l16, ha + s * 1024);
  f32x4 acc[6];
#pragma unroll
  for (int t = 0; t < 6; ++t) acc[t] = f32x4{0.f, 0.f, 0.f, 0.f};
  v4u B[2][4];
  LAS char* bb = lds + l16;
#define P3_RD(s, t) ldsr16(bb + (s) * 6144 + (t) * 1024)
#pragma unroll
  for (int t = 0; t < 4; ++t) B[0][t] = P3_RD(0, t);
  SCHED0();
#pragma unroll
  for (int s = 0; s < 20; ++s) {
    asm volatile("s_waitcnt vmcnt(%1)" : "+v"(a[s]) : "i"(s + P3D - 1 < 22 ? P3D - 1 : 21 - s) : "memory");
    if (s + P3D < 20) aload(a[s + P3D], srd, l16, ha + (s + P3D) * 1024);
    else if (s + P3D == 20) aload(st0, srd, l16, WS_ST + (unsigned)blk * 2048);
    else if (s + P3D == 21) aload(st1, srd, l16, WS_ST + (unsigned)blk * 2048 + 1024);
    if (s + 1 < 20) {
#pragma unroll
      for (int t = 0; t < 4; ++t) B[(s + 1) & 1][t] = P3_RD(s + 1, t);
    }
#pragma unroll
    for (int t = 0; t < 4; ++t) acc[t] = mfma(B[s & 1][t], a[s], acc[t]);
#pragma unroll
    for (int t = 0; t < 4; ++t) { __builtin_amdgcn_sched_group_barrier(0x100, 1, 0); __builtin_amdgcn_sched_group_barrier(0x8, 1, 0); }
    SCHED0();
  }
  lbar();
  const int g = lane >> 4;
  f32x4 cs[6]; v2u bb1[6];
#pragma unroll
  for (int t = 0; t < 4; ++t) { cs[t] = *(LAS f32x4*)(lds + L_P3_B1 + 1024 + (t * 16 + g * 4) * 4); bb1[t] = *(LAS v2u*)(lds + L_P3_B1 + (t * 16 + g * 4) * 2); }
  v4u C[2][4];
#define P3_RD2(s2) do { C[(s2) & 1][0] = P3_RD(2 * (s2), 4); C[(s2) & 1][1] = P3_RD(2 * (s2), 5); C[(s2) & 1][2] = P3_RD(2 * (s2) + 1, 4); C[(s2) & 1][3] = P3_RD(2 * (s2) + 1, 5); } while (0)
  P3_RD2(0);
  WAITV2(0, st0, st1);
  SCHED0();
  float s1 = (__uint_as_float(st0[0]) + __uint_as_float(st0[2])) + (__uint_as_float(st1[0]) + __uint_as_float(st1[2]));
  float s2 = (__uint_as_float(st0[1]) + __uint_as_float(st0[3])) + (__uint_as_float(st1[1]) + __uint_as_float(st1[3]));
  s1 = xrow_sum(s1); s2 = xrow_sum(s2);
  const float mean = s1 * (1.f / 640.f);
  const float var = fmaxf(s2 * (1.f / 640.f) - mean * mean, 0.f);
  const float rstd = __builtin_amdgcn_rsqf(var + 1e-5f), mr = -mean * rstd;
#define P3_OUT(t, o0, o1) do { \
    const float y0 = fmaxf(fmaf(rstd, acc[t][0], fmaf(mr, cs[t][0], bflo(bb1[t].x))), 0.f), y1 = fmaxf(fmaf(rstd, acc[t][1], fmaf(mr, cs[t][1], bfhi(bb1[t].x))), 0.f); \
    const float y2 = fmaxf(fmaf(rstd, acc[t][2], fmaf(mr, cs[t][2], bflo(bb1[t].y))), 0.f), y3 = fmaxf(fmaf(rstd, acc[t][3], fmaf(mr, cs[t][3], bfhi(bb1[t].y))), 0.f); \
    o0 = pk2(y0 * y0, y1 * y1); o1 = pk2(y2 * y2, y3 * y3); } while (0)
  const unsigned h1o = WS_H1 + (unsigned)blk * 49152 + 3 * jj * 1024;
  {
    v4u o0, o1;
    P3_OUT(0, o0[0], o0[1]); P3_OUT(1, o0[2], o0[3]); P3_OUT(2, o1[0], o1[1]); P3_OUT(3, o1[2], o1[3]);
    st16(ws, o0, l16, h1o); st16(ws, o1, l16, h1o + 1024);
  }
#pragma unroll
  for (int t = 4; t < 6; ++t) { cs[t] = *(LAS f32x4*)(lds + L_P3_B1 + 1024 + (t * 16 + g * 4) * 4); bb1[t] = *(LAS v2u*)(lds + L_P3_B1 + (t * 16 + g * 4) * 2); }
  SCHED0();
#pragma unroll
  for (int s2 = 0; s2 < 10; ++s2) {
    if (s2 + 1 < 10) P3_RD2(s2 + 1);
    acc[4] = mfma(C[s2 & 1][0], a[2 * s2], acc[4]); acc[5] = mfma(C[s2 & 1][1], a[2 * s2], acc[5]);
    acc[4] = mfma(C[s2 & 1][2], a[2 * s2 + 1], acc[4]); acc[5] = mfma(C[s2 & 1][3], a[2 * s2 + 1], acc[5]);
#pragma unroll
    for (int t = 0; t < 4; ++t) { __builtin_amdgcn_sched_group_barrier(0x100, 1, 0); __builtin_amdgcn_sched_group_barrier(0x8, 1, 0); }
    SCHED0();
  }
#undef P3_RD2
#undef P3_RD
  STAMP(11);
  {
    v4u o2;
    P3_OUT(4, o2[0], o2[1]); P3_OUT(5, o2[2], o2[3]);
    st16(ws, o2, l16, h1o + 2048);
  }
  lbar();
#undef P3_OUT
  STAMP(12);
}

#define P4D 8
constexpr unsigned w2_slot(int sl, int f) { return sl == 2 ? (f >> 1) * 6144 + (4 + (f & 1)) * 1024 : (10 * sl + (f >> 2)) * 6144 + (f & 3) * 1024; }
DEVI void phase4(const Params& p, LAS char* lds, int lane, int w STAMP_ARG) {
  const int x8 = blockIdx.x & 7, r = blockIdx.x >> 3;
  const int ti = 2 * x8 + (r >> 4), hb = (r >> 3) & 1, j = r & 7;
  const v4u srd = mk_srd(p.ws, WS_END);
  const unsigned l16 = lane * 16;
  const int b = w & 3, kh = w >> 2, blk = 8 * ti + 4 * hb + b;
  rbar();
  STAMP(14);
  const unsigned ha = WS_H1 + (unsigned)blk * 49152 + kh * 24 * 1024;
  v4u a[24];
#pragma unroll
  for (int s = 0; s < P4D; ++s) aload(a[s], srd, l16, ha + s * 1024);
  f32x4 acc[5];
#pragma unroll
  for (int t = 0; t < 5; ++t) acc[t] = f32x4{0.f, 0.f, 0.f, 0.f};
  v4u B[2][5];
  LAS char* bb01 = lds + kh * 5 * 6144 + l16;
  LAS char* bb2 = lds + kh * 10 * 6144 + l16;
#define P4_READ(s) do { _Pragma("unroll") for (int t = 0; t < 5; ++t) B[(s) & 1][t] = \
    ldsr16((((s) >> 2) % 3 == 2 ? bb2 : bb01) + w2_slot(((s) >> 2) % 3, ((s) & 3) * 5 + t)); } while (0)
  P4_READ(0);
  SCHED0();
#pragma unroll
  for (int s = 0; s < 24; ++s) {
    if ((s & 3) == 3 && s < 23) lbar();
    asm volatile("s_waitcnt vmcnt(%1)" : "+v"(a[s]) : "i"(s + P4D - 1 < 24 ? P4D - 1 : 23 - s) : "memory");
    if (s + P4D < 24) aload(a[s + P4D], srd, l16, ha + (s + P4D) * 1024);
    if (s + 1 < 24) P4_READ(s + 1);
#pragma unroll
    for (int t = 0; t < 5; ++t) acc[t] = mfma(B[s & 1][t], a[s], acc[t]);
#pragma unroll
    for (int t = 0; t < 5; ++t) { __builtin_amdgcn_sched_group_barrier(0x100, 1, 0); __builtin_amdgcn_sched_group_barrier(0x8, 1, 0); }
    SCHED0();
  }
#undef P4_READ
  STAMP(15);
  const int g = lane >> 4, c = lane & 15, row = 16 * b + c, mt0 = 3 * kh;
  LAS char* sx = lds + L_STASH + row * 160 + mt0 * 32 + g * 8;
  v2u hv[3], bv[3];
#pragma unroll
  for (int e = 0; e < 3; ++e) {
    const int ee = (e < 2 || kh == 0) ? e : 0;
    hv[e] = *(LAS v2u*)(sx + ee * 32);
    bv[e] = *(LAS v2u*)(lds + L_P4_B2 + (mt0 + ee) * 32 + g * 8);
  }
  f32x4 fin[3];
  exchange_halves(lds, acc, fin, lane, w);
#pragma unroll
  for (int e = 0; e < 3; ++e) {
    if (e < 2 || kh == 0)
      *(LAS v2u*)(sx + e * 32) = v2u{pk2(fin[e][0] + bflo(bv[e].x) + bflo(hv[e].x), fin[e][1] + bfhi(bv[e].x) + bfhi(hv[e].x)),
                                     pk2(fin[e][2] + bflo(bv[e].y) + bflo(hv[e].y), fin[e][3] + bfhi(bv[e].y) + bfhi(hv[e].y))};
  }
  lbar();
  {
    const int q0 = w * 64 + lane, q1 = q0 + 512;
    const v4u o0 = *(LAS v4u*)(lds + L_STASH + q0 * 16);
    v4u o1 = o0; if (w < 2) o1 = *(LAS v4u*)(lds + L_STASH + q1 * 16);
    const int r0 = q0 / 10, r1 = q1 / 10;
    rsrc_t outr = mk_rsrc(p.out, T_ * 1280);
    const unsigned ob = (unsigned)(128 * ti + 64 * hb) * 1280 + 160 * j;
    __builtin_amdgcn_raw_buffer_store_b128(o0, outr, ob + r0 * 1280 + (q0 - r0 * 10) * 16, 0, 0);
    if (w < 2) __builtin_amdgcn_raw_buffer_store_b128(o1, outr, ob + r1 * 1280 + (q1 - r1 * 10) * 16, 0, 0);
  }
  STAMP(16);
}

DEVI unsigned ldsaddr(LAS char* p) { return (unsigned)(uintptr_t)p; }
DEVI void convert5(LAS char* u, int lane) {
  const int g = lane >> 4;
  const unsigned lo = ldsaddr(u) + (8 * g + 4 * (g & 1) + ((lane & 15) >> 2)) * 160 + (lane & 3) * 8, hi = lo + ((g & 1) ? -640 : 640), d = ldsaddr(u) + lane * 16;
  v2u a0, a1, a2, a3, a4, b0, b1, b2, b3, b4;
  asm volatile("ds_read_b64_tr_b16 %0, %10\n\tds_read_b64_tr_b16 %5, %11\n\tds_read_b64_tr_b16 %1, %10 offset:32\n\tds_read_b64_tr_b16 %6, %11 offset:32\n\t"
               "ds_read_b64_tr_b16 %2, %10 offset:64\n\tds_read_b64_tr_b16 %7, %11 offset:64\n\tds_read_b64_tr_b16 %3, %10 offset:96\n\tds_read_b64_tr_b16 %8, %11 offset:96\n\t"
               "ds_read_b64_tr_b16 %4, %10 offset:128\n\tds_read_b64_tr_b16 %9, %11 offset:128\n\ts_waitcnt lgkmcnt(0)"
               : "=&v"(a0), "=&v"(a1), "=&v"(a2), "=&v"(a3), "=&v"(a4), "=&v"(b0), "=&v"(b1), "=&v"(b2), "=&v"(b3), "=&v"(b4) : "v"(lo), "v"(hi) : "memory");
  const v4u f0 = {a0.x, a0.y, b0.x, b0.y}, f1 = {a1.x, a1.y, b1.x, b1.y}, f2 = {a2.x, a2.y, b2.x, b2.y}, f3 = {a3.x, a3.y, b3.x, b3.y}, f4 = {a4.x, a4.y, b4.x, b4.y};
  asm volatile("ds_write_b128 %0, %1\n\tds_write_b128 %0, %2 offset:1024\n\tds_write_b128 %0, %3 offset:2048\n\tds_write_b128 %0, %4 offset:3072\n\tds_write_b128 %0, %5 offset:4096\n\ts_waitcnt lgkmcnt(0)"
               ::"v"(d), "v"(f0), "v"(f1), "v"(f2), "v"(f3), "v"(f4) : "memory");
}
DEVI void convert6(LAS char* u, int lane) {
  const int g = lane >> 4, od = g & 1;
  const unsigned row = ldsaddr(u) + (8 * g + ((lane & 15) >> 2)) * 192 + (lane & 3) * 8, lo = row + 32 * od, l5 = row + (od ? 0 : 160), d = ldsaddr(u) + lane * 16;
  v2u a0, a1, a2, a3, a4, a5, b0, b1, b2, b3, b4, b5;
  asm volatile("ds_read_b64_tr_b16 %0, %12\n\tds_read_b64_tr_b16 %6, %12 offset:768\n\tds_read_b64_tr_b16 %1, %12 offset:32\n\tds_read_b64_tr_b16 %7, %12 offset:800\n\t"
               "ds_read_b64_tr_b16 %2, %12 offset:64\n\tds_read_b64_tr_b16 %8, %12 offset:832\n\tds_read_b64_tr_b16 %3, %12 offset:96\n\tds_read_b64_tr_b16 %9, %12 offset:864\n\t"
               "ds_read_b64_tr_b16 %4, %12 offset:128\n\tds_read_b64_tr_b16 %10, %12 offset:896\n\tds_read_b64_tr_b16 %5, %13\n\tds_read_b64_tr_b16 %11, %13 offset:768\n\ts_waitcnt lgkmcnt(0)"
               : "=&v"(a0), "=&v"(a1), "=&v"(a2), "=&v"(a3), "=&v"(a4), "=&v"(a5), "=&v"(b0), "=&v"(b1), "=&v"(b2), "=&v"(b3), "=&v"(b4), "=&v"(b5) : "v"(lo), "v"(l5) : "memory");
  const v4u f0 = {a0.x, a0.y, b0.x, b0.y}, f1 = {a1.x, a1.y, b1.x, b1.y}, f2 = {a2.x, a2.y, b2.x, b2.y}, f3 = {a3.x, a3.y, b3.x, b3.y}, f4 = {a4.x, a4.y, b4.x, b4.y}, f5 = {a5.x, a5.y, b5.x, b5.y};
  asm volatile("ds_write_b128 %0, %1\n\tds_write_b128 %0, %2 offset:1024\n\tds_write_b128 %0, %3 offset:2048\n\tds_write_b128 %0, %4 offset:3072\n\tds_write_b128 %0, %5 offset:4096\n\t"
               "ds_write_b128 %0, %6 offset:5120\n\ts_waitcnt lgkmcnt(0)"
               ::"v"(d), "v"(f0), "v"(f1), "v"(f2), "v"(f3), "v"(f4), "v"(f5) : "memory");
}

DEVI void loader(const Params& p, LAS char* lds, int lane, int lw STAMP_ARG) {
  const int x8 = blockIdx.x & 7, r = blockIdx.x >> 3;
  const int t2d = 4 * (x8 >> 1) + (r >> 3), c2d = 8 * (x8 & 1) + (r & 7);
  const int tl = 2 * x8 + (r >> 4), hb = (r >> 3) & 1, j = r & 7;
  rsrc_t ws = mk_rsrc(p.ws, WS_END);
  const v4u srd = mk_srd(p.ws, WS_END);
  const unsigned l16 = lane * 16;
  const int tt = 8 * (x8 >> 1) + (r >> 2), grp = 4 * (x8 & 1) + (r & 3);
  rsrc_t xs = mk_rsrc(p.x, T_ * 1280), wqs = mk_rsrc(p.wqkv, 640u * 3840u);
  unsigned vx[3], vw[8];
#pragma unroll
  for (int i = 0; i < 3; ++i) {
    const int m = lw + 4 * i, row = 8 * m + (lane >> 3);
    int tok = 64 * tt - 16 + row; if (tok < 0) tok = 0; if (tok > T_ - 1) tok = T_ - 1;
    vx[i] = (unsigned)tok * 1280 + (((lane & 7) ^ (row & 7)) * 16);
  }
  const int wn0 = lw < 2 ? lw * 7 - 1 : 14 + (lw - 2) * 8;
#pragma unroll
  for (int i = 0; i < 8; ++i) {
    int n = wn0 + i; if (n < 0) n = 0;
    const int q = n * 64 + lane, rho = (q * 2185) >> 16, ch = q - rho * 30, sec = (ch * 205) >> 11;
    const int gg = (rho >> 3) & 3, krow = (rho & 32) + 8 * gg + 4 * (((rho >> 2) & 1) ^ (gg & 1)) + (rho & 3);
    vw[i] = (unsigned)krow * 3840u + sec * 1280 + 160 * grp + 16 * (ch - sec * 10);
  }
#define P1_STG(s) do { LAS char* sb_ = lds + ((s) % 3) * P1_STAGE; \
    dma16<0>(xs, sb_ + lw * 1024, vx[0], (s) * 128); dma16<0>(xs, sb_ + (lw + 4) * 1024, vx[1], (s) * 128); \
    if (lw < 2) dma16<0>(xs, sb_ + (lw + 8) * 1024, vx[2], (s) * 128); else dma16<0>(wqs, sb_ + 10240 + wn0 * 1024, vw[0], (s) * 64 * 3840); \
    _Pragma("unroll") for (int i = 1; i < 8; ++i) dma16<0>(wqs, sb_ + 10240 + (wn0 + i) * 1024, vw[i], (s) * 64 * 3840); } while (0)
  STAMP(17);
  P1_STG(0);
  STAMP(18);
  if (PRO == 0) P1_STG(1);
  STAMP(19);
  if (PRO == 0) WAITV(10); else WAITV(0);
  STAMP(20);
  rbar();
  const u64 tag = rfl64(*(LAS u64*)(lds + L_TAG));
  if (lw == 0 && lane < 4) *((LAS unsigned*)(lds + L_CNT) + lane) = 0u;
  STAMP(2);
  if (PRO == 1) P1_STG(1);
  P1_STG(2);
  rsrc_t wos = mk_rsrc(p.wo, 640u * 1280u), w1s = mk_rsrc(p.w1, 640u * 3072u);
  unsigned vA[5], vB[5], vD[6];
#pragma unroll
  for (int i = 0; i < 5; ++i) {
    const int q = 64 * i + lane, rho = (q * 6554) >> 16, cc = q - rho * 10, g = rho >> 3, t = ((rho >> 2) & 1) ^ (g & 1), e = rho & 3;
    const unsigned col = 160 * j + 16 * cc;
    vA[i] = (unsigned)(20 * t + 4 * g + e) * 1280u + col;
    vB[i] = (unsigned)(40 * g + 20 * t + 16 + e) * 1280u + col;
  }
#pragma unroll
  for (int i = 0; i < 6; ++i) {
    const int q = 64 * i + lane, rho = (q * 5462) >> 16, pos = q - rho * 12, cc = ((rho >> 3) & 1) ? (pos < 2 ? pos + 10 : pos - 2) : pos;
    vD[i] = (unsigned)rho * 3072u + 192 * c2d + 16 * cc;
  }
#define WO_KS(ks) do { _Pragma("unroll") for (int i = 0; i < 5; ++i) {     if ((ks) < 16) dma16<0>(wos, lds + (ks) * 5120 + i * 1024, vA[i], (ks) * 40 * 1280); else dma16<0>(wos, lds + (ks) * 5120 + i * 1024, vB[i], ((ks) - 16) * 160 * 1280); } } while (0)
#define W1_KS(ks) do { _Pragma("unroll") for (int i = 0; i < 6; ++i) dma16<0>(w1s, lds + (ks) * 6144 + i * 1024, vD[i], (ks) * 32 * 3072); } while (0)
  const unsigned w2 = WS_W2 + (unsigned)j * 245760;
#define W2_STG(st) do { _Pragma("unroll") for (int i = 0; i < 10; ++i) { const int f = lw + 4 * i; \
    dma16<16>(ws, lds + ((st) % 3 == 2 ? (f >> 1) * 6144 + (4 + (f & 1)) * 1024 : (10 * ((st) % 3) + (f >> 2)) * 6144 + (f & 3) * 1024), l16, w2 + (st) * 40960 + f * 1024); } } while (0)
  const int uslot = blockIdx.x + 256 * (3 - lw);
  const bool haveu = lw >= 2 && uslot < 384;
  LAS char* ust = lds + L_P0 + (lw & 1) * 5120;
#pragma unroll
  for (int q = 1; q <= 9; ++q) {
    if (q == 1) WAITL0();
    if (q >= 8 && haveu) WAITV(15); else WAITV(10);
    rbar();
    if (q + 2 <= 9) P1_STG(q + 2);
    if (q == 7 && haveu) w2_unit_issue(p, ust, lane, uslot & 7, uslot >> 3);
    if (q == 8) { WO_KS(8 + lw); WO_KS(12 + lw); }
    if (q == 9) WO_KS(16 + lw);
  }
  if (haveu) { WAITV(15); w2_unit_store(p, ust, lane, uslot & 7, uslot >> 3); }
#undef P1_STG
  STAMP(3);
  rbar();
  WO_KS(lw); WO_KS(4 + lw);
  {
#pragma unroll
    for (int m = 0; m < 3; ++m) {
      const int n = lw + 4 * m;
      if (n < 10) { const int q = n * 64 + lane, row = q / 10, ch = q - row * 10;
        dma16<0>(xs, lds + L_STASH + n * 1024, (unsigned)(128 * tl + 64 * hb + row) * 1280 + 160 * j + 16 * ch, 0); }
    }
  }
  STAMP(5);
  rbar();
  WAITV(0);
  if (haveu && lane == 0) publish(p.ws, WS_F0B + ((uslot & 7) * 64 + (uslot >> 3)) * 8, tag);
  convert5(lds + (8 + lw) * 5120, lane); convert5(lds + (12 + lw) * 5120, lane); convert5(lds + (16 + lw) * 5120, lane);
  convert5(lds + lw * 5120, lane); convert5(lds + (4 + lw) * 5120, lane);
  if (lw == 0) wait_flags(p.ws, WS_F1 + (tl * 16 + hb * 8 + lane) * 8, lane < 8, tag);
  lbar();
  STAMP(6);
  if (lw == 0) { rsrc_t bs = mk_rsrc(p.b1, F_ * 2); dma16<0>(bs, lds + L_P3_B1, lane < 12 ? 192 * c2d + lane * 16 : 0x7fffffffu, 0); }
  if (lw < 3) W1_KS(17 + lw);
  rbar();
  W1_KS(lw); W1_KS(9 + lw); if (lw == 3) W1_KS(4);
  rbar();
  W1_KS(5 + lw); W1_KS(13 + lw);
  STAMP(9);
  WAITV(12);
  if (lw < 3) convert6(lds + (17 + lw) * 6144, lane);
  convert6(lds + lw * 6144, lane); convert6(lds + (9 + lw) * 6144, lane); if (lw == 3) convert6(lds + 4 * 6144, lane);
  WAITV(0);
  convert6(lds + (5 + lw) * 6144, lane); convert6(lds + (13 + lw) * 6144, lane);
  lbar();
  if (lw == 0) wait_flags(p.ws, WS_F2 + (t2d * 16 + lane) * 8, lane < 16, tag);
  rbar();
  STAMP(10);
  wait_flags(p.ws, WS_F0B + (j * 64 + lane) * 8, lane < 48, tag);
  if (lw == 0) { rsrc_t bs = mk_rsrc(p.b2, D_ * 2); dma16<0>(bs, lds + L_P4_B2, lane < 10 ? 160 * j + lane * 16 : 0x7fffffffu, 0); }
  rbar();
  W2_STG(0); W2_STG(1);
  rbar();
  STAMP(11);
  W2_STG(2);
  STAMP(13);
  if (lw == 0) wait_flags(p.ws, WS_F3 + (tl * 16 + lane) * 8, lane < 16, tag);
  WAITV(0); rbar();
  STAMP(14);
  rbar(); W2_STG(3);
  rbar(); W2_STG(4);
  WAITV(10); rbar(); W2_STG(5);
  if (blockIdx.x == 0 && lw == 1) {
    for (int q = 0; q < 4; ++q) wait_flags(p.ws, WS_F1 + (q * 64 + lane) * 8, true, tag);
    if (lane == 0) publish(p.ws, WS_NONCE, tag);
  }
  WAITV(10); rbar();
  WAITV(0); rbar();
#undef W2_STG
#undef WO_KS
#undef W1_KS
  STAMP(15);
  rbar();
  rbar();
  STAMP(16);
}

DEVI void wg_publish(const Params& p, LAS char* lds, int k, int lane, unsigned off, u64 tag) {
  WAITV(0);
  if (lane == 0) {
    const unsigned old = __hip_atomic_fetch_add((LAS unsigned*)(lds + L_CNT) + k, 1u, __ATOMIC_RELAXED, __HIP_MEMORY_SCOPE_WORKGROUP);
    if (old == 7u) publish(p.ws, off, tag);
  }
}

__global__ __launch_bounds__(768, 3) __attribute__((amdgpu_waves_per_eu(3, 3))) void fused_block(Params p) {
  extern __shared__ __attribute__((aligned(16))) char smem[];
  LAS char* lds = (LAS char*)smem;
  const int lane = threadIdx.x & 63, w = __builtin_amdgcn_readfirstlane(threadIdx.x >> 6);
  const int x8 = blockIdx.x & 7, r = blockIdx.x >> 3;
  u64 tag = 0;
  STAMP(0);
  if (w >= 8) loader(p, lds, lane, w - 8 STAMP_PASS);
  else {

    if (CPRIO) __builtin_amdgcn_s_setprio(CPRIO);
    const int t2d = 4 * (x8 >> 1) + (r >> 3), c2d = 8 * (x8 & 1) + (r & 7), tl = 2 * x8 + (r >> 4);
    phase1(p, lds, lane, w, tag STAMP_PASS);
    { const int tt = 8 * (x8 >> 1) + (r >> 2), grp = 4 * (x8 & 1) + (r & 3);
      wg_publish(p, lds, 0, fresh_lane(), WS_F1 + ((tt >> 1) * 16 + (tt & 1) * 8 + grp) * 8, tag); }
    STAMP(5);
    phase2(p, lds, fresh_lane(), w STAMP_PASS);
    wg_publish(p, lds, 1, fresh_lane(), WS_F2 + (tl * 16 + (r & 15)) * 8, tag);
    STAMP(9);
    phase3(p, lds, fresh_lane(), w STAMP_PASS);
    wg_publish(p, lds, 2, fresh_lane(), WS_F3 + (t2d * 16 + c2d) * 8, tag);
    STAMP(13);
    phase4(p, lds, fresh_lane(), w STAMP_PASS);
  }
}

extern "C" void kernel_launch(void* const* d_in, const int* in_sizes, int n_in, void* d_out, int out_size, void* d_ws, size_t ws_size, hipStream_t stream) {
  Params p;
  p.x = (const unsigned short*)d_in[0]; p.wqkv = (const unsigned short*)d_in[1]; p.wo = (const unsigned short*)d_in[2];
  p.w1 = (const unsigned short*)d_in[4]; p.b1 = (const unsigned short*)d_in[5]; p.w2 = (const unsigned short*)d_in[6]; p.b2 = (const unsigned short*)d_in[7];
  p.out = (unsigned short*)d_out; p.ws = (unsigned char*)d_ws;
  hipFuncSetAttribute((const void*)fused_block, hipFuncAttributeMaxDynamicSharedMemorySize, LDS_BYTES);
  hipLaunchKernelGGL(fused_block, dim3(256), dim3(768), LDS_BYTES, stream, p);
}
```

```cpp
#include <hip/hip_runtime.h>
#include <hip/hip_bf16.h>
#include <stdint.h>

#define GAS __attribute__((address_space(1)))
#define LAS __attribute__((address_space(3)))
#define DEVI __device__ __forceinline__
typedef unsigned v4u __attribute__((ext_vector_type(4)));
typedef unsigned v2u __attribute__((ext_vector_type(2)));
typedef float f32x4 __attribute__((ext_vector_type(4)));
typedef short bf16x8 __attribute__((ext_vector_type(8)));
typedef short s16x4 __attribute__((ext_vector_type(4)));
typedef __bf16 bf16x2 __attribute__((ext_vector_type(2)));
typedef unsigned long long u64;
typedef GAS u64 gu64;
#define RLX_AGENT __ATOMIC_RELAXED, __HIP_MEMORY_SCOPE_AGENT

#define NLAUNCH 1
#define OUT_SC1 0
#define WS_AT_END 0
#define TOUCH 0
#define P1_SAME 0
#define POLL_SLEEP 4
#define P0_POLL_DELAY 64
#define CPRIO 2
#define WARM 1
#define RT_STORE 11
#define RT_PUB 18
#define TOUCH_SLEEP 2
#define WT0 3
#define EARLY_W2 2
#define RT_ISSUE 1
#define TOUCH2_SLEEP 10
#define PRO 0
#define WT_SPREAD 5
#define FLAG_TOUCH 0
#define ST_FULL 1
#define DIAG 0

constexpr int T_ = 2048, D_ = 640, F_ = 1536;
constexpr unsigned WS_NONCE = 0;
constexpr unsigned WS_F0Q = 40960;
constexpr unsigned WS_F0O = 8192;
constexpr unsigned WS_F0A = 12288;
constexpr unsigned WS_F0B = 1048576;
constexpr unsigned WS_F0X = 20480;
#define F0STR 8
#define FSTR 128
constexpr unsigned WS_F1 = 131072;
constexpr unsigned WS_F2 = WS_F1 + 256 * FSTR;
constexpr unsigned WS_F3 = WS_F2 + 256 * FSTR;
constexpr unsigned WS_XN = 131072;
constexpr unsigned WS_WQ = WS_XN + 128 * 20480;
constexpr unsigned WS_WO = WS_WQ + 16 * 20 * 8 * 1024;
constexpr unsigned WS_W1 = WS_WO + 8 * 20 * 5 * 1024;
constexpr unsigned WS_W2 = WS_W1 + 16 * 20 * 6 * 1024;
constexpr unsigned WS_O = WS_W2 + 8 * 48 * 5 * 1024;
constexpr unsigned WS_H = WS_O + 128 * 20480;
constexpr unsigned WS_H1 = WS_H + 128 * 20480;
constexpr unsigned WS_ST = WS_H1 + 128 * 49152;
constexpr unsigned WS_END = WS_ST + 128 * 2048;
constexpr unsigned LDS_BYTES = 163840;
constexpr unsigned L_STASH = 153600;
constexpr unsigned L_P0 = 122880;
constexpr unsigned L_TAG = 153600;
constexpr unsigned L_GO = 153616;
#define SHADOW_SLEEP 40
constexpr unsigned P1_STAGE = 40960;
constexpr unsigned L_P1_EX = 102400;
constexpr unsigned P1_RS = 520;
constexpr unsigned L_P1_ST = 144000;
constexpr unsigned L_EXH = 124928;
constexpr unsigned L_P3_B1 = 122880;
constexpr unsigned L_P4_B2 = 145408;
constexpr unsigned L_CNT = 147456;

struct Params {
  const unsigned short* x; const unsigned short* wqkv; const unsigned short* wo; const unsigned short* w1; const unsigned short* b1;
  const unsigned short* w2; const unsigned short* b2; unsigned short* out; unsigned char* ws;
};

typedef __amdgpu_buffer_rsrc_t rsrc_t;
DEVI rsrc_t mk_rsrc(const void* p, unsigned bytes) { return __builtin_amdgcn_make_buffer_rsrc((void*)p, 0, bytes, 0x00020000); }
DEVI v4u mk_srd(const void* p, unsigned bytes) {
  u64 a = (u64)p; v4u r; r.x = (unsigned)a; r.y = (unsigned)(a >> 32) & 0xffffu; r.z = bytes; r.w = 0x00020000u; return r;
}
template <int AUX> DEVI void dma16(rsrc_t rs, LAS char* lds, unsigned voff, unsigned soff) {
  __builtin_amdgcn_raw_ptr_buffer_load_lds(rs, (LAS void*)lds, 16, voff, soff, 0, AUX);
}
DEVI void dma16_asm(v4u srd, unsigned ldsbase, unsigned voff) {
  unsigned keep;
  asm volatile("s_mov_b32 %0, m0\n\ts_mov_b32 m0, %3\n\ts_nop 0\n\tbuffer_load_dwordx4 %1, %2, 0 offen lds\n\ts_mov_b32 m0, %0"
               : "=&s"(keep) : "v"(voff), "s"(srd), "s"(ldsbase) : "memory");
}
DEVI void aload(v4u& d, v4u srd, unsigned voff, unsigned soff) {
  asm volatile("buffer_load_dwordx4 %0, %1, %2, %3 offen sc1" : "=v"(d) : "v"(voff), "s"(srd), "s"(soff) : "memory");
}
DEVI void st16(rsrc_t rs, v4u v, unsigned voff, unsigned soff) { __builtin_amdgcn_raw_buffer_store_b128(v, rs, voff + soff, 0, 16); }
#define WAITV(n) asm volatile("s_waitcnt vmcnt(%0)" ::"i"(n) : "memory")
#define WAITV1(n, a) asm volatile("s_waitcnt vmcnt(%1)" : "+v"(a) : "i"(n) : "memory")
#define WAITV2(n, a, b) asm volatile("s_waitcnt vmcnt(%2)" : "+v"(a), "+v"(b) : "i"(n) : "memory")
#define WAITV4(n, a, b, c, d) asm volatile("s_waitcnt vmcnt(%4)" : "+v"(a), "+v"(b), "+v"(c), "+v"(d) : "i"(n) : "memory")
#define WAITL0() asm volatile("s_waitcnt lgkmcnt(0)" ::: "memory")
#define BAR() __builtin_amdgcn_s_barrier()
#define SCHED0() __builtin_amdgcn_sched_barrier(0)
DEVI void lbar() { WAITL0(); BAR(); asm volatile("" ::: "memory"); }
DEVI void rbar() { asm volatile("" ::: "memory"); BAR(); asm volatile("" ::: "memory"); }

DEVI unsigned pk2(float a, float b) { bf16x2 p; p[0] = (__bf16)a; p[1] = (__bf16)b; return __builtin_bit_cast(unsigned, p); }
DEVI float bflo(unsigned u) { return __uint_as_float(u << 16); }
DEVI float bfhi(unsigned u) { return __uint_as_float(u & 0xffff0000u); }
DEVI bf16x8 asbf(v4u v) { return __builtin_bit_cast(bf16x8, v); }
DEVI f32x4 mfma(v4u a, v4u b, f32x4 c) { return __builtin_amdgcn_mfma_f32_16x16x32_bf16(asbf(a), asbf(b), c, 0, 0, 0); }
DEVI v4u ldsr16(LAS char* p) { return *(LAS v4u*)p; }
DEVI int fresh_lane() { int v; asm volatile("v_mbcnt_lo_u32_b32 %0, -1, 0\n\tv_mbcnt_hi_u32_b32 %0, -1, %0" : "=v"(v)); return v; }
DEVI float fsel(bool c, float a, float b) { asm("" : "+v"(a)); asm("" : "+v"(b)); return c ? a : b; }
DEVI float xrow_max(float v) {
  unsigned u = __float_as_uint(v);
  auto a = __builtin_amdgcn_permlane32_swap(u, u, false, false); v = fmaxf(__uint_as_float(a[0]), __uint_as_float(a[1]));
  u = __float_as_uint(v);
  auto b = __builtin_amdgcn_permlane16_swap(u, u, false, false); return fmaxf(__uint_as_float(b[0]), __uint_as_float(b[1]));
}
DEVI float xrow_sum(float v) {
  unsigned u = __float_as_uint(v);
  auto a = __builtin_amdgcn_permlane32_swap(u, u, false, false); v = __uint_as_float(a[0]) + __uint_as_float(a[1]);
  u = __float_as_uint(v);
  auto b = __builtin_amdgcn_permlane16_swap(u, u, false, false); return __uint_as_float(b[0]) + __uint_as_float(b[1]);
}
DEVI float row16_sum(float v) {
  v += __uint_as_float(__builtin_amdgcn_update_dpp(0u, __float_as_uint(v), 0x128, 0xf, 0xf, false));
  v += __uint_as_float(__builtin_amdgcn_update_dpp(0u, __float_as_uint(v), 0x124, 0xf, 0xf, false));
  v += __uint_as_float(__builtin_amdgcn_update_dpp(0u, __float_as_uint(v), 0x122, 0xf, 0xf, false));
  v += __uint_as_float(__builtin_amdgcn_update_dpp(0u, __float_as_uint(v), 0x121, 0xf, 0xf, false));
  return v;
}

DEVI u64 rfl64(u64 v) { return ((u64)__builtin_amdgcn_readfirstlane((unsigned)(v >> 32)) << 32) | (unsigned)__builtin_amdgcn_readfirstlane((unsigned)v); }
DEVI void publish(unsigned char* ws, unsigned off, u64 tag) { __hip_atomic_store((gu64*)(ws + off), tag, RLX_AGENT); }
DEVI void wait_flags(unsigned char* ws, unsigned laneoff, bool active, u64 tag) {
  for (unsigned sp = 0; sp < (1u << 18); ++sp) {
    u64 v = tag;
    if (active) v = __hip_atomic_load((gu64*)(ws + laneoff), RLX_AGENT);
    if (__all(v == tag)) break;
    __builtin_amdgcn_s_sleep(POLL_SLEEP);
  }
  __builtin_amdgcn_fence(__ATOMIC_ACQUIRE, "wavefront");
  asm volatile("" ::: "memory");
}

DEVI void wait_flags2(unsigned char* ws, unsigned off1, bool act1, unsigned off2, bool act2, u64 tag) {
  for (unsigned sp = 0; sp < (1u << 18); ++sp) {
    u64 v1 = tag, v2 = tag;
    if (act1) v1 = __hip_atomic_load((gu64*)(ws + off1), RLX_AGENT);
    if (act2) v2 = __hip_atomic_load((gu64*)(ws + off2), RLX_AGENT);
    if (__all(v1 == tag && v2 == tag)) break;
    __builtin_amdgcn_s_sleep(POLL_SLEEP);
  }
  __builtin_amdgcn_fence(__ATOMIC_ACQUIRE, "wavefront");
  asm volatile("" ::: "memory");
}

#define STAMP_ARG
#define STAMP_PASS
#define STAMP(id) do {} while (0)

DEVI void w2_unit_issue(const Params& p, LAS char* st, int lane, int a, int ks) {
  const v4u rs = mk_srd(p.w2, 1536u * 1280u);
  const unsigned sb = __builtin_amdgcn_readfirstlane((unsigned)(uintptr_t)st);
#pragma unroll
  for (int n = 0; n < 5; ++n) {
    const int q = n * 64 + lane, rho = (q * 6554) >> 16, ch = q - rho * 10, g = rho >> 3, t = (rho >> 2) & 1, e = rho & 3;
    dma16_asm(rs, sb + n * 1024, (unsigned)(32 * ks + 16 * t + 4 * g + e) * 1280u + 160 * a + 16 * ch);
  }
}
DEVI void w2_unit_store(const Params& p, LAS char* st, int lane, int a, int ks) {
  rsrc_t ws = mk_rsrc(p.ws, WS_END);
  const int kh = ks / 24, s = ks - kh * 24;
  const unsigned dst = WS_W2 + (unsigned)a * 245760 + (s >> 2) * 40960 + kh * 20480 + (s & 3) * 5120;
  const unsigned lo = (unsigned)(uintptr_t)st + (8 * (lane >> 4) + ((lane & 15) >> 2)) * 160 + 8 * (lane & 3);
  v2u a0, a1, a2, a3, a4, b0, b1, b2, b3, b4;
  asm volatile("ds_read_b64_tr_b16 %0, %10\n\tds_read_b64_tr_b16 %5, %10 offset:640\n\tds_read_b64_tr_b16 %1, %10 offset:32\n\tds_read_b64_tr_b16 %6, %10 offset:672\n\t"
               "ds_read_b64_tr_b16 %2, %10 offset:64\n\tds_read_b64_tr_b16 %7, %10 offset:704\n\tds_read_b64_tr_b16 %3, %10 offset:96\n\tds_read_b64_tr_b16 %8, %10 offset:736\n\t"
               "ds_read_b64_tr_b16 %4, %10 offset:128\n\tds_read_b64_tr_b16 %9, %10 offset:768\n\ts_waitcnt lgkmcnt(0)"
               : "=&v"(a0), "=&v"(a1), "=&v"(a2), "=&v"(a3), "=&v"(a4), "=&v"(b0), "=&v"(b1), "=&v"(b2), "=&v"(b3), "=&v"(b4) : "v"(lo) : "memory");
  st16(ws, v4u{a0.x, a0.y, b0.x, b0.y}, lane * 16, dst);
  st16(ws, v4u{a1.x, a1.y, b1.x, b1.y}, lane * 16, dst + 1024);
  st16(ws, v4u{a2.x, a2.y, b2.x, b2.y}, lane * 16, dst + 2048);
  st16(ws, v4u{a3.x, a3.y, b3.x, b3.y}, lane * 16, dst + 3072);
  st16(ws, v4u{a4.x, a4.y, b4.x, b4.y}, lane * 16, dst + 4096);
}
constexpr unsigned L_JUNK = 147520;
DEVI void touch_line(LAS char* lds, const GAS char* a) {
  unsigned keep;
  asm volatile("s_mov_b32 %0, m0\n\ts_mov_b32 m0, %2\n\ts_nop 0\n\tglobal_load_lds_dword %1, off\n\ts_mov_b32 m0, %0" : "=&s"(keep) : "v"(a), "s"((unsigned)(uintptr_t)(lds + L_JUNK)) : "memory");
}
DEVI const GAS char* proj_touch(const Params& p, int n, int lane) {
  int idx = n * 64 + lane; if (idx > 39679) idx = 39679;
  const int s = idx / 3968, rem = idx - s * 3968;
  if (rem < 2048) return (const GAS char*)p.x + (unsigned)rem * 1280u + s * 128;
  const int q = rem - 2048, row = q / 30;
  return (const GAS char*)p.wqkv + (unsigned)(64 * s + row) * 3840u + (q - row * 30) * 128;
}
DEVI const GAS char* weight_touch(const Params& p, int n, int lane) {
  const int idx = n * 64 + lane;
  if (idx < 6400) return (const GAS char*)p.wo + (unsigned)idx * 128u;
  if (idx < 21760) return (const GAS char*)p.w2 + (unsigned)(idx - 6400) * 128u;
  return (const GAS char*)p.w1 + (unsigned)(idx - 21760) * 128u;
}
struct StageTouch { const GAS char* a; unsigned stride; };
DEVI StageTouch stage_touch(const Params& p, int lane) {
  const int x8 = blockIdx.x & 7, r = blockIdx.x >> 3, idx = r * 47 + lane;
  StageTouch t;
  if (lane < 47 && idx < 960) { const int row = (idx * 4370) >> 16, l = idx - row * 15, sec = (l * 52) >> 8;
    t.a = (const GAS char*)p.wqkv + (unsigned)row * 3840u + sec * 1280 + 640 * (x8 & 1) + (l - sec * 5) * 128; t.stride = 64 * 3840; }
  else { int tok = 512 * (x8 >> 1) - 16 + (idx - 960); if (lane >= 47 || idx >= 1488 || tok < 0) tok = 512 * (x8 >> 1);
    t.a = (const GAS char*)p.x + (unsigned)tok * 1280u; t.stride = 128; }
  return t;
}
DEVI v4u trfrag(LAS char* lo, LAS char* hi) {
  const v2u a = __builtin_bit_cast(v2u, __builtin_amdgcn_ds_read_tr16_b64_v4i16((LAS s16x4*)lo));
  const v2u b = __builtin_bit_cast(v2u, __builtin_amdgcn_ds_read_tr16_b64_v4i16((LAS s16x4*)hi));
  return v4u{a.x, a.y, b.x, b.y};
}

DEVI void p1_tail(const Params& p, LAS char* lds, int lane, int w, int tt, int grp, f32x4 (&acc)[2][5], f32x4 st1, f32x4 st2 STAMP_ARG);
DEVI void phase1(const Params& p, LAS char* lds, int lane, int w, u64& tag STAMP_ARG) {
  const int x8 = blockIdx.x & 7, r = blockIdx.x >> 3;
  const int tt = 8 * (x8 >> 1) + (r >> 2), grp = 4 * (x8 & 1) + (r & 3);
  rsrc_t ws = mk_rsrc(p.ws, WS_END);
  const unsigned l16 = lane * 16;
  const int g = lane >> 4, c = lane & 15;
  f32x4 acc[2][5], st1 = {0.f, 0.f, 0.f, 0.f}, st2 = st1;
#pragma unroll
  for (int e = 0; e < 2; ++e)
#pragma unroll
    for (int b = 0; b < 5; ++b) acc[e][b] = f32x4{0.f, 0.f, 0.f, 0.f};
  const unsigned xo0 = c * 128 + ((g ^ (c & 7)) * 16), xo1 = c * 128 + (((4 + g) ^ (c & 7)) * 16);
  const unsigned sblk = (w < 5 ? w : 0) * 2048;
  unsigned wo0, wo1;
  {
    const int p4 = lane & 3, pp = (w >> 2) * 80, m = w & 3;
    wo0 = m == 0 ? 160 + 8 * p4 : m == 1 ? (p4 < 2 ? 160 : 0) + 32 + 40 * (p4 & 1) : m == 2 ? 40 + 8 * p4 : 320 + 40 + 8 * p4;
    wo1 = m == 0 ? 160 + 40 + 8 * p4 : m == 1 ? 8 * p4 : m == 2 ? 320 + 8 * p4 : 320 + 32 + 40 * (p4 & 1);
    const int q4 = (lane & 15) >> 2;
    const unsigned r0 = 10240 + (8 * g + 4 * (g & 1) + q4) * 480 + pp;
    wo0 += r0; wo1 += r0;
  }
  const int hiD = (g & 1) ? -4 * 480 : 4 * 480;
  v4u W[2][2], X[2][6];
#define P1_RO(ks) ((((ks) >> 1) % 3) * P1_STAGE)
#define P1_TR(addr) __builtin_bit_cast(v2u, __builtin_amdgcn_ds_read_tr16_b64_v4i16((LAS s16x4*)(addr)))
#define P1_READ(ks) do { \
    LAS char* wb_ = lds + P1_RO(ks) + ((ks) & 1) * 15360; \
    { v2u l0 = P1_TR(wb_ + wo0), h0 = P1_TR(wb_ + wo0 + hiD), l1 = P1_TR(wb_ + wo1), h1 = P1_TR(wb_ + wo1 + hiD); \
      W[(ks) & 1][0] = v4u{l0.x, l0.y, h0.x, h0.y}; W[(ks) & 1][1] = v4u{l1.x, l1.y, h1.x, h1.y}; } \
    LAS char* xb_ = lds + P1_RO(ks) + (((ks) & 1) ? xo1 : xo0); \
    _Pragma("unroll") for (int b = 0; b < 5; ++b) X[(ks) & 1][b] = ldsr16(xb_ + b * 2048); \
    X[(ks) & 1][5] = ldsr16(xb_ + sblk); } while (0)
  if (WARM && w == 4) { __builtin_amdgcn_s_sleep(TOUCH_SLEEP); touch_line(lds, proj_touch(p, blockIdx.x, lane)); }
  if (WARM && w == 5) { __builtin_amdgcn_s_sleep(TOUCH2_SLEEP); touch_line(lds, proj_touch(p, blockIdx.x + 256, lane)); }
  if (FLAG_TOUCH && w == 7 && blockIdx.x < 8) touch_line(lds, (const GAS char*)p.ws + WS_F0B + (lane + 64 * (blockIdx.x & 3)) * 128 % 20480);
  const int wstage = 1 + (blockIdx.x >> 3) % WT_SPREAD;
  if (w == 3) {
    const u64 nonce = __hip_atomic_load((gu64*)(p.ws + WS_NONCE), RLX_AGENT);
    if (lane == 0) *(LAS u64*)(lds + L_TAG) = nonce * 6364136223846793005ull + 1442695040888963407ull;
  }
  STAMP(2);
  lbar();
  tag = rfl64(*(LAS u64*)(lds + L_TAG));
  P1_READ(0);
  SCHED0();
  const v4u ones = {0x3f803f80u, 0x3f803f80u, 0x3f803f80u, 0x3f803f80u};
#pragma unroll
  for (int ks = 0; ks < 20; ++ks) {
    if ((ks & 1) && ks < 19) lbar();
    if (WARM) {
      if (ks == 0 && w == 5 && blockIdx.x + 512 < 620) touch_line(lds, proj_touch(p, blockIdx.x + 512, fresh_lane()));
      if ((ks & 1) && ks < 2 * WT_SPREAD + 1 && w == 6 && wstage == (ks + 1) / 2) {
        const int ln = fresh_lane();
        touch_line(lds, weight_touch(p, blockIdx.x, ln)); touch_line(lds, weight_touch(p, blockIdx.x + 256, ln));
        if (blockIdx.x + 512 < 580) touch_line(lds, weight_touch(p, blockIdx.x + 512, ln));
      }
    }
    if (ks + 1 < 20) P1_READ(ks + 1);
    v4u x0 = X[ks & 1][0];
    if (c == 0) x0 = ones;
#pragma unroll
    for (int b = 0; b < 5; ++b) {
      const v4u xb = b == 0 ? x0 : X[ks & 1][b];
      acc[0][b] = mfma(W[ks & 1][0], xb, acc[0][b]); acc[1][b] = mfma(W[ks & 1][1], xb, acc[1][b]);
    }
    st1 = mfma(ones, X[ks & 1][5], st1); st2 = mfma(X[ks & 1][5], X[ks & 1][5], st2);
    __builtin_amdgcn_sched_group_barrier(0x100, 4, 0);
#pragma unroll
    for (int b = 0; b < 6; ++b) { __builtin_amdgcn_sched_group_barrier(0x8, 2, 0); __builtin_amdgcn_sched_group_barrier(0x100, 1, 0); }
    SCHED0();
  }
#undef P1_RO
#undef P1_TR
#undef P1_READ
  STAMP(3);
  p1_tail(p, lds, fresh_lane(), w, tt, grp, acc, st1, st2 STAMP_PASS);
}
DEVI void p1_tail(const Params& p, LAS char* lds, int lane, int w, int tt, int grp, f32x4 (&acc)[2][5], f32x4 st1, f32x4 st2 STAMP_ARG) {
  rsrc_t ws = mk_rsrc(p.ws, WS_END);
  const unsigned l16 = lane * 16;
  const int g = lane >> 4, c = lane & 15;
  if (w < 5) {
    const float mean = st1[0] * (1.f / 640.f);
    const float e2 = fsel((c & 2) != 0, fsel((c & 1) != 0, st2[3], st2[2]), fsel((c & 1) != 0, st2[1], st2[0])) * (1.f / 640.f);
    const float rstd = __builtin_amdgcn_rsqf(fmaxf(e2 - mean * mean, 0.f) + 1e-5f);
    if (g == (c >> 2)) *(LAS v2u*)(lds + L_P1_ST + (w * 16 + c) * 8) = v2u{__float_as_uint(rstd), __float_as_uint(-mean * rstd)};
  }
  lbar();
  {
    v2u sv[5];
#pragma unroll
    for (int b = 0; b < 5; ++b) sv[b] = *(LAS v2u*)(lds + L_P1_ST + (b * 16 + c) * 8);
    float cs[2][4];
#pragma unroll
    for (int e = 0; e < 2; ++e)
#pragma unroll
      for (int q = 0; q < 4; ++q) cs[e][q] = __uint_as_float(__builtin_amdgcn_update_dpp(0u, __float_as_uint(acc[e][0][q]), 0x150, 0xf, 0xf, false));
    LAS char* d = lds + L_P1_EX + c * P1_RS + w * 64 + g * 8;
#pragma unroll
    for (int b = 0; b < 5; ++b) {
      const float rs = __uint_as_float(sv[b].x), mr = __uint_as_float(sv[b].y);
#pragma unroll
      for (int e = 0; e < 2; ++e)
        *(LAS v2u*)(d + b * 16 * P1_RS + e * 32) = v2u{pk2(fmaf(rs, acc[e][b][0], mr * cs[e][0]), fmaf(rs, acc[e][b][1], mr * cs[e][1])),
                                                       pk2(fmaf(rs, acc[e][b][2], mr * cs[e][2]), fmaf(rs, acc[e][b][3], mr * cs[e][3]))};
    }
  }
  if (DIAG != 4) STAMP(22);
  lbar();
  if (DIAG != 4) STAMP(23);
  const int blk = 4 * tt + (w & 3), pr = 2 * grp + (w >> 2);
  LAS char* own = lds + L_P1_EX + (((w & 3) + 1) * 16 + c) * P1_RS + (w >> 2) * 256 + g * 8;
  LAS char* trp = lds + L_P1_EX + (((w & 3) + 1) * 16 + 4 * g + (c >> 2)) * P1_RS + (w >> 2) * 256 + (c & 3) * 8;
  v2u ka[2], kp[2], qa[2], qr[2], va[2], vp[2];
  const v2u kr = *(LAS v2u*)(own + 64), krp = *(LAS v2u*)(own + 64 - 16 * P1_RS);
#pragma unroll
  for (int hh = 0; hh < 2; ++hh) {
    ka[hh] = *(LAS v2u*)(own + hh * 32); kp[hh] = *(LAS v2u*)(own + hh * 32 - 16 * P1_RS);
    qa[hh] = *(LAS v2u*)(own + (3 + hh) * 32);
    qr[hh] = *(LAS v2u*)(own - g * 8 + 64 + 16 + hh * 8);
    va[hh] = __builtin_bit_cast(v2u, __builtin_amdgcn_ds_read_tr16_b64_v4i16((LAS s16x4*)(trp + (5 + hh) * 32)));
    vp[hh] = __builtin_bit_cast(v2u, __builtin_amdgcn_ds_read_tr16_b64_v4i16((LAS s16x4*)(trp + (5 + hh) * 32 - 16 * P1_RS)));
  }
  const v2u vr = __builtin_bit_cast(v2u, __builtin_amdgcn_ds_read_tr16_b64_v4i16((LAS s16x4*)(trp + 7 * 32)));
  const v2u vrp = __builtin_bit_cast(v2u, __builtin_amdgcn_ds_read_tr16_b64_v4i16((LAS s16x4*)(trp + 7 * 32 - 16 * P1_RS)));
  if (DIAG != 4) STAMP(24);
  SCHED0();
  const int odd = c & 1, ce = c - odd;
  const bool havePrev = (tt > 0) || ((w & 3) > 0);
  const bool v1a = havePrev && (4 * g >= ce + 2), v1b = havePrev && (4 * g >= ce), v2a = ce >= 4 * g, v2b = ce - 2 >= 4 * g;
  const float c2 = 0.22360679774997896f * 1.4426950408889634f, NEG = -3.0e38f;
  const f32x4 z = {0.f, 0.f, 0.f, 0.f};
  f32x4 S1[2], S2[2];
#pragma unroll
  for (int hh = 0; hh < 2; ++hh) {
    const bool mine = g == hh;
    const v4u Q = {qa[hh].x, qa[hh].y, mine ? qr[hh].x : 0u, mine ? qr[hh].y : 0u};
    S1[hh] = mfma(v4u{kp[hh].x, kp[hh].y, krp.x, krp.y}, Q, z);
    S2[hh] = mfma(v4u{ka[hh].x, ka[hh].y, kr.x, kr.y}, Q, z);
  }
  float s1a[2], s1b[2], s2a[2], s2b[2], m[2], inv[2];
#pragma unroll
  for (int hh = 0; hh < 2; ++hh) {
    s1a[hh] = fsel(v1a, fsel(odd, S1[hh][1], S1[hh][0]), NEG); s1b[hh] = fsel(v1b, fsel(odd, S1[hh][3], S1[hh][2]), NEG);
    s2a[hh] = fsel(v2a, fsel(odd, S2[hh][1], S2[hh][0]), NEG); s2b[hh] = fsel(v2b, fsel(odd, S2[hh][3], S2[hh][2]), NEG);
    m[hh] = fmaxf(fmaxf(s1a[hh], s1b[hh]), fmaxf(s2a[hh], s2b[hh]));
  }
  {
    auto a0 = __builtin_amdgcn_permlane32_swap(__float_as_uint(m[0]), __float_as_uint(m[0]), false, false);
    auto a1 = __builtin_amdgcn_permlane32_swap(__float_as_uint(m[1]), __float_as_uint(m[1]), false, false);
    m[0] = fmaxf(__uint_as_float(a0[0]), __uint_as_float(a0[1])); m[1] = fmaxf(__uint_as_float(a1[0]), __uint_as_float(a1[1]));
    auto b0 = __builtin_amdgcn_permlane16_swap(__float_as_uint(m[0]), __float_as_uint(m[0]), false, false);
    auto b1 = __builtin_amdgcn_permlane16_swap(__float_as_uint(m[1]), __float_as_uint(m[1]), false, false);
    m[0] = fmaxf(__uint_as_float(b0[0]), __uint_as_float(b0[1])); m[1] = fmaxf(__uint_as_float(b1[0]), __uint_as_float(b1[1]));
  }
#pragma unroll
  for (int hh = 0; hh < 2; ++hh) {
    s1a[hh] = __builtin_amdgcn_exp2f((s1a[hh] - m[hh]) * c2); s1b[hh] = __builtin_amdgcn_exp2f((s1b[hh] - m[hh]) * c2);
    s2a[hh] = __builtin_amdgcn_exp2f((s2a[hh] - m[hh]) * c2); s2b[hh] = __builtin_amdgcn_exp2f((s2b[hh] - m[hh]) * c2);
    inv[hh] = (s1a[hh] + s1b[hh]) + (s2a[hh] + s2b[hh]);
  }
  {
    auto a0 = __builtin_amdgcn_permlane32_swap(__float_as_uint(inv[0]), __float_as_uint(inv[0]), false, false);
    auto a1 = __builtin_amdgcn_permlane32_swap(__float_as_uint(inv[1]), __float_as_uint(inv[1]), false, false);
    inv[0] = __uint_as_float(a0[0]) + __uint_as_float(a0[1]); inv[1] = __uint_as_float(a1[0]) + __uint_as_float(a1[1]);
    auto b0 = __builtin_amdgcn_permlane16_swap(__float_as_uint(inv[0]), __float_as_uint(inv[0]), false, false);
    auto b1 = __builtin_amdgcn_permlane16_swap(__float_as_uint(inv[1]), __float_as_uint(inv[1]), false, false);
    inv[0] = __builtin_amdgcn_rcpf(__uint_as_float(b0[0]) + __uint_as_float(b0[1])); inv[1] = __builtin_amdgcn_rcpf(__uint_as_float(b1[0]) + __uint_as_float(b1[1]));
  }
  if (DIAG != 4) STAMP(25);
  f32x4 OA[2], OR[2];
  const unsigned sh = 16 * odd;
#pragma unroll
  for (int hh = 0; hh < 2; ++hh) {
    const v4u P = {pk2(s1a[hh] * inv[hh], 0.f) << sh, pk2(s1b[hh] * inv[hh], 0.f) << sh, pk2(s2a[hh] * inv[hh], 0.f) << sh, pk2(s2b[hh] * inv[hh], 0.f) << sh};
    OA[hh] = mfma(v4u{vp[hh].x, vp[hh].y, va[hh].x, va[hh].y}, P, z);
    OR[hh] = mfma(v4u{vrp.x, vrp.y, vr.x, vr.y}, P, z);
  }
  {
    v4u om = {pk2(OA[0][0], OA[0][1]), pk2(OA[0][2], OA[0][3]), pk2(OA[1][0], OA[1][1]), pk2(OA[1][2], OA[1][3])};
    st16(ws, om, l16, WS_O + (unsigned)blk * 20480 + pr * 1024);
    unsigned r0x = pk2(OR[0][0], OR[0][1]), r0y = pk2(OR[0][2], OR[0][3]), r1x = pk2(OR[1][0], OR[1][1]), r1y = pk2(OR[1][2], OR[1][3]);
    auto t0 = __builtin_amdgcn_permlane16_swap(r1x, r1x, false, false); auto t1 = __builtin_amdgcn_permlane16_swap(r1y, r1y, false, false);
    v4u orr = {r0x, r0y, t0[1], t1[1]};
    if (lane < 16) st16(ws, orr, l16, WS_O + (unsigned)blk * 20480 + (16 + (pr >> 2)) * 1024 + (pr & 3) * 256);
  }
  STAMP(4);
}

DEVI void exchange_halves(LAS char* lds, const f32x4 (&acc)[5], f32x4 (&fin)[3], int lane, int w) {
  LAS char* d = lds + L_EXH + (w & 3) * 5120 + lane * 16;
  if (w < 4) { *(LAS f32x4*)(d + 3072) = acc[3]; *(LAS f32x4*)(d + 4096) = acc[4]; }
  else { *(LAS f32x4*)(d) = acc[0]; *(LAS f32x4*)(d + 1024) = acc[1]; *(LAS f32x4*)(d + 2048) = acc[2]; }
  lbar();
  if (w < 4) { f32x4 u0 = *(LAS f32x4*)(d), u1 = *(LAS f32x4*)(d + 1024), u2 = *(LAS f32x4*)(d + 2048); fin[0] = acc[0] + u0; fin[1] = acc[1] + u1; fin[2] = acc[2] + u2; }
  else { f32x4 u0 = *(LAS f32x4*)(d + 3072), u1 = *(LAS f32x4*)(d + 4096); fin[0] = acc[3] + u0; fin[1] = acc[4] + u1; fin[2] = f32x4{0.f, 0.f, 0.f, 0.f}; }
}

DEVI void phase2(const Params& p, LAS char* lds, int lane, int w STAMP_ARG) {
  const int x8 = blockIdx.x & 7, r = blockIdx.x >> 3;
  const int ti = 2 * x8 + (r >> 4), hb = (r >> 3) & 1, j = r & 7;
  rsrc_t ws = mk_rsrc(p.ws, WS_END);
  const v4u srd = mk_srd(p.ws, WS_END);
  const unsigned l16 = lane * 16;
  const int b = w & 3, kh = w >> 2, blk = 8 * ti + 4 * hb + b;
  rbar();
  STAMP(6);
  v4u a[10];
  const unsigned oa = WS_O + (unsigned)blk * 20480 + 10 * kh * 1024;
#pragma unroll
  for (int s = 0; s < 10; ++s) aload(a[s], srd, l16, oa + s * 1024);
  f32x4 acc[5];
#pragma unroll
  for (int t = 0; t < 5; ++t) acc[t] = f32x4{0.f, 0.f, 0.f, 0.f};
  v4u B[2][5];
  LAS char* bb = lds + kh * 51200 + l16;
#pragma unroll
  for (int t = 0; t < 5; ++t) B[0][t] = ldsr16(bb + t * 1024);
  SCHED0();
#pragma unroll
  for (int s = 0; s < 10; ++s) {
    if (s == 5) lbar();
    asm volatile("s_waitcnt vmcnt(%1)" : "+v"(a[s]) : "i"(9 - s) : "memory");
    if (s + 1 < 10) {
#pragma unroll
      for (int t = 0; t < 5; ++t) B[(s + 1) & 1][t] = ldsr16(bb + (s + 1) * 5120 + t * 1024);
    }
#pragma unroll
    for (int t = 0; t < 5; ++t) acc[t] = mfma(B[s & 1][t], a[s], acc[t]);
#pragma unroll
    for (int t = 0; t < 5; ++t) { __builtin_amdgcn_sched_group_barrier(0x100, 1, 0); __builtin_amdgcn_sched_group_barrier(0x8, 1, 0); }
    SCHED0();
  }
  STAMP(7);
  const int g = lane >> 4, c = lane & 15, row = 16 * b + c, mt0 = 3 * kh;
  LAS char* sx = lds + L_STASH + row * 160 + mt0 * 32 + g * 8;
  v2u xv[3];
#pragma unroll
  for (int e = 0; e < 3; ++e) xv[e] = *(LAS v2u*)(sx + (e < 2 || kh == 0 ? e : 0) * 32);
  f32x4 fin[3];
  exchange_halves(lds, acc, fin, lane, w);
  v2u hv[3];
  float ssum = 0.f, ssq = 0.f;
#pragma unroll
  for (int e = 0; e < 3; ++e) {
    hv[e] = v2u{pk2(fin[e][0] + bflo(xv[e].x), fin[e][1] + bfhi(xv[e].x)), pk2(fin[e][2] + bflo(xv[e].y), fin[e][3] + bfhi(xv[e].y))};
    if (e < 2 || kh == 0) {
      *(LAS v2u*)(sx + e * 32) = hv[e];
      const float h0 = bflo(hv[e].x), h1 = bfhi(hv[e].x), h2 = bflo(hv[e].y), h3 = bfhi(hv[e].y);
      ssum += (h0 + h1) + (h2 + h3); ssq += fmaf(h0, h0, h1 * h1) + fmaf(h2, h2, h3 * h3);
    }
  }
  {
    auto sx0 = __builtin_amdgcn_permlane16_swap(hv[0].x, hv[1].x, false, false); auto sy0 = __builtin_amdgcn_permlane16_swap(hv[0].y, hv[1].y, false, false);
    const int G = 2 * (mt0 + (g & 1)) + (g >> 1);
    st16(ws, v4u{sx0[0], sy0[0], sx0[1], sy0[1]}, c * 16 + (10 * j + G) * 256, WS_H + (unsigned)blk * 20480);
    if (kh == 0) __builtin_amdgcn_raw_buffer_store_b64(hv[2], ws, WS_H + (unsigned)blk * 20480 + (10 * j + 4 + (g >> 1)) * 256 + c * 16 + (g & 1) * 8, 0, 16);
  }
  ssum = xrow_sum(ssum); ssq = xrow_sum(ssq);
  {
    const int idx = 2 * j + kh;
    if (lane < 16) {
      v2u sv = {__float_as_uint(ssum), __float_as_uint(ssq)};
      __builtin_amdgcn_raw_buffer_store_b64(sv, ws, WS_ST + (unsigned)blk * 2048 + idx * 128 + c * 8, 0, 16);
    }
  }
  STAMP(8);
}

#define P3D 6
DEVI void phase3(const Params& p, LAS char* lds, int lane, int w STAMP_ARG) {
  const int x8 = blockIdx.x & 7, r = blockIdx.x >> 3;
  const int ti = 4 * (x8 >> 1) + (r >> 3), jj = 8 * (x8 & 1) + (r & 7);
  rsrc_t ws = mk_rsrc(p.ws, WS_END);
  const v4u srd = mk_srd(p.ws, WS_END);
  const unsigned l16 = lane * 16;
  const int blk = 8 * ti + w;
  rbar();
  if (w < 6) {
    v4u cf[20];
#pragma unroll
    for (int ks = 0; ks < 20; ++ks) cf[ks] = ldsr16(lds + (ks * 6 + w) * 1024 + l16);
    SCHED0();
    f32x4 cs0 = {0.f, 0.f, 0.f, 0.f}, cs1 = cs0;
    const v4u ones = {0x3f803f80u, 0x3f803f80u, 0x3f803f80u, 0x3f803f80u};
#pragma unroll
    for (int ks = 0; ks < 20; ks += 2) { cs0 = mfma(cf[ks], ones, cs0); cs1 = mfma(cf[ks + 1], ones, cs1); }
    if ((lane & 15) == 0) *(LAS f32x4*)(lds + L_P3_B1 + 1024 + (w * 16 + (lane >> 4) * 4) * 4) = cs0 + cs1;
  }
  lbar();
  STAMP(10);
  v4u a[20];
  v2u st[4];
  const unsigned sto = WS_ST + (unsigned)blk * 2048 + (lane >> 4) * 512 + (lane & 15) * 8;
  const unsigned ha = WS_H + (unsigned)blk * 20480;
#pragma unroll
  for (int s = 0; s < P3D; ++s) aload(a[s], srd, l16, ha + s * 1024);
  f32x4 acc[6];
#pragma unroll
  for (int t = 0; t < 6; ++t) acc[t] = f32x4{0.f, 0.f, 0.f, 0.f};
  v4u B[2][4];
  LAS char* bb = lds + l16;
#define P3_RD(s, t) ldsr16(bb + (s) * 6144 + (t) * 1024)
#pragma unroll
  for (int t = 0; t < 4; ++t) B[0][t] = P3_RD(0, t);
  SCHED0();
#pragma unroll
  for (int s = 0; s < 20; ++s) {
    asm volatile("s_waitcnt vmcnt(%1)" : "+v"(a[s]) : "i"(s + P3D - 1 < 24 ? P3D - 1 : 23 - s) : "memory");
    if (s + P3D < 20) aload(a[s + P3D], srd, l16, ha + (s + P3D) * 1024);
    else if (s + P3D < 24) asm volatile("buffer_load_dwordx2 %0, %1, %2, 0 offen sc1" : "=v"(st[s + P3D - 20]) : "v"(sto + (s + P3D - 20) * 128), "s"(srd) : "memory");
    if (s + 1 < 20) {
#pragma unroll
      for (int t = 0; t < 4; ++t) B[(s + 1) & 1][t] = P3_RD(s + 1, t);
    }
#pragma unroll
    for (int t = 0; t < 4; ++t) acc[t] = mfma(B[s & 1][t], a[s], acc[t]);
#pragma unroll
    for (int t = 0; t < 4; ++t) { __builtin_amdgcn_sched_group_barrier(0x100, 1, 0); __builtin_amdgcn_sched_group_barrier(0x8, 1, 0); }
    SCHED0();
  }
  lbar();
  const int g = lane >> 4;
  f32x4 cs[6]; v2u bb1[6];
#pragma unroll
  for (int t = 0; t < 4; ++t) { cs[t] = *(LAS f32x4*)(lds + L_P3_B1 + 1024 + (t * 16 + g * 4) * 4); bb1[t] = *(LAS v2u*)(lds + L_P3_B1 + (t * 16 + g * 4) * 2); }
  v4u C[2][4];
#define P3_RD2(s2) do { C[(s2) & 1][0] = P3_RD(2 * (s2), 4); C[(s2) & 1][1] = P3_RD(2 * (s2), 5); C[(s2) & 1][2] = P3_RD(2 * (s2) + 1, 4); C[(s2) & 1][3] = P3_RD(2 * (s2) + 1, 5); } while (0)
  P3_RD2(0);
  WAITV4(0, st[0], st[1], st[2], st[3]);
  SCHED0();
  float s1 = (__uint_as_float(st[0].x) + __uint_as_float(st[1].x)) + (__uint_as_float(st[2].x) + __uint_as_float(st[3].x));
  float s2 = (__uint_as_float(st[0].y) + __uint_as_float(st[1].y)) + (__uint_as_float(st[2].y) + __uint_as_float(st[3].y));
  s1 = xrow_sum(s1); s2 = xrow_sum(s2);
  const float mean = s1 * (1.f / 640.f);
  const float var = fmaxf(s2 * (1.f / 640.f) - mean * mean, 0.f);
  const float rstd = __builtin_amdgcn_rsqf(var + 1e-5f), mr = -mean * rstd;
#define P3_OUT(t, o0, o1) do { \
    const float y0 = fmaxf(fmaf(rstd, acc[t][0], fmaf(mr, cs[t][0], bflo(bb1[t].x))), 0.f), y1 = fmaxf(fmaf(rstd, acc[t][1], fmaf(mr, cs[t][1], bfhi(bb1[t].x))), 0.f); \
    const float y2 = fmaxf(fmaf(rstd, acc[t][2], fmaf(mr, cs[t][2], bflo(bb1[t].y))), 0.f), y3 = fmaxf(fmaf(rstd, acc[t][3], fmaf(mr, cs[t][3], bfhi(bb1[t].y))), 0.f); \
    o0 = pk2(y0 * y0, y1 * y1); o1 = pk2(y2 * y2, y3 * y3); } while (0)
  const unsigned h1o = WS_H1 + (unsigned)blk * 49152 + 3 * jj * 1024;
#pragma unroll
  for (int t = 4; t < 6; ++t) { cs[t] = *(LAS f32x4*)(lds + L_P3_B1 + 1024 + (t * 16 + g * 4) * 4); bb1[t] = *(LAS v2u*)(lds + L_P3_B1 + (t * 16 + g * 4) * 2); }
  v4u o0, o1;
  SCHED0();
#pragma unroll
  for (int s2 = 0; s2 < 10; ++s2) {
    if (s2 + 1 < 10) P3_RD2(s2 + 1);
    acc[4] = mfma(C[s2 & 1][0], a[2 * s2], acc[4]); acc[5] = mfma(C[s2 & 1][1], a[2 * s2], acc[5]);
    acc[4] = mfma(C[s2 & 1][2], a[2 * s2 + 1], acc[4]); acc[5] = mfma(C[s2 & 1][3], a[2 * s2 + 1], acc[5]);
    if (s2 == 0) P3_OUT(0, o0[0], o0[1]);
    if (s2 == 1) { P3_OUT(1, o0[2], o0[3]); st16(ws, o0, l16, h1o); }
    if (s2 == 2) P3_OUT(2, o1[0], o1[1]);
    if (s2 == 3) { P3_OUT(3, o1[2], o1[3]); st16(ws, o1, l16, h1o + 1024); }
#pragma unroll
    for (int t = 0; t < 4; ++t) {
      __builtin_amdgcn_sched_group_barrier(0x100, 1, 0); __builtin_amdgcn_sched_group_barrier(0x8, 1, 0);
      if (s2 < 4) __builtin_amdgcn_sched_group_barrier(0x2, 8, 0);
    }
    SCHED0();
  }
#undef P3_RD2
#undef P3_RD
  STAMP(11);
  {
    v4u o2;
    P3_OUT(4, o2[0], o2[1]); P3_OUT(5, o2[2], o2[3]);
    st16(ws, o2, l16, h1o + 2048);
  }
  lbar();
#undef P3_OUT
  STAMP(12);
}

#define P4D 6
constexpr unsigned w2_slot(int sl, int f) { return sl == 2 ? (f >> 1) * 6144 + (4 + (f & 1)) * 1024 : (10 * sl + (f >> 2)) * 6144 + (f & 3) * 1024; }
DEVI void phase4(const Params& p, LAS char* lds, int lane, int w STAMP_ARG) {
  const int x8 = blockIdx.x & 7, r = blockIdx.x >> 3;
  const int ti = 2 * x8 + (r >> 4), hb = (r >> 3) & 1, j = r & 7;
  const v4u srd = mk_srd(p.ws, WS_END);
  const unsigned l16 = lane * 16;
  const int b = w & 3, kh = w >> 2, blk = 8 * ti + 4 * hb + b;
  rbar();
  STAMP(14);
  const unsigned ha = WS_H1 + (unsigned)blk * 49152 + kh * 24 * 1024;
  v4u a[24];
#pragma unroll
  for (int s = 0; s < P4D; ++s) aload(a[s], srd, l16, ha + s * 1024);
  f32x4 acc[5];
#pragma unroll
  for (int t = 0; t < 5; ++t) acc[t] = f32x4{0.f, 0.f, 0.f, 0.f};
  v4u B[2][5];
  LAS char* bb01 = lds + kh * 5 * 6144 + l16;
  LAS char* bb2 = lds + kh * 10 * 6144 + l16;
#define P4_READ(s) do { _Pragma("unroll") for (int t = 0; t < 5; ++t) B[(s) & 1][t] = \
    ldsr16((((s) >> 2) % 3 == 2 ? bb2 : bb01) + w2_slot(((s) >> 2) % 3, ((s) & 3) * 5 + t)); } while (0)
  P4_READ(0);
  SCHED0();
#pragma unroll
  for (int s = 0; s < 24; ++s) {
    if ((s & 3) == 3 && s < 23) lbar();
    asm volatile("s_waitcnt vmcnt(%1)" : "+v"(a[s]) : "i"(s + P4D - 1 < 24 ? P4D - 1 : 23 - s) : "memory");
    if (s + P4D < 24) aload(a[s + P4D], srd, l16, ha + (s + P4D) * 1024);
    if (s + 1 < 24) P4_READ(s + 1);
#pragma unroll
    for (int t = 0; t < 5; ++t) acc[t] = mfma(B[s & 1][t], a[s], acc[t]);
#pragma unroll
    for (int t = 0; t < 5; ++t) { __builtin_amdgcn_sched_group_barrier(0x100, 1, 0); __builtin_amdgcn_sched_group_barrier(0x8, 1, 0); }
    SCHED0();
  }
#undef P4_READ
  STAMP(15);
  const int g = lane >> 4, c = lane & 15, row = 16 * b + c, mt0 = 3 * kh;
  LAS char* sx = lds + L_STASH + row * 160 + mt0 * 32 + g * 8;
  v2u hv[3], bv[3];
#pragma unroll
  for (int e = 0; e < 3; ++e) {
    const int ee = (e < 2 || kh == 0) ? e : 0;
    hv[e] = *(LAS v2u*)(sx + ee * 32);
    bv[e] = *(LAS v2u*)(lds + L_P4_B2 + (mt0 + ee) * 32 + g * 8);
  }
  f32x4 fin[3];
  exchange_halves(lds, acc, fin, lane, w);
#pragma unroll
  for (int e = 0; e < 3; ++e) {
    if (e < 2 || kh == 0)
      *(LAS v2u*)(sx + e * 32) = v2u{pk2(fin[e][0] + bflo(bv[e].x) + bflo(hv[e].x), fin[e][1] + bfhi(bv[e].x) + bfhi(hv[e].x)),
                                     pk2(fin[e][2] + bflo(bv[e].y) + bflo(hv[e].y), fin[e][3] + bfhi(bv[e].y) + bfhi(hv[e].y))};
  }
  lbar();
  {
    const int q0 = w * 64 + lane, q1 = q0 + 512;
    const v4u o0 = *(LAS v4u*)(lds + L_STASH + q0 * 16);
    v4u o1 = o0; if (w < 2) o1 = *(LAS v4u*)(lds + L_STASH + q1 * 16);
    const int r0 = q0 / 10, r1 = q1 / 10;
    rsrc_t outr = mk_rsrc(p.out, T_ * 1280);
    const unsigned ob = (unsigned)(128 * ti + 64 * hb) * 1280 + 160 * j;
    __builtin_amdgcn_raw_buffer_store_b128(o0, outr, ob + r0 * 1280 + (q0 - r0 * 10) * 16, 0, 0);
    if (w < 2) __builtin_amdgcn_raw_buffer_store_b128(o1, outr, ob + r1 * 1280 + (q1 - r1 * 10) * 16, 0, 0);
  }
  STAMP(16);
}

DEVI unsigned ldsaddr(LAS char* p) { return (unsigned)(uintptr_t)p; }
DEVI void convert5(LAS char* u, int lane) {
  const int g = lane >> 4;
  const unsigned lo = ldsaddr(u) + (8 * g + 4 * (g & 1) + ((lane & 15) >> 2)) * 160 + (lane & 3) * 8, hi = lo + ((g & 1) ? -640 : 640), d = ldsaddr(u) + lane * 16;
  v2u a0, a1, a2, a3, a4, b0, b1, b2, b3, b4;
  asm volatile("ds_read_b64_tr_b16 %0, %10\n\tds_read_b64_tr_b16 %5, %11\n\tds_read_b64_tr_b16 %1, %10 offset:32\n\tds_read_b64_tr_b16 %6, %11 offset:32\n\t"
               "ds_read_b64_tr_b16 %2, %10 offset:64\n\tds_read_b64_tr_b16 %7, %11 offset:64\n\tds_read_b64_tr_b16 %3, %10 offset:96\n\tds_read_b64_tr_b16 %8, %11 offset:96\n\t"
               "ds_read_b64_tr_b16 %4, %10 offset:128\n\tds_read_b64_tr_b16 %9, %11 offset:128\n\ts_waitcnt lgkmcnt(0)"
               : "=&v"(a0), "=&v"(a1), "=&v"(a2), "=&v"(a3), "=&v"(a4), "=&v"(b0), "=&v"(b1), "=&v"(b2), "=&v"(b3), "=&v"(b4) : "v"(lo), "v"(hi) : "memory");
  const v4u f0 = {a0.x, a0.y, b0.x, b0.y}, f1 = {a1.x, a1.y, b1.x, b1.y}, f2 = {a2.x, a2.y, b2.x, b2.y}, f3 = {a3.x, a3.y, b3.x, b3.y}, f4 = {a4.x, a4.y, b4.x, b4.y};
  asm volatile("ds_write_b128 %0, %1\n\tds_write_b128 %0, %2 offset:1024\n\tds_write_b128 %0, %3 offset:2048\n\tds_write_b128 %0, %4 offset:3072\n\tds_write_b128 %0, %5 offset:4096\n\ts_waitcnt lgkmcnt(0)"
               ::"v"(d), "v"(f0), "v"(f1), "v"(f2), "v"(f3), "v"(f4) : "memory");
}
DEVI void convert6(LAS char* u, int lane) {
  const int g = lane >> 4, od = g & 1;
  const unsigned row = ldsaddr(u) + (8 * g + ((lane & 15) >> 2)) * 192 + (lane & 3) * 8, lo = row + 32 * od, l5 = row + (od ? 0 : 160), d = ldsaddr(u) + lane * 16;
  v2u a0, a1, a2, a3, a4, a5, b0, b1, b2, b3, b4, b5;
  asm volatile("ds_read_b64_tr_b16 %0, %12\n\tds_read_b64_tr_b16 %6, %12 offset:768\n\tds_read_b64_tr_b16 %1, %12 offset:32\n\tds_read_b64_tr_b16 %7, %12 offset:800\n\t"
               "ds_read_b64_tr_b16 %2, %12 offset:64\n\tds_read_b64_tr_b16 %8, %12 offset:832\n\tds_read_b64_tr_b16 %3, %12 offset:96\n\tds_read_b64_tr_b16 %9, %12 offset:864\n\t"
               "ds_read_b64_tr_b16 %4, %12 offset:128\n\tds_read_b64_tr_b16 %10, %12 offset:896\n\tds_read_b64_tr_b16 %5, %13\n\tds_read_b64_tr_b16 %11, %13 offset:768\n\ts_waitcnt lgkmcnt(0)"
               : "=&v"(a0), "=&v"(a1), "=&v"(a2), "=&v"(a3), "=&v"(a4), "=&v"(a5), "=&v"(b0), "=&v"(b1), "=&v"(b2), "=&v"(b3), "=&v"(b4), "=&v"(b5) : "v"(lo), "v"(l5) : "memory");
  const v4u f0 = {a0.x, a0.y, b0.x, b0.y}, f1 = {a1.x, a1.y, b1.x, b1.y}, f2 = {a2.x, a2.y, b2.x, b2.y}, f3 = {a3.x, a3.y, b3.x, b3.y}, f4 = {a4.x, a4.y, b4.x, b4.y}, f5 = {a5.x, a5.y, b5.x, b5.y};
  asm volatile("ds_write_b128 %0, %1\n\tds_write_b128 %0, %2 offset:1024\n\tds_write_b128 %0, %3 offset:2048\n\tds_write_b128 %0, %4 offset:3072\n\tds_write_b128 %0, %5 offset:4096\n\t"
               "ds_write_b128 %0, %6 offset:5120\n\ts_waitcnt lgkmcnt(0)"
               ::"v"(d), "v"(f0), "v"(f1), "v"(f2), "v"(f3), "v"(f4), "v"(f5) : "memory");
}

DEVI void loader(const Params& p, LAS char* lds, int lane, int lw STAMP_ARG) {
  const int x8 = blockIdx.x & 7, r = blockIdx.x >> 3;
  const int t2d = 4 * (x8 >> 1) + (r >> 3), c2d = 8 * (x8 & 1) + (r & 7);
  const int tl = 2 * x8 + (r >> 4), hb = (r >> 3) & 1, j = r & 7;
  rsrc_t ws = mk_rsrc(p.ws, WS_END);
  const v4u srd = mk_srd(p.ws, WS_END);
  const unsigned l16 = lane * 16;
  const int tt = 8 * (x8 >> 1) + (r >> 2), grp = 4 * (x8 & 1) + (r & 3);
  rsrc_t xs = mk_rsrc(p.x, T_ * 1280), wqs = mk_rsrc(p.wqkv, 640u * 3840u);
  unsigned vx[3], vw[8];
#pragma unroll
  for (int i = 0; i < 3; ++i) {
    const int m = lw + 4 * i, row = 8 * m + (lane >> 3);
    int tok = 64 * tt - 16 + row; if (tok < 0) tok = 0; if (tok > T_ - 1) tok = T_ - 1;
    vx[i] = (unsigned)tok * 1280 + (((lane & 7) ^ (row & 7)) * 16);
  }
  const int wn0 = lw < 2 ? lw * 7 - 1 : 14 + (lw - 2) * 8;
#pragma unroll
  for (int i = 0; i < 8; ++i) {
    int n = wn0 + i; if (n < 0) n = 0;
    const int q = n * 64 + lane, rho = (q * 2185) >> 16, ch = q - rho * 30, sec = (ch * 205) >> 11;
    const int gg = (rho >> 3) & 3, krow = (rho & 32) + 8 * gg + 4 * (((rho >> 2) & 1) ^ (gg & 1)) + (rho & 3);
    vw[i] = (unsigned)krow * 3840u + sec * 1280 + 160 * grp + 16 * (ch - sec * 10);
  }
#define P1_STG(s) do { LAS char* sb_ = lds + ((s) % 3) * P1_STAGE; \
    dma16<0>(xs, sb_ + lw * 1024, vx[0], (s) * 128); dma16<0>(xs, sb_ + (lw + 4) * 1024, vx[1], (s) * 128); \
    if (lw < 2) dma16<0>(xs, sb_ + (lw + 8) * 1024, vx[2], (s) * 128); else dma16<0>(wqs, sb_ + 10240 + wn0 * 1024, vw[0], (s) * 64 * 3840); \
    _Pragma("unroll") for (int i = 1; i < 8; ++i) dma16<0>(wqs, sb_ + 10240 + (wn0 + i) * 1024, vw[i], (s) * 64 * 3840); } while (0)
  STAMP(17);
  P1_STG(0);
  STAMP(18);
  if (PRO == 0) P1_STG(1);
  STAMP(19);
  if (PRO == 0) WAITV(10); else WAITV(0);
  STAMP(20);
  rbar();
  const u64 tag = rfl64(*(LAS u64*)(lds + L_TAG));
  if (lw == 0 && lane < 4) *((LAS unsigned*)(lds + L_CNT) + lane) = 0u;
  STAMP(2);
  if (PRO == 1) P1_STG(1);
  P1_STG(2);
  rsrc_t wos = mk_rsrc(p.wo, 640u * 1280u), w1s = mk_rsrc(p.w1, 640u * 3072u);
  unsigned vA[5], vB[5], vD[6];
#pragma unroll
  for (int i = 0; i < 5; ++i) {
    const int q = 64 * i + lane, rho = (q * 6554) >> 16, cc = q - rho * 10, g = rho >> 3, t = ((rho >> 2) & 1) ^ (g & 1), e = rho & 3;
    const unsigned col = 160 * j + 16 * cc;
    vA[i] = (unsigned)(20 * t + 4 * g + e) * 1280u + col;
    vB[i] = (unsigned)(40 * g + 20 * t + 16 + e) * 1280u + col;
  }
#pragma unroll
  for (int i = 0; i < 6; ++i) {
    const int q = 64 * i + lane, rho = (q * 5462) >> 16, pos = q - rho * 12, cc = ((rho >> 3) & 1) ? (pos < 2 ? pos + 10 : pos - 2) : pos;
    vD[i] = (unsigned)rho * 3072u + 192 * c2d + 16 * cc;
  }
#define WO_KS(ks) do { _Pragma("unroll") for (int i = 0; i < 5; ++i) {     if ((ks) < 16) dma16<0>(wos, lds + (ks) * 5120 + i * 1024, vA[i], (ks) * 40 * 1280); else dma16<0>(wos, lds + (ks) * 5120 + i * 1024, vB[i], ((ks) - 16) * 160 * 1280); } } while (0)
#define W1_KS(ks) do { _Pragma("unroll") for (int i = 0; i < 6; ++i) dma16<0>(w1s, lds + (ks) * 6144 + i * 1024, vD[i], (ks) * 32 * 3072); } while (0)
  const unsigned w2 = WS_W2 + (unsigned)j * 245760;
#define W2_STG(st) do { _Pragma("unroll") for (int i = 0; i < 10; ++i) { const int f = lw + 4 * i; \
    dma16<16>(ws, lds + ((st) % 3 == 2 ? (f >> 1) * 6144 + (4 + (f & 1)) * 1024 : (10 * ((st) % 3) + (f >> 2)) * 6144 + (f & 3) * 1024), l16, w2 + (st) * 40960 + f * 1024); } } while (0)
  const int uslot = blockIdx.x + 256 * (3 - lw);
  const bool haveu = lw >= 2 && uslot < 384;
  LAS char* ust = lds + L_P0 + (lw & 1) * 5120;
#pragma unroll
  for (int q = 1; q <= 9; ++q) {
    if (q == 1) WAITL0();
    if (q >= 8 && haveu) WAITV(15); else WAITV(10);
    rbar();
    if (q + 2 <= 9) P1_STG(q + 2);
    if (q == 7 && haveu) w2_unit_issue(p, ust, lane, uslot & 7, uslot >> 3);
    if (q == 8) { WO_KS(8 + lw); WO_KS(12 + lw); }
  }
  if (haveu) { WAITV(10); w2_unit_store(p, ust, lane, uslot & 7, uslot >> 3); }
#undef P1_STG
  STAMP(3);
  rbar();
  rbar();
  WO_KS(16 + lw); WO_KS(lw); WO_KS(4 + lw);
#pragma unroll
  for (int m = 0; m < 3; ++m) {
    const int n = lw + 4 * m;
    if (n < 10) { const int q = n * 64 + lane, row = (q * 6554) >> 16, ch = q - row * 10;
      dma16<0>(xs, lds + L_STASH + n * 1024, (unsigned)(128 * tl + 64 * hb + row) * 1280 + 160 * j + 16 * ch, 0); }
  }
  STAMP(5);
  if (lw < 2) WAITV(18); else WAITV(17);
  if (haveu && lane == 0) publish(p.ws, WS_F0B + ((uslot & 7) * 64 + (uslot >> 3)) * F0STR, tag);
  convert5(lds + (8 + lw) * 5120, lane); convert5(lds + (12 + lw) * 5120, lane);
  WAITV(0);
  convert5(lds + (16 + lw) * 5120, lane); convert5(lds + lw * 5120, lane); convert5(lds + (4 + lw) * 5120, lane);
  if (lw == 0) wait_flags(p.ws, WS_F1 + (tl * 16 + hb * 8 + lane) * FSTR, lane < 8, tag);
  lbar();
  STAMP(6);
  if (lw == 0) { rsrc_t bs = mk_rsrc(p.b1, F_ * 2); dma16<0>(bs, lds + L_P3_B1, lane < 12 ? 192 * c2d + lane * 16 : 0x7fffffffu, 0); }
  if (lw < 3) W1_KS(17 + lw);
  rbar();
  W1_KS(lw); W1_KS(9 + lw); if (lw == 3) W1_KS(4);
  rbar();
  W1_KS(5 + lw); W1_KS(13 + lw);
  STAMP(9);
  WAITV(12);
  if (lw < 3) convert6(lds + (17 + lw) * 6144, lane);
  convert6(lds + lw * 6144, lane); convert6(lds + (9 + lw) * 6144, lane); if (lw == 3) convert6(lds + 4 * 6144, lane);
  WAITV(0);
  convert6(lds + (5 + lw) * 6144, lane); convert6(lds + (13 + lw) * 6144, lane);
  lbar();
  if (lw == 0) wait_flags(p.ws, WS_F2 + (t2d * 16 + lane) * FSTR, lane < 16, tag);
  rbar();
  STAMP(10);
  wait_flags(p.ws, WS_F0B + (j * 64 + lane) * F0STR, lane < 48, tag);
  if (lw == 0) { rsrc_t bs = mk_rsrc(p.b2, D_ * 2); dma16<0>(bs, lds + L_P4_B2, lane < 10 ? 160 * j + lane * 16 : 0x7fffffffu, 0); }
  if (blockIdx.x == 0 && lw == 1) {
    for (int q = 0; q < 4; ++q) wait_flags(p.ws, WS_F1 + (q * 64 + lane) * FSTR, true, tag);
    if (lane == 0) publish(p.ws, WS_NONCE, tag);
  }
  rbar();
  W2_STG(0); W2_STG(1);
  rbar();
  STAMP(11);
  W2_STG(2);
  STAMP(13);
  if (lw == 0) wait_flags(p.ws, WS_F3 + (tl * 16 + lane) * FSTR, lane < 16, tag);
  WAITV(0); rbar();
  STAMP(14);
  rbar(); W2_STG(3);
  rbar(); W2_STG(4);
  WAITV(10); rbar(); W2_STG(5);
  WAITV(10); rbar();
  WAITV(0); rbar();
#undef W2_STG
#undef WO_KS
#undef W1_KS
  STAMP(15);
  rbar();
  rbar();
  STAMP(16);
}

DEVI void wg_publish(const Params& p, LAS char* lds, int k, int lane, unsigned off, u64 tag STAMP_ARG) {
  WAITV(0);
  if (lane == 0) {
    const unsigned old = __hip_atomic_fetch_add((LAS unsigned*)(lds + L_CNT) + k, 1u, __ATOMIC_RELAXED, __HIP_MEMORY_SCOPE_WORKGROUP);
    if (old == 7u) publish(p.ws, off, tag);
  }
}

__global__ __launch_bounds__(768, 3) __attribute__((amdgpu_waves_per_eu(3, 3))) void fused_block(Params p) {
  extern __shared__ __attribute__((aligned(16))) char smem[];
  LAS char* lds = (LAS char*)smem;
  const int lane = threadIdx.x & 63, w = __builtin_amdgcn_readfirstlane(threadIdx.x >> 6);
  const int x8 = blockIdx.x & 7, r = blockIdx.x >> 3;
  u64 tag = 0;
  STAMP(0);
  if (w >= 8) loader(p, lds, lane, w - 8 STAMP_PASS);
  else {

    if (CPRIO) __builtin_amdgcn_s_setprio(CPRIO);
    const int t2d = 4 * (x8 >> 1) + (r >> 3), c2d = 8 * (x8 & 1) + (r & 7), tl = 2 * x8 + (r >> 4);
    phase1(p, lds, lane, w, tag STAMP_PASS);
    { const int tt = 8 * (x8 >> 1) + (r >> 2), grp = 4 * (x8 & 1) + (r & 3);
      wg_publish(p, lds, 0, fresh_lane(), WS_F1 + ((tt >> 1) * 16 + (tt & 1) * 8 + grp) * FSTR, tag STAMP_PASS); }
    STAMP(5);
    phase2(p, lds, fresh_lane(), w STAMP_PASS);
    wg_publish(p, lds, 1, fresh_lane(), WS_F2 + (tl * 16 + (r & 15)) * FSTR, tag STAMP_PASS);
    STAMP(9);
    phase3(p, lds, fresh_lane(), w STAMP_PASS);
    wg_publish(p, lds, 2, fresh_lane(), WS_F3 + (t2d * 16 + c2d) * FSTR, tag STAMP_PASS);
    STAMP(13);
    phase4(p, lds, fresh_lane(), w STAMP_PASS);
  }
}

extern "C" void kernel_launch(void* const* d_in, const int* in_sizes, int n_in, void* d_out, int out_size, void* d_ws, size_t ws_size, hipStream_t stream) {
  Params p;
  p.x = (const unsigned short*)d_in[0]; p.wqkv = (const unsigned short*)d_in[1]; p.wo = (const unsigned short*)d_in[2];
  p.w1 = (const unsigned short*)d_in[4]; p.b1 = (const unsigned short*)d_in[5]; p.w2 = (const unsigned short*)d_in[6]; p.b2 = (const unsigned short*)d_in[7];
  p.out = (unsigned short*)d_out; p.ws = (unsigned char*)d_ws;
  hipFuncSetAttribute((const void*)fused_block, hipFuncAttributeMaxDynamicSharedMemorySize, LDS_BYTES);
  hipLaunchKernelGGL(fused_block, dim3(256), dim3(768), LDS_BYTES, stream, p);
}
```

```cpp
#include <hip/hip_runtime.h>
#include <hip/hip_bf16.h>
#include <stdint.h>

#define GAS __attribute__((address_space(1)))
#define LAS __attribute__((address_space(3)))
#define DEVI __device__ __forceinline__
typedef unsigned v4u __attribute__((ext_vector_type(4)));
typedef unsigned v2u __attribute__((ext_vector_type(2)));
typedef float f32x4 __attribute__((ext_vector_type(4)));
typedef short bf16x8 __attribute__((ext_vector_type(8)));
typedef short s16x4 __attribute__((ext_vector_type(4)));
typedef __bf16 bf16x2 __attribute__((ext_vector_type(2)));
typedef unsigned long long u64;
typedef GAS u64 gu64;
#define RLX_AGENT __ATOMIC_RELAXED, __HIP_MEMORY_SCOPE_AGENT

constexpr int POLL_SLEEP = 4;
constexpr int CPRIO = 2;
constexpr int LPRIO = 3;
constexpr int TOUCH_SLEEP = 2, TOUCH2_SLEEP = 10, WT_SPREAD = 5;
constexpr int P3D = 6, P4D = 6;

constexpr int T_ = 2048, D_ = 640, F_ = 1536;
constexpr unsigned WS_NONCE = 0;
constexpr unsigned WS_F0B = 4096;
constexpr unsigned FSTR = 128;
constexpr unsigned WS_F1 = 32768;
constexpr unsigned WS_F2 = WS_F1 + 256 * FSTR;
constexpr unsigned WS_F3 = WS_F2 + 256 * FSTR;
constexpr unsigned WS_W2 = WS_F3 + 256 * FSTR;
constexpr unsigned WS_O = WS_W2 + 8 * 48 * 5 * 1024;
constexpr unsigned WS_H = WS_O + 128 * 20480;
constexpr unsigned WS_H1 = WS_H + 128 * 20480;
constexpr unsigned WS_ST = WS_H1 + 128 * 49152;
constexpr unsigned WS_END = WS_ST + 128 * 2048;
constexpr unsigned LDS_BYTES = 163840;
constexpr unsigned P1_STAGE = 40960;
constexpr unsigned L_P1_EX = 102400;
constexpr unsigned P1_RS = 520;
constexpr unsigned L_UNIT = 122880;
constexpr unsigned L_P1_ST = 144000;
constexpr unsigned L_P3_B1 = 122880;
constexpr unsigned L_EXH = 124928;
constexpr unsigned L_P4_B2 = 145408;
constexpr unsigned L_CNT = 147456;
constexpr unsigned L_JUNK = 147520;
constexpr unsigned L_STASH = 153600;
constexpr unsigned L_TAG = 153600;

struct Params {
  const unsigned short* x; const unsigned short* wqkv; const unsigned short* wo; const unsigned short* w1; const unsigned short* b1;
  const unsigned short* w2; const unsigned short* b2; unsigned short* out; unsigned char* ws;
};

typedef __amdgpu_buffer_rsrc_t rsrc_t;
DEVI rsrc_t mk_rsrc(const void* p, unsigned bytes) { return __builtin_amdgcn_make_buffer_rsrc((void*)p, 0, bytes, 0x00020000); }
DEVI v4u mk_srd(const void* p, unsigned bytes) {
  u64 a = (u64)p; v4u r; r.x = (unsigned)a; r.y = (unsigned)(a >> 32) & 0xffffu; r.z = bytes; r.w = 0x00020000u; return r;
}
template <int AUX> DEVI void dma16(rsrc_t rs, LAS char* lds, unsigned voff, unsigned soff) {
  __builtin_amdgcn_raw_ptr_buffer_load_lds(rs, (LAS void*)lds, 16, voff, soff, 0, AUX);
}
DEVI void dma16_asm(v4u srd, unsigned ldsbase, unsigned voff) {
  unsigned keep;
  asm volatile("s_mov_b32 %0, m0\n\ts_mov_b32 m0, %3\n\ts_nop 0\n\tbuffer_load_dwordx4 %1, %2, 0 offen lds\n\ts_mov_b32 m0, %0"
               : "=&s"(keep) : "v"(voff), "s"(srd), "s"(ldsbase) : "memory");
}
DEVI void aload(v4u& d, v4u srd, unsigned voff, unsigned soff) {
  asm volatile("buffer_load_dwordx4 %0, %1, %2, %3 offen sc1" : "=v"(d) : "v"(voff), "s"(srd), "s"(soff) : "memory");
}
DEVI void st16(rsrc_t rs, v4u v, unsigned voff, unsigned soff) { __builtin_amdgcn_raw_buffer_store_b128(v, rs, voff + soff, 0, 16); }
#define WAITV(n) asm volatile("s_waitcnt vmcnt(%0)" ::"i"(n) : "memory")
#define WAITV4(n, a, b, c, d) asm volatile("s_waitcnt vmcnt(%4)" : "+v"(a), "+v"(b), "+v"(c), "+v"(d) : "i"(n) : "memory")
#define WAITL0() asm volatile("s_waitcnt lgkmcnt(0)" ::: "memory")
#define BAR() __builtin_amdgcn_s_barrier()
#define SCHED0() __builtin_amdgcn_sched_barrier(0)
DEVI void lbar() { WAITL0(); BAR(); asm volatile("" ::: "memory"); }
DEVI void rbar() { asm volatile("" ::: "memory"); BAR(); asm volatile("" ::: "memory"); }

DEVI unsigned pk2(float a, float b) { bf16x2 p; p[0] = (__bf16)a; p[1] = (__bf16)b; return __builtin_bit_cast(unsigned, p); }
DEVI float bflo(unsigned u) { return __uint_as_float(u << 16); }
DEVI float bfhi(unsigned u) { return __uint_as_float(u & 0xffff0000u); }
DEVI bf16x8 asbf(v4u v) { return __builtin_bit_cast(bf16x8, v); }
DEVI f32x4 mfma(v4u a, v4u b, f32x4 c) { return __builtin_amdgcn_mfma_f32_16x16x32_bf16(asbf(a), asbf(b), c, 0, 0, 0); }
DEVI v4u ldsr16(LAS char* p) { return *(LAS v4u*)p; }
DEVI float max2(float a, float b) { return __builtin_amdgcn_fmed3f(a, b, 3.4028234664e38f); }
DEVI int fresh_lane() { int v; asm volatile("v_mbcnt_lo_u32_b32 %0, -1, 0\n\tv_mbcnt_hi_u32_b32 %0, -1, %0" : "=v"(v)); return v; }
DEVI float fsel(bool c, float a, float b) { asm("" : "+v"(a)); asm("" : "+v"(b)); return c ? a : b; }
DEVI float xrow_sum(float v) {
  unsigned u = __float_as_uint(v);
  auto a = __builtin_amdgcn_permlane32_swap(u, u, false, false); v = __uint_as_float(a[0]) + __uint_as_float(a[1]);
  u = __float_as_uint(v);
  auto b = __builtin_amdgcn_permlane16_swap(u, u, false, false); return __uint_as_float(b[0]) + __uint_as_float(b[1]);
}
DEVI u64 rfl64(u64 v) { return ((u64)__builtin_amdgcn_readfirstlane((unsigned)(v >> 32)) << 32) | (unsigned)__builtin_amdgcn_readfirstlane((unsigned)v); }
DEVI void publish(unsigned char* ws, unsigned off, u64 tag) { __hip_atomic_store((gu64*)(ws + off), tag, RLX_AGENT); }
DEVI void wait_flags(unsigned char* ws, unsigned laneoff, bool active, u64 tag) {
  for (unsigned sp = 0; sp < (1u << 18); ++sp) {
    u64 v = tag;
    if (active) v = __hip_atomic_load((gu64*)(ws + laneoff), RLX_AGENT);
    if (__all(v == tag)) break;
    __builtin_amdgcn_s_sleep(POLL_SLEEP);
  }
  __builtin_amdgcn_fence(__ATOMIC_ACQUIRE, "wavefront");
  asm volatile("" ::: "memory");
}

DEVI void w2_unit_issue(const Params& p, LAS char* st, int lane, int a, int ks) {
  const v4u rs = mk_srd(p.w2, 1536u * 1280u);
  const unsigned sb = __builtin_amdgcn_readfirstlane((unsigned)(uintptr_t)st);
#pragma unroll
  for (int n = 0; n < 5; ++n) {
    const int q = n * 64 + lane, rho = (q * 6554) >> 16, ch = q - rho * 10, g = rho >> 3, t = (rho >> 2) & 1, e = rho & 3;
    dma16_asm(rs, sb + n * 1024, (unsigned)(32 * ks + 16 * t + 4 * g + e) * 1280u + 160 * a + 16 * ch);
  }
}
DEVI void w2_unit_store(const Params& p, LAS char* st, int lane, int a, int ks) {
  rsrc_t ws = mk_rsrc(p.ws, WS_END);
  const int kh = ks / 24, s = ks - kh * 24;
  const unsigned dst = WS_W2 + (unsigned)a * 245760 + (s >> 2) * 40960 + kh * 20480 + (s & 3) * 5120;
  const unsigned lo = (unsigned)(uintptr_t)st + (8 * (lane >> 4) + ((lane & 15) >> 2)) * 160 + 8 * (lane & 3);
  v2u a0, a1, a2, a3, a4, b0, b1, b2, b3, b4;
  asm volatile("ds_read_b64_tr_b16 %0, %10\n\tds_read_b64_tr_b16 %5, %10 offset:640\n\tds_read_b64_tr_b16 %1, %10 offset:32\n\tds_read_b64_tr_b16 %6, %10 offset:672\n\t"
               "ds_read_b64_tr_b16 %2, %10 offset:64\n\tds_read_b64_tr_b16 %7, %10 offset:704\n\tds_read_b64_tr_b16 %3, %10 offset:96\n\tds_read_b64_tr_b16 %8, %10 offset:736\n\t"
               "ds_read_b64_tr_b16 %4, %10 offset:128\n\tds_read_b64_tr_b16 %9, %10 offset:768\n\ts_waitcnt lgkmcnt(0)"
               : "=&v"(a0), "=&v"(a1), "=&v"(a2), "=&v"(a3), "=&v"(a4), "=&v"(b0), "=&v"(b1), "=&v"(b2), "=&v"(b3), "=&v"(b4) : "v"(lo) : "memory");
  st16(ws, v4u{a0.x, a0.y, b0.x, b0.y}, lane * 16, dst);
  st16(ws, v4u{a1.x, a1.y, b1.x, b1.y}, lane * 16, dst + 1024);
  st16(ws, v4u{a2.x, a2.y, b2.x, b2.y}, lane * 16, dst + 2048);
  st16(ws, v4u{a3.x, a3.y, b3.x, b3.y}, lane * 16, dst + 3072);
  st16(ws, v4u{a4.x, a4.y, b4.x, b4.y}, lane * 16, dst + 4096);
}
DEVI void touch_line(LAS char* lds, const GAS char* a) {
  unsigned keep;
  asm volatile("s_mov_b32 %0, m0\n\ts_mov_b32 m0, %2\n\ts_nop 0\n\tglobal_load_lds_dword %1, off\n\ts_mov_b32 m0, %0" : "=&s"(keep) : "v"(a), "s"((unsigned)(uintptr_t)(lds + L_JUNK)) : "memory");
}
DEVI const GAS char* proj_touch(const Params& p, int n, int lane) {
  int idx = n * 64 + lane; if (idx > 39679) idx = 39679;
  const int s = idx / 3968, rem = idx - s * 3968;
  if (rem < 2048) return (const GAS char*)p.x + (unsigned)rem * 1280u + s * 128;
  const int q = rem - 2048, row = q / 30;
  return (const GAS char*)p.wqkv + (unsigned)(64 * s + row) * 3840u + (q - row * 30) * 128;
}
DEVI const GAS char* weight_touch(const Params& p, int n, int lane) {
  const int idx = n * 64 + lane;
  if (idx < 6400) return (const GAS char*)p.wo + (unsigned)idx * 128u;
  if (idx < 21760) return (const GAS char*)p.w2 + (unsigned)(idx - 6400) * 128u;
  return (const GAS char*)p.w1 + (unsigned)(idx - 21760) * 128u;
}
DEVI void p1_tail(const Params& p, LAS char* lds, int lane, int w, int tt, int grp, f32x4 (&acc)[2][5]);
DEVI void phase1(const Params& p, LAS char* lds, int lane, int w, u64& tag) {
  const int x8 = blockIdx.x & 7, r = blockIdx.x >> 3;
  const int tt = 8 * (x8 >> 1) + (r >> 2), grp = 4 * (x8 & 1) + (r & 3);
  rsrc_t ws = mk_rsrc(p.ws, WS_END);
  const unsigned l16 = lane * 16;
  const int g = lane >> 4, c = lane & 15;
  f32x4 acc[2][5], st1 = {0.f, 0.f, 0.f, 0.f}, st2 = st1;
#pragma unroll
  for (int e = 0; e < 2; ++e)
#pragma unroll
    for (int b = 0; b < 5; ++b) acc[e][b] = f32x4{0.f, 0.f, 0.f, 0.f};
  const unsigned xo0 = c * 128 + ((g ^ (c & 7)) * 16), xo1 = c * 128 + (((4 + g) ^ (c & 7)) * 16);
  const unsigned sblk = (w < 5 ? w : 0) * 2048;
  unsigned wo0, wo1;
  {
    const int p4 = lane & 3, pp = (w >> 2) * 80, m = w & 3;
    wo0 = m == 0 ? 160 + 8 * p4 : m == 1 ? (p4 < 2 ? 160 : 0) + 32 + 40 * (p4 & 1) : m == 2 ? 40 + 8 * p4 : 320 + 40 + 8 * p4;
    wo1 = m == 0 ? 160 + 40 + 8 * p4 : m == 1 ? 8 * p4 : m == 2 ? 320 + 8 * p4 : 320 + 32 + 40 * (p4 & 1);
    const int q4 = (lane & 15) >> 2;
    const unsigned r0 = 10240 + (8 * g + q4) * 480 + pp;
    wo0 += r0; wo1 += r0;
  }
  constexpr int hiD = 4 * 480;
  v4u W[2][2], X[2][6];
#define P1_RO(ks) ((((ks) >> 1) % 3) * P1_STAGE)
#define P1_TR(addr) __builtin_bit_cast(v2u, __builtin_amdgcn_ds_read_tr16_b64_v4i16((LAS s16x4*)(addr)))
#define P1_READ(ks) do { \
    LAS char* wb_ = lds + P1_RO(ks) + ((ks) & 1) * 15360; \
    { v2u l0 = P1_TR(wb_ + wo0), h0 = P1_TR(wb_ + wo0 + hiD), l1 = P1_TR(wb_ + wo1), h1 = P1_TR(wb_ + wo1 + hiD); \
      W[(ks) & 1][0] = v4u{l0.x, l0.y, h0.x, h0.y}; W[(ks) & 1][1] = v4u{l1.x, l1.y, h1.x, h1.y}; } \
    LAS char* xb_ = lds + P1_RO(ks) + (((ks) & 1) ? xo1 : xo0); \
    _Pragma("unroll") for (int b = 0; b < 5; ++b) X[(ks) & 1][b] = ldsr16(xb_ + b * 2048); \
    X[(ks) & 1][5] = ldsr16(xb_ + sblk); } while (0)
  if (w == 4) { __builtin_amdgcn_s_sleep(TOUCH_SLEEP); touch_line(lds, proj_touch(p, blockIdx.x, lane)); }
  if (w == 5) { __builtin_amdgcn_s_sleep(TOUCH2_SLEEP); touch_line(lds, proj_touch(p, blockIdx.x + 256, lane)); }
  const int wstage = 1 + (blockIdx.x >> 3) % WT_SPREAD;
  if (w == 3) {
    const u64 nonce = __hip_atomic_load((gu64*)(p.ws + WS_NONCE), RLX_AGENT);
    if (lane == 0) *(LAS u64*)(lds + L_TAG) = nonce * 6364136223846793005ull + 1442695040888963407ull;
  }
  lbar();
  tag = rfl64(*(LAS u64*)(lds + L_TAG));
  P1_READ(0);
  SCHED0();
  const v4u ones = {0x3f803f80u, 0x3f803f80u, 0x3f803f80u, 0x3f803f80u};
#pragma unroll
  for (int ks = 0; ks < 20; ++ks) {
    if ((ks & 1) && ks < 19) lbar();
    if (ks == 0 && w == 5 && blockIdx.x + 512 < 620) touch_line(lds, proj_touch(p, blockIdx.x + 512, fresh_lane()));
    if ((ks & 1) && ks < 2 * WT_SPREAD + 1 && w == 6 && wstage == (ks + 1) / 2) {
      const int ln = fresh_lane();
      touch_line(lds, weight_touch(p, blockIdx.x, ln)); touch_line(lds, weight_touch(p, blockIdx.x + 256, ln));
      if (blockIdx.x + 512 < 580) touch_line(lds, weight_touch(p, blockIdx.x + 512, ln));
    }
    if (ks == 19) lbar();
    if (ks + 1 < 20) P1_READ(ks + 1);
    if (ks == 18) {
      st1 = mfma(ones, X[0][5], st1); st2 = mfma(X[0][5], X[0][5], st2);
      st1 = mfma(ones, X[1][5], st1); st2 = mfma(X[1][5], X[1][5], st2);
    }
    v4u x0 = X[ks & 1][0];
    if (c == 0) x0 = ones;
#pragma unroll
    for (int b = 0; b < 5; ++b) {
      const v4u xb = b == 0 ? x0 : X[ks & 1][b];
      acc[0][b] = mfma(W[ks & 1][0], xb, acc[0][b]); acc[1][b] = mfma(W[ks & 1][1], xb, acc[1][b]);
    }
    if (ks < 18) {
      st1 = mfma(ones, X[ks & 1][5], st1); st2 = mfma(X[ks & 1][5], X[ks & 1][5], st2);
      __builtin_amdgcn_sched_group_barrier(0x100, 4, 0);
#pragma unroll
      for (int b = 0; b < 6; ++b) { __builtin_amdgcn_sched_group_barrier(0x8, 2, 0); __builtin_amdgcn_sched_group_barrier(0x100, 1, 0); }
    }
    SCHED0();
    if (ks == 18) {
      if (w < 5) {
        const float mean = st1[0] * (1.f / 640.f);
        const float e2 = fsel((c & 2) != 0, fsel((c & 1) != 0, st2[3], st2[2]), fsel((c & 1) != 0, st2[1], st2[0])) * (1.f / 640.f);
        const float rstd = __builtin_amdgcn_rsqf(fmaxf(e2 - mean * mean, 0.f) + 1e-5f);
        if (g == (c >> 2)) *(LAS v2u*)(lds + L_P1_ST + (w * 16 + c) * 8) = v2u{__float_as_uint(rstd), __float_as_uint(-mean * rstd)};
      }
    }
  }
#undef P1_RO
#undef P1_TR
#undef P1_READ
  p1_tail(p, lds, fresh_lane(), w, tt, grp, acc);
}
DEVI void p1_tail(const Params& p, LAS char* lds, int lane, int w, int tt, int grp, f32x4 (&acc)[2][5]) {
  rsrc_t ws = mk_rsrc(p.ws, WS_END);
  const unsigned l16 = lane * 16;
  const int g = lane >> 4, c = lane & 15;
  {
    v2u sv[5];
#pragma unroll
    for (int b = 0; b < 5; ++b) sv[b] = *(LAS v2u*)(lds + L_P1_ST + (b * 16 + c) * 8);
    float cs[2][4];
#pragma unroll
    for (int e = 0; e < 2; ++e)
#pragma unroll
      for (int q = 0; q < 4; ++q) cs[e][q] = __uint_as_float(__builtin_amdgcn_update_dpp(0u, __float_as_uint(acc[e][0][q]), 0x150, 0xf, 0xf, false));
    LAS char* d = lds + L_P1_EX + c * P1_RS + w * 64 + g * 8;
#pragma unroll
    for (int b = 0; b < 5; ++b) {
      const float rs = __uint_as_float(sv[b].x), mr = __uint_as_float(sv[b].y);
#pragma unroll
      for (int e = 0; e < 2; ++e)
        *(LAS v2u*)(d + b * 16 * P1_RS + e * 32) = v2u{pk2(fmaf(rs, acc[e][b][0], mr * cs[e][0]), fmaf(rs, acc[e][b][1], mr * cs[e][1])),
                                                       pk2(fmaf(rs, acc[e][b][2], mr * cs[e][2]), fmaf(rs, acc[e][b][3], mr * cs[e][3]))};
    }
  }
  lbar();
  const int blk = 4 * tt + (w & 3), pr = 2 * grp + (w >> 2);
  LAS char* own = lds + L_P1_EX + (((w & 3) + 1) * 16 + c) * P1_RS + (w >> 2) * 256 + g * 8;
  LAS char* trp = lds + L_P1_EX + (((w & 3) + 1) * 16 + 4 * g + (c >> 2)) * P1_RS + (w >> 2) * 256 + (c & 3) * 8;
  v2u ka[2], kp[2], qa[2], qr[2], va[2], vp[2];
  const v2u kr = *(LAS v2u*)(own + 64), krp = *(LAS v2u*)(own + 64 - 16 * P1_RS);
#pragma unroll
  for (int hh = 0; hh < 2; ++hh) {
    ka[hh] = *(LAS v2u*)(own + hh * 32); kp[hh] = *(LAS v2u*)(own + hh * 32 - 16 * P1_RS);
    qa[hh] = *(LAS v2u*)(own + (3 + hh) * 32);
    qr[hh] = *(LAS v2u*)(own - g * 8 + 64 + 16 + hh * 8);
    va[hh] = __builtin_bit_cast(v2u, __builtin_amdgcn_ds_read_tr16_b64_v4i16((LAS s16x4*)(trp + (5 + hh) * 32)));
    vp[hh] = __builtin_bit_cast(v2u, __builtin_amdgcn_ds_read_tr16_b64_v4i16((LAS s16x4*)(trp + (5 + hh) * 32 - 16 * P1_RS)));
  }
  const v2u vr = __builtin_bit_cast(v2u, __builtin_amdgcn_ds_read_tr16_b64_v4i16((LAS s16x4*)(trp + 7 * 32)));
  const v2u vrp = __builtin_bit_cast(v2u, __builtin_amdgcn_ds_read_tr16_b64_v4i16((LAS s16x4*)(trp + 7 * 32 - 16 * P1_RS)));
  SCHED0();
  const int odd = c & 1, ce = c - odd;
  const bool havePrev = (tt > 0) || ((w & 3) > 0);
  const bool v1a = havePrev && (4 * g >= ce + 2), v1b = havePrev && (4 * g >= ce), v2a = ce >= 4 * g, v2b = ce - 2 >= 4 * g;
  const float c2 = 0.22360679774997896f * 1.4426950408889634f, NEG = -3.0e38f;
  const f32x4 z = {0.f, 0.f, 0.f, 0.f};
  f32x4 S1[2], S2[2];
#pragma unroll
  for (int hh = 0; hh < 2; ++hh) {
    const bool mine = g == hh;
    const v4u Q = {qa[hh].x, qa[hh].y, mine ? qr[hh].x : 0u, mine ? qr[hh].y : 0u};
    S1[hh] = mfma(v4u{kp[hh].x, kp[hh].y, krp.x, krp.y}, Q, z);
    S2[hh] = mfma(v4u{ka[hh].x, ka[hh].y, kr.x, kr.y}, Q, z);
  }
  float s1a[2], s1b[2], s2a[2], s2b[2], m[2], sum[2];
#pragma unroll
  for (int hh = 0; hh < 2; ++hh) {
    s1a[hh] = fsel(v1a, fsel(odd, S1[hh][1], S1[hh][0]), NEG); s1b[hh] = fsel(v1b, fsel(odd, S1[hh][3], S1[hh][2]), NEG);
    s2a[hh] = fsel(v2a, fsel(odd, S2[hh][1], S2[hh][0]), NEG); s2b[hh] = fsel(v2b, fsel(odd, S2[hh][3], S2[hh][2]), NEG);
    m[hh] = max2(max2(s1a[hh], s1b[hh]), max2(s2a[hh], s2b[hh]));
  }
  {
    auto a0 = __builtin_amdgcn_permlane32_swap(__float_as_uint(m[0]), __float_as_uint(m[0]), false, false);
    auto a1 = __builtin_amdgcn_permlane32_swap(__float_as_uint(m[1]), __float_as_uint(m[1]), false, false);
    m[0] = max2(__uint_as_float(a0[0]), __uint_as_float(a0[1])); m[1] = max2(__uint_as_float(a1[0]), __uint_as_float(a1[1]));
    auto b0 = __builtin_amdgcn_permlane16_swap(__float_as_uint(m[0]), __float_as_uint(m[0]), false, false);
    auto b1 = __builtin_amdgcn_permlane16_swap(__float_as_uint(m[1]), __float_as_uint(m[1]), false, false);
    m[0] = max2(__uint_as_float(b0[0]), __uint_as_float(b0[1])); m[1] = max2(__uint_as_float(b1[0]), __uint_as_float(b1[1]));
  }
  f32x4 OA[2], OR[2];
  const unsigned keep = odd ? 0xffff0000u : 0x0000ffffu;
#pragma unroll
  for (int hh = 0; hh < 2; ++hh) {
    const float mc = -m[hh] * c2;
    s1a[hh] = __builtin_amdgcn_exp2f(fmaf(s1a[hh], c2, mc)); s1b[hh] = __builtin_amdgcn_exp2f(fmaf(s1b[hh], c2, mc));
    s2a[hh] = __builtin_amdgcn_exp2f(fmaf(s2a[hh], c2, mc)); s2b[hh] = __builtin_amdgcn_exp2f(fmaf(s2b[hh], c2, mc));
    const v4u P = {pk2(s1a[hh], s1a[hh]) & keep, pk2(s1b[hh], s1b[hh]) & keep, pk2(s2a[hh], s2a[hh]) & keep, pk2(s2b[hh], s2b[hh]) & keep};
    OA[hh] = mfma(v4u{vp[hh].x, vp[hh].y, va[hh].x, va[hh].y}, P, z);
    OR[hh] = mfma(v4u{vrp.x, vrp.y, vr.x, vr.y}, P, z);
    sum[hh] = (s1a[hh] + s1b[hh]) + (s2a[hh] + s2b[hh]);
  }
  {
    auto a0 = __builtin_amdgcn_permlane32_swap(__float_as_uint(sum[0]), __float_as_uint(sum[0]), false, false);
    auto a1 = __builtin_amdgcn_permlane32_swap(__float_as_uint(sum[1]), __float_as_uint(sum[1]), false, false);
    sum[0] = __uint_as_float(a0[0]) + __uint_as_float(a0[1]); sum[1] = __uint_as_float(a1[0]) + __uint_as_float(a1[1]);
    auto b0 = __builtin_amdgcn_permlane16_swap(__float_as_uint(sum[0]), __float_as_uint(sum[0]), false, false);
    auto b1 = __builtin_amdgcn_permlane16_swap(__float_as_uint(sum[1]), __float_as_uint(sum[1]), false, false);
    sum[0] = __builtin_amdgcn_rcpf(__uint_as_float(b0[0]) + __uint_as_float(b0[1])); sum[1] = __builtin_amdgcn_rcpf(__uint_as_float(b1[0]) + __uint_as_float(b1[1]));
  }
  {
    v4u om = {pk2(OA[0][0] * sum[0], OA[0][1] * sum[0]), pk2(OA[0][2] * sum[0], OA[0][3] * sum[0]),
              pk2(OA[1][0] * sum[1], OA[1][1] * sum[1]), pk2(OA[1][2] * sum[1], OA[1][3] * sum[1])};
    st16(ws, om, l16, WS_O + (unsigned)blk * 20480 + pr * 1024);
    unsigned r0x = pk2(OR[0][0] * sum[0], OR[0][1] * sum[0]), r0y = pk2(OR[0][2] * sum[0], OR[0][3] * sum[0]);
    unsigned r1x = pk2(OR[1][0] * sum[1], OR[1][1] * sum[1]), r1y = pk2(OR[1][2] * sum[1], OR[1][3] * sum[1]);
    auto t0 = __builtin_amdgcn_permlane16_swap(r1x, r1x, false, false); auto t1 = __builtin_amdgcn_permlane16_swap(r1y, r1y, false, false);
    v4u orr = {r0x, r0y, t0[1], t1[1]};
    if (lane < 16) st16(ws, orr, l16, WS_O + (unsigned)blk * 20480 + (16 + (pr >> 2)) * 1024 + (pr & 3) * 256);
  }
}

DEVI void exchange_halves(LAS char* lds, const f32x4 (&acc)[5], f32x4 (&fin)[3], int lane, int w) {
  LAS char* d = lds + L_EXH + (w & 3) * 5120 + lane * 16;
  if (w < 4) { *(LAS f32x4*)(d + 3072) = acc[3]; *(LAS f32x4*)(d + 4096) = acc[4]; }
  else { *(LAS f32x4*)(d) = acc[0]; *(LAS f32x4*)(d + 1024) = acc[1]; *(LAS f32x4*)(d + 2048) = acc[2]; }
  lbar();
  if (w < 4) { f32x4 u0 = *(LAS f32x4*)(d), u1 = *(LAS f32x4*)(d + 1024), u2 = *(LAS f32x4*)(d + 2048); fin[0] = acc[0] + u0; fin[1] = acc[1] + u1; fin[2] = acc[2] + u2; }
  else { f32x4 u0 = *(LAS f32x4*)(d + 3072), u1 = *(LAS f32x4*)(d + 4096); fin[0] = acc[3] + u0; fin[1] = acc[4] + u1; fin[2] = f32x4{0.f, 0.f, 0.f, 0.f}; }
}

DEVI void phase2(const Params& p, LAS char* lds, int lane, int w) {
  const int x8 = blockIdx.x & 7, r = blockIdx.x >> 3;
  const int ti = 2 * x8 + (r >> 4), hb = (r >> 3) & 1, j = r & 7;
  rsrc_t ws = mk_rsrc(p.ws, WS_END);
  const v4u srd = mk_srd(p.ws, WS_END);
  const unsigned l16 = lane * 16;
  const int b = w & 3, kh = w >> 2, blk = 8 * ti + 4 * hb + b;
  rbar();
  v4u a[10];
  const unsigned oa = WS_O + (unsigned)blk * 20480 + 10 * kh * 1024;
#pragma unroll
  for (int s = 0; s < 10; ++s) aload(a[s], srd, l16, oa + s * 1024);
  f32x4 acc[5];
#pragma unroll
  for (int t = 0; t < 5; ++t) acc[t] = f32x4{0.f, 0.f, 0.f, 0.f};
  v4u B[2][5];
  LAS char* bb = lds + kh * 51200 + l16;
#pragma unroll
  for (int t = 0; t < 5; ++t) B[0][t] = ldsr16(bb + t * 1024);
  SCHED0();
#pragma unroll
  for (int s = 0; s < 10; ++s) {
    if (s == 5) lbar();
    asm volatile("s_waitcnt vmcnt(%1)" : "+v"(a[s]) : "i"(9 - s) : "memory");
    if (s + 1 < 10) {
#pragma unroll
      for (int t = 0; t < 5; ++t) B[(s + 1) & 1][t] = ldsr16(bb + (s + 1) * 5120 + t * 1024);
    }
#pragma unroll
    for (int t = 0; t < 5; ++t) acc[t] = mfma(B[s & 1][t], a[s], acc[t]);
#pragma unroll
    for (int t = 0; t < 5; ++t) { __builtin_amdgcn_sched_group_barrier(0x100, 1, 0); __builtin_amdgcn_sched_group_barrier(0x8, 1, 0); }
    SCHED0();
  }
  const int g = lane >> 4, c = lane & 15, row = 16 * b + c, mt0 = 3 * kh;
  LAS char* sx = lds + L_STASH + row * 160 + mt0 * 32 + g * 8;
  v2u xv[3];
#pragma unroll
  for (int e = 0; e < 3; ++e) xv[e] = *(LAS v2u*)(sx + (e < 2 || kh == 0 ? e : 0) * 32);
  f32x4 fin[3];
  exchange_halves(lds, acc, fin, lane, w);
  v2u hv[3];
  float ssum = 0.f, ssq = 0.f;
#pragma unroll
  for (int e = 0; e < 3; ++e) {
    hv[e] = v2u{pk2(fin[e][0] + bflo(xv[e].x), fin[e][1] + bfhi(xv[e].x)), pk2(fin[e][2] + bflo(xv[e].y), fin[e][3] + bfhi(xv[e].y))};
    if (e < 2 || kh == 0) {
      *(LAS v2u*)(sx + e * 32) = hv[e];
      const float h0 = bflo(hv[e].x), h1 = bfhi(hv[e].x), h2 = bflo(hv[e].y), h3 = bfhi(hv[e].y);
      ssum += (h0 + h1) + (h2 + h3); ssq += fmaf(h0, h0, h1 * h1) + fmaf(h2, h2, h3 * h3);
    }
  }
  {
    auto sx0 = __builtin_amdgcn_permlane16_swap(hv[0].x, hv[1].x, false, false); auto sy0 = __builtin_amdgcn_permlane16_swap(hv[0].y, hv[1].y, false, false);
    const int G = 2 * (mt0 + (g & 1)) + (g >> 1);
    st16(ws, v4u{sx0[0], sy0[0], sx0[1], sy0[1]}, c * 16 + (10 * j + G) * 256, WS_H + (unsigned)blk * 20480);
    if (kh == 0) __builtin_amdgcn_raw_buffer_store_b64(hv[2], ws, WS_H + (unsigned)blk * 20480 + (10 * j + 4 + (g >> 1)) * 256 + c * 16 + (g & 1) * 8, 0, 16);
  }
  ssum = xrow_sum(ssum); ssq = xrow_sum(ssq);
  {
    const int idx = 2 * j + kh;
    if (lane < 16) {
      v2u sv = {__float_as_uint(ssum), __float_as_uint(ssq)};
      __builtin_amdgcn_raw_buffer_store_b64(sv, ws, WS_ST + (unsigned)blk * 2048 + idx * 128 + c * 8, 0, 16);
    }
  }
}

DEVI void phase3(const Params& p, LAS char* lds, int lane, int w) {
  const int x8 = blockIdx.x & 7, r = blockIdx.x >> 3;
  const int ti = 4 * (x8 >> 1) + (r >> 3), jj = 8 * (x8 & 1) + (r & 7);
  rsrc_t ws = mk_rsrc(p.ws, WS_END);
  const v4u srd = mk_srd(p.ws, WS_END);
  const unsigned l16 = lane * 16;
  const int blk = 8 * ti + w;
  rbar();
  if (w < 6) {
    v4u cf[20];
#pragma unroll
    for (int ks = 0; ks < 20; ++ks) cf[ks] = ldsr16(lds + (ks * 6 + w) * 1024 + l16);
    SCHED0();
    f32x4 cs0 = {0.f, 0.f, 0.f, 0.f}, cs1 = cs0;
    const v4u ones = {0x3f803f80u, 0x3f803f80u, 0x3f803f80u, 0x3f803f80u};
#pragma unroll
    for (int ks = 0; ks < 20; ks += 2) { cs0 = mfma(cf[ks], ones, cs0); cs1 = mfma(cf[ks + 1], ones, cs1); }
    if ((lane & 15) == 0) *(LAS f32x4*)(lds + L_P3_B1 + 1024 + (w * 16 + (lane >> 4) * 4) * 4) = cs0 + cs1;
  }
  lbar();
  v4u a[20];
  v2u st[4];
  const unsigned sto = WS_ST + (unsigned)blk * 2048 + (lane >> 4) * 512 + (lane & 15) * 8;
  const unsigned ha = WS_H + (unsigned)blk * 20480;
#pragma unroll
  for (int s = 0; s < P3D; ++s) aload(a[s], srd, l16, ha + s * 1024);
  f32x4 acc[6];
#pragma unroll
  for (int t = 0; t < 6; ++t) acc[t] = f32x4{0.f, 0.f, 0.f, 0.f};
  v4u B[2][4];
  LAS char* bb = lds + l16;
#define P3_RD(s, t) ldsr16(bb + (s) * 6144 + (t) * 1024)
#pragma unroll
  for (int t = 0; t < 4; ++t) B[0][t] = P3_RD(0, t);
  SCHED0();
#pragma unroll
  for (int s = 0; s < 20; ++s) {
    asm volatile("s_waitcnt vmcnt(%1)" : "+v"(a[s]) : "i"(s + P3D - 1 < 24 ? P3D - 1 : 23 - s) : "memory");
    if (s + P3D < 20) aload(a[s + P3D], srd, l16, ha + (s + P3D) * 1024);
    else if (s + P3D < 24) asm volatile("buffer_load_dwordx2 %0, %1, %2, 0 offen sc1" : "=v"(st[s + P3D - 20]) : "v"(sto + (s + P3D - 20) * 128), "s"(srd) : "memory");
    if (s + 1 < 20) {
#pragma unroll
      for (int t = 0; t < 4; ++t) B[(s + 1) & 1][t] = P3_RD(s + 1, t);
    }
#pragma unroll
    for (int t = 0; t < 4; ++t) acc[t] = mfma(B[s & 1][t], a[s], acc[t]);
#pragma unroll
    for (int t = 0; t < 4; ++t) { __builtin_amdgcn_sched_group_barrier(0x100, 1, 0); __builtin_amdgcn_sched_group_barrier(0x8, 1, 0); }
    SCHED0();
  }
  lbar();
  const int g = lane >> 4;
  f32x4 cs[6]; v2u bb1[6];
#pragma unroll
  for (int t = 0; t < 4; ++t) { cs[t] = *(LAS f32x4*)(lds + L_P3_B1 + 1024 + (t * 16 + g * 4) * 4); bb1[t] = *(LAS v2u*)(lds + L_P3_B1 + (t * 16 + g * 4) * 2); }
  v4u C[2][4];
#define P3_RD2(s2) do { C[(s2) & 1][0] = P3_RD(2 * (s2), 4); C[(s2) & 1][1] = P3_RD(2 * (s2), 5); C[(s2) & 1][2] = P3_RD(2 * (s2) + 1, 4); C[(s2) & 1][3] = P3_RD(2 * (s2) + 1, 5); } while (0)
  P3_RD2(0);
  WAITV4(0, st[0], st[1], st[2], st[3]);
  SCHED0();
  float s1 = (__uint_as_float(st[0].x) + __uint_as_float(st[1].x)) + (__uint_as_float(st[2].x) + __uint_as_float(st[3].x));
  float s2 = (__uint_as_float(st[0].y) + __uint_as_float(st[1].y)) + (__uint_as_float(st[2].y) + __uint_as_float(st[3].y));
  s1 = xrow_sum(s1); s2 = xrow_sum(s2);
  const float mean = s1 * (1.f / 640.f);
  const float var = fmaxf(s2 * (1.f / 640.f) - mean * mean, 0.f);
  const float rstd = __builtin_amdgcn_rsqf(var + 1e-5f), mr = -mean * rstd;
#define P3_OUT(t, o0, o1) do { \
    const float y0 = fmaxf(fmaf(rstd, acc[t][0], fmaf(mr, cs[t][0], bflo(bb1[t].x))), 0.f), y1 = fmaxf(fmaf(rstd, acc[t][1], fmaf(mr, cs[t][1], bfhi(bb1[t].x))), 0.f); \
    const float y2 = fmaxf(fmaf(rstd, acc[t][2], fmaf(mr, cs[t][2], bflo(bb1[t].y))), 0.f), y3 = fmaxf(fmaf(rstd, acc[t][3], fmaf(mr, cs[t][3], bfhi(bb1[t].y))), 0.f); \
    o0 = pk2(y0 * y0, y1 * y1); o1 = pk2(y2 * y2, y3 * y3); } while (0)
  const unsigned h1o = WS_H1 + (unsigned)blk * 49152 + 3 * jj * 1024;
#pragma unroll
  for (int t = 4; t < 6; ++t) { cs[t] = *(LAS f32x4*)(lds + L_P3_B1 + 1024 + (t * 16 + g * 4) * 4); bb1[t] = *(LAS v2u*)(lds + L_P3_B1 + (t * 16 + g * 4) * 2); }
  v4u o0, o1;
  SCHED0();
#pragma unroll
  for (int s2 = 0; s2 < 10; ++s2) {
    if (s2 + 1 < 10) P3_RD2(s2 + 1);
    acc[4] = mfma(C[s2 & 1][0], a[2 * s2], acc[4]); acc[5] = mfma(C[s2 & 1][1], a[2 * s2], acc[5]);
    acc[4] = mfma(C[s2 & 1][2], a[2 * s2 + 1], acc[4]); acc[5] = mfma(C[s2 & 1][3], a[2 * s2 + 1], acc[5]);
    if (s2 == 0) P3_OUT(0, o0[0], o0[1]);
    if (s2 == 1) { P3_OUT(1, o0[2], o0[3]); st16(ws, o0, l16, h1o); }
    if (s2 == 2) P3_OUT(2, o1[0], o1[1]);
    if (s2 == 3) { P3_OUT(3, o1[2], o1[3]); st16(ws, o1, l16, h1o + 1024); }
#pragma unroll
    for (int t = 0; t < 4; ++t) {
      __builtin_amdgcn_sched_group_barrier(0x100, 1, 0); __builtin_amdgcn_sched_group_barrier(0x8, 1, 0);
      if (s2 < 4) __builtin_amdgcn_sched_group_barrier(0x2, 8, 0);
    }
    SCHED0();
  }
#undef P3_RD2
#undef P3_RD
  {
    v4u o2;
    P3_OUT(4, o2[0], o2[1]); P3_OUT(5, o2[2], o2[3]);
    st16(ws, o2, l16, h1o + 2048);
  }
  lbar();
#undef P3_OUT
}

constexpr unsigned w2_slot(int sl, int f) { return sl == 2 ? (f >> 1) * 6144 + (4 + (f & 1)) * 1024 : (10 * sl + (f >> 2)) * 6144 + (f & 3) * 1024; }
DEVI void phase4(const Params& p, LAS char* lds, int lane, int w) {
  const int x8 = blockIdx.x & 7, r = blockIdx.x >> 3;
  const int ti = 2 * x8 + (r >> 4), hb = (r >> 3) & 1, j = r & 7;
  const v4u srd = mk_srd(p.ws, WS_END);
  const unsigned l16 = lane * 16;
  const int b = w & 3, kh = w >> 2, blk = 8 * ti + 4 * hb + b;
  rbar();
  const unsigned ha = WS_H1 + (unsigned)blk * 49152 + kh * 24 * 1024;
  v4u a[24];
#pragma unroll
  for (int s = 0; s < P4D; ++s) aload(a[s], srd, l16, ha + s * 1024);
  f32x4 acc[5];
#pragma unroll
  for (int t = 0; t < 5; ++t) acc[t] = f32x4{0.f, 0.f, 0.f, 0.f};
  v4u B[2][5];
  LAS char* bb01 = lds + kh * 5 * 6144 + l16;
  LAS char* bb2 = lds + kh * 10 * 6144 + l16;
#define P4_READ(s) do { _Pragma("unroll") for (int t = 0; t < 5; ++t) B[(s) & 1][t] = \
    ldsr16((((s) >> 2) % 3 == 2 ? bb2 : bb01) + w2_slot(((s) >> 2) % 3, ((s) & 3) * 5 + t)); } while (0)
  P4_READ(0);
  SCHED0();
#pragma unroll
  for (int s = 0; s < 24; ++s) {
    if ((s & 3) == 3 && s < 23) lbar();
    asm volatile("s_waitcnt vmcnt(%1)" : "+v"(a[s]) : "i"(s + P4D - 1 < 24 ? P4D - 1 : 23 - s) : "memory");
    if (s + P4D < 24) aload(a[s + P4D], srd, l16, ha + (s + P4D) * 1024);
    if (s + 1 < 24) P4_READ(s + 1);
#pragma unroll
    for (int t = 0; t < 5; ++t) acc[t] = mfma(B[s & 1][t], a[s], acc[t]);
#pragma unroll
    for (int t = 0; t < 5; ++t) { __builtin_amdgcn_sched_group_barrier(0x100, 1, 0); __builtin_amdgcn_sched_group_barrier(0x8, 1, 0); }
    SCHED0();
  }
#undef P4_READ
  const int g = lane >> 4, c = lane & 15, row = 16 * b + c, mt0 = 3 * kh;
  LAS char* sx = lds + L_STASH + row * 160 + mt0 * 32 + g * 8;
  v2u hv[3], bv[3];
#pragma unroll
  for (int e = 0; e < 3; ++e) {
    const int ee = (e < 2 || kh == 0) ? e : 0;
    hv[e] = *(LAS v2u*)(sx + ee * 32);
    bv[e] = *(LAS v2u*)(lds + L_P4_B2 + (mt0 + ee) * 32 + g * 8);
  }
  f32x4 fin[3];
  exchange_halves(lds, acc, fin, lane, w);
#pragma unroll
  for (int e = 0; e < 3; ++e) {
    if (e < 2 || kh == 0)
      *(LAS v2u*)(sx + e * 32) = v2u{pk2(fin[e][0] + bflo(bv[e].x) + bflo(hv[e].x), fin[e][1] + bfhi(bv[e].x) + bfhi(hv[e].x)),
                                     pk2(fin[e][2] + bflo(bv[e].y) + bflo(hv[e].y), fin[e][3] + bfhi(bv[e].y) + bfhi(hv[e].y))};
  }
  lbar();
  {
    const int q0 = w * 64 + lane, q1 = q0 + 512;
    const v4u o0 = *(LAS v4u*)(lds + L_STASH + q0 * 16);
    v4u o1 = o0; if (w < 2) o1 = *(LAS v4u*)(lds + L_STASH + q1 * 16);
    const int r0 = q0 / 10, r1 = q1 / 10;
    rsrc_t outr = mk_rsrc(p.out, T_ * 1280);
    const unsigned ob = (unsigned)(128 * ti + 64 * hb) * 1280 + 160 * j;
    __builtin_amdgcn_raw_buffer_store_b128(o0, outr, ob + r0 * 1280 + (q0 - r0 * 10) * 16, 0, 0);
    if (w < 2) __builtin_amdgcn_raw_buffer_store_b128(o1, outr, ob + r1 * 1280 + (q1 - r1 * 10) * 16, 0, 0);
  }
}

DEVI unsigned ldsaddr(LAS char* p) { return (unsigned)(uintptr_t)p; }
DEVI void convert5(LAS char* u, int lane) {
  const int g = lane >> 4;
  const unsigned lo = ldsaddr(u) + (8 * g + 4 * (g & 1) + ((lane & 15) >> 2)) * 160 + (lane & 3) * 8, hi = lo + ((g & 1) ? -640 : 640), d = ldsaddr(u) + lane * 16;
  v2u a0, a1, a2, a3, a4, b0, b1, b2, b3, b4;
  asm volatile("ds_read_b64_tr_b16 %0, %10\n\tds_read_b64_tr_b16 %5, %11\n\tds_read_b64_tr_b16 %1, %10 offset:32\n\tds_read_b64_tr_b16 %6, %11 offset:32\n\t"
               "ds_read_b64_tr_b16 %2, %10 offset:64\n\tds_read_b64_tr_b16 %7, %11 offset:64\n\tds_read_b64_tr_b16 %3, %10 offset:96\n\tds_read_b64_tr_b16 %8, %11 offset:96\n\t"
               "ds_read_b64_tr_b16 %4, %10 offset:128\n\tds_read_b64_tr_b16 %9, %11 offset:128\n\ts_waitcnt lgkmcnt(0)"
               : "=&v"(a0), "=&v"(a1), "=&v"(a2), "=&v"(a3), "=&v"(a4), "=&v"(b0), "=&v"(b1), "=&v"(b2), "=&v"(b3), "=&v"(b4) : "v"(lo), "v"(hi) : "memory");
  const v4u f0 = {a0.x, a0.y, b0.x, b0.y}, f1 = {a1.x, a1.y, b1.x, b1.y}, f2 = {a2.x, a2.y, b2.x, b2.y}, f3 = {a3.x, a3.y, b3.x, b3.y}, f4 = {a4.x, a4.y, b4.x, b4.y};
  asm volatile("ds_write_b128 %0, %1\n\tds_write_b128 %0, %2 offset:1024\n\tds_write_b128 %0, %3 offset:2048\n\tds_write_b128 %0, %4 offset:3072\n\tds_write_b128 %0, %5 offset:4096\n\ts_waitcnt lgkmcnt(0)"
               ::"v"(d), "v"(f0), "v"(f1), "v"(f2), "v"(f3), "v"(f4) : "memory");
}
DEVI void convert6(LAS char* u, int lane) {
  const int g = lane >> 4, od = g & 1;
  const unsigned row = ldsaddr(u) + (8 * g + ((lane & 15) >> 2)) * 192 + (lane & 3) * 8, lo = row + 32 * od, l5 = row + (od ? 0 : 160), d = ldsaddr(u) + lane * 16;
  v2u a0, a1, a2, a3, a4, a5, b0, b1, b2, b3, b4, b5;
  asm volatile("ds_read_b64_tr_b16 %0, %12\n\tds_read_b64_tr_b16 %6, %12 offset:768\n\tds_read_b64_tr_b16 %1, %12 offset:32\n\tds_read_b64_tr_b16 %7, %12 offset:800\n\t"
               "ds_read_b64_tr_b16 %2, %12 offset:64\n\tds_read_b64_tr_b16 %8, %12 offset:832\n\tds_read_b64_tr_b16 %3, %12 offset:96\n\tds_read_b64_tr_b16 %9, %12 offset:864\n\t"
               "ds_read_b64_tr_b16 %4, %12 offset:128\n\tds_read_b64_tr_b16 %10, %12 offset:896\n\tds_read_b64_tr_b16 %5, %13\n\tds_read_b64_tr_b16 %11, %13 offset:768\n\ts_waitcnt lgkmcnt(0)"
               : "=&v"(a0), "=&v"(a1), "=&v"(a2), "=&v"(a3), "=&v"(a4), "=&v"(a5), "=&v"(b0), "=&v"(b1), "=&v"(b2), "=&v"(b3), "=&v"(b4), "=&v"(b5) : "v"(lo), "v"(l5) : "memory");
  const v4u f0 = {a0.x, a0.y, b0.x, b0.y}, f1 = {a1.x, a1.y, b1.x, b1.y}, f2 = {a2.x, a2.y, b2.x, b2.y}, f3 = {a3.x, a3.y, b3.x, b3.y}, f4 = {a4.x, a4.y, b4.x, b4.y}, f5 = {a5.x, a5.y, b5.x, b5.y};
  asm volatile("ds_write_b128 %0, %1\n\tds_write_b128 %0, %2 offset:1024\n\tds_write_b128 %0, %3 offset:2048\n\tds_write_b128 %0, %4 offset:3072\n\tds_write_b128 %0, %5 offset:4096\n\t"
               "ds_write_b128 %0, %6 offset:5120\n\ts_waitcnt lgkmcnt(0)"
               ::"v"(d), "v"(f0), "v"(f1), "v"(f2), "v"(f3), "v"(f4), "v"(f5) : "memory");
}

DEVI void loader(const Params& p, LAS char* lds, int lane, int lw) {
  const int x8 = blockIdx.x & 7, r = blockIdx.x >> 3;
  const int t2d = 4 * (x8 >> 1) + (r >> 3), c2d = 8 * (x8 & 1) + (r & 7);
  const int tl = 2 * x8 + (r >> 4), hb = (r >> 3) & 1, j = r & 7;
  rsrc_t ws = mk_rsrc(p.ws, WS_END);
  const v4u srd = mk_srd(p.ws, WS_END);
  const unsigned l16 = lane * 16;
  __builtin_amdgcn_s_setprio(LPRIO);
  const int tt = 8 * (x8 >> 1) + (r >> 2), grp = 4 * (x8 & 1) + (r & 3);
  rsrc_t xs = mk_rsrc(p.x, T_ * 1280), wqs = mk_rsrc(p.wqkv, 640u * 3840u);
  unsigned vx[3], vw[8];
#pragma unroll
  for (int i = 0; i < 3; ++i) {
    const int m = lw + 4 * i, row = 8 * m + (lane >> 3);
    int tok = 64 * tt - 16 + row; if (tok < 0) tok = 0; if (tok > T_ - 1) tok = T_ - 1;
    vx[i] = (unsigned)tok * 1280 + (((lane & 7) ^ (row & 7)) * 16);
  }
  const int wn0 = lw < 2 ? lw * 7 - 1 : 14 + (lw - 2) * 8;
#pragma unroll
  for (int i = 0; i < 8; ++i) {
    int n = wn0 + i; if (n < 0) n = 0;
    const int q = n * 64 + lane, rho = (q * 2185) >> 16, ch = q - rho * 30, sec = (ch * 205) >> 11;
    vw[i] = (unsigned)rho * 3840u + sec * 1280 + 160 * grp + 16 * (ch - sec * 10);
  }
#define P1_STG(s) do { LAS char* sb_ = lds + ((s) % 3) * P1_STAGE; \
    dma16<0>(xs, sb_ + lw * 1024, vx[0], (s) * 128); dma16<0>(xs, sb_ + (lw + 4) * 1024, vx[1], (s) * 128); \
    if (lw < 2) dma16<0>(xs, sb_ + (lw + 8) * 1024, vx[2], (s) * 128); else dma16<0>(wqs, sb_ + 10240 + wn0 * 1024, vw[0], (s) * 64 * 3840); \
    _Pragma("unroll") for (int i = 1; i < 8; ++i) dma16<0>(wqs, sb_ + 10240 + (wn0 + i) * 1024, vw[i], (s) * 64 * 3840); } while (0)
  P1_STG(0); P1_STG(1);
  WAITV(10); rbar();
  const u64 tag = rfl64(*(LAS u64*)(lds + L_TAG));
  if (lw == 0 && lane < 4) *((LAS unsigned*)(lds + L_CNT) + lane) = 0u;
  P1_STG(2);
  rsrc_t wos = mk_rsrc(p.wo, 640u * 1280u), w1s = mk_rsrc(p.w1, 640u * 3072u);
  unsigned vA[5], vB[5], vD[6];
#pragma unroll
  for (int i = 0; i < 5; ++i) {
    const int q = 64 * i + lane, rho = (q * 6554) >> 16, cc = q - rho * 10, g = rho >> 3, t = ((rho >> 2) & 1) ^ (g & 1), e = rho & 3;
    const unsigned col = 160 * j + 16 * cc;
    vA[i] = (unsigned)(20 * t + 4 * g + e) * 1280u + col;
    vB[i] = (unsigned)(40 * g + 20 * t + 16 + e) * 1280u + col;
  }
#pragma unroll
  for (int i = 0; i < 6; ++i) {
    const int q = 64 * i + lane, rho = (q * 5462) >> 16, pos = q - rho * 12, cc = ((rho >> 3) & 1) ? (pos < 2 ? pos + 10 : pos - 2) : pos;
    vD[i] = (unsigned)rho * 3072u + 192 * c2d + 16 * cc;
  }
#define WO_KS(ks) do { _Pragma("unroll") for (int i = 0; i < 5; ++i) { \
    if ((ks) < 16) dma16<0>(wos, lds + (ks) * 5120 + i * 1024, vA[i], (ks) * 40 * 1280); else dma16<0>(wos, lds + (ks) * 5120 + i * 1024, vB[i], ((ks) - 16) * 160 * 1280); } } while (0)
#define W1_KS(ks) do { _Pragma("unroll") for (int i = 0; i < 6; ++i) dma16<0>(w1s, lds + (ks) * 6144 + i * 1024, vD[i], (ks) * 32 * 3072); } while (0)
  const unsigned w2 = WS_W2 + (unsigned)j * 245760;
#define W2_STG(st) do { _Pragma("unroll") for (int i = 0; i < 10; ++i) { const int f = lw + 4 * i; \
    dma16<16>(ws, lds + ((st) % 3 == 2 ? (f >> 1) * 6144 + (4 + (f & 1)) * 1024 : (10 * ((st) % 3) + (f >> 2)) * 6144 + (f & 3) * 1024), l16, w2 + (st) * 40960 + f * 1024); } } while (0)
  const int uslot = blockIdx.x + 256 * (3 - lw);
  const bool haveu = lw >= 2 && uslot < 384;
  LAS char* ust = lds + L_UNIT + (lw & 1) * 5120;
#pragma unroll
  for (int q = 1; q <= 9; ++q) {
    if (q == 1) WAITL0();
    if (q >= 8 && haveu) WAITV(15); else WAITV(10);
    rbar();
    if (q + 2 <= 9) P1_STG(q + 2);
    if (q == 7 && haveu) w2_unit_issue(p, ust, lane, uslot & 7, uslot >> 3);
    if (q == 8) { WO_KS(8 + lw); WO_KS(12 + lw); }
  }
  if (haveu) { WAITV(10); w2_unit_store(p, ust, lane, uslot & 7, uslot >> 3); }
#undef P1_STG
  __builtin_amdgcn_s_setprio(0);
  rbar();
  rbar();
  WO_KS(16 + lw); WO_KS(lw); WO_KS(4 + lw);
#pragma unroll
  for (int m = 0; m < 3; ++m) {
    const int n = lw + 4 * m;
    if (n < 10) { const int q = n * 64 + lane, row = (q * 6554) >> 16, ch = q - row * 10;
      dma16<0>(xs, lds + L_STASH + n * 1024, (unsigned)(128 * tl + 64 * hb + row) * 1280 + 160 * j + 16 * ch, 0); }
  }
  if (lw < 2) WAITV(18); else WAITV(17);
  if (haveu && lane == 0) publish(p.ws, WS_F0B + ((uslot & 7) * 64 + (uslot >> 3)) * 8, tag);
  convert5(lds + (8 + lw) * 5120, lane); convert5(lds + (12 + lw) * 5120, lane);
  WAITV(0);
  convert5(lds + (16 + lw) * 5120, lane); convert5(lds + lw * 5120, lane); convert5(lds + (4 + lw) * 5120, lane);
  if (lw == 0) wait_flags(p.ws, WS_F1 + (tl * 16 + hb * 8 + lane) * FSTR, lane < 8, tag);
  lbar();
  if (lw == 0) { rsrc_t bs = mk_rsrc(p.b1, F_ * 2); dma16<0>(bs, lds + L_P3_B1, lane < 12 ? 192 * c2d + lane * 16 : 0x7fffffffu, 0); }
  if (lw < 3) W1_KS(17 + lw);
  rbar();
  W1_KS(lw); W1_KS(9 + lw); if (lw == 3) W1_KS(4);
  rbar();
  W1_KS(5 + lw); W1_KS(13 + lw);
  WAITV(12);
  if (lw < 3) convert6(lds + (17 + lw) * 6144, lane);
  convert6(lds + lw * 6144, lane); convert6(lds + (9 + lw) * 6144, lane); if (lw == 3) convert6(lds + 4 * 6144, lane);
  WAITV(0);
  convert6(lds + (5 + lw) * 6144, lane); convert6(lds + (13 + lw) * 6144, lane);
  lbar();
  if (lw == 0) wait_flags(p.ws, WS_F2 + (t2d * 16 + lane) * FSTR, lane < 16, tag);
  rbar();
  wait_flags(p.ws, WS_F0B + (j * 64 + lane) * 8, lane < 48, tag);
  if (lw == 0) { rsrc_t bs = mk_rsrc(p.b2, D_ * 2); dma16<0>(bs, lds + L_P4_B2, lane < 10 ? 160 * j + lane * 16 : 0x7fffffffu, 0); }
  if (blockIdx.x == 0 && lw == 1) {
    for (int q = 0; q < 4; ++q) wait_flags(p.ws, WS_F1 + (q * 64 + lane) * FSTR, true, tag);
    if (lane == 0) publish(p.ws, WS_NONCE, tag);
  }
  rbar();
  W2_STG(0); W2_STG(1);
  rbar();
  W2_STG(2);
  if (lw == 0) wait_flags(p.ws, WS_F3 + (tl * 16 + lane) * FSTR, lane < 16, tag);
  WAITV(0); rbar();
  rbar(); W2_STG(3);
  rbar(); W2_STG(4);
  WAITV(10); rbar(); W2_STG(5);
  WAITV(10); rbar();
  WAITV(0); rbar();
#undef W2_STG
#undef WO_KS
#undef W1_KS
  rbar();
  rbar();
}

DEVI void wg_publish(const Params& p, LAS char* lds, int k, int lane, unsigned off, u64 tag) {
  WAITV(0);
  if (lane == 0) {
    const unsigned old = __hip_atomic_fetch_add((LAS unsigned*)(lds + L_CNT) + k, 1u, __ATOMIC_RELAXED, __HIP_MEMORY_SCOPE_WORKGROUP);
    if (old == 7u) publish(p.ws, off, tag);
  }
}

__global__ __launch_bounds__(768, 3) __attribute__((amdgpu_waves_per_eu(3, 3))) void fused_block(Params p) {
  extern __shared__ __attribute__((aligned(16))) char smem[];
  LAS char* lds = (LAS char*)smem;
  const int lane = threadIdx.x & 63, w = __builtin_amdgcn_readfirstlane(threadIdx.x >> 6);
  const int x8 = blockIdx.x & 7, r = blockIdx.x >> 3;
  u64 tag = 0;
  if (__builtin_expect(w >= 8, 1)) loader(p, lds, lane, w - 8);
  else {
    __builtin_amdgcn_s_setprio(CPRIO);
    const int t2d = 4 * (x8 >> 1) + (r >> 3), c2d = 8 * (x8 & 1) + (r & 7), tl = 2 * x8 + (r >> 4);
    phase1(p, lds, lane, w, tag);
    { const int tt = 8 * (x8 >> 1) + (r >> 2), grp = 4 * (x8 & 1) + (r & 3);
      wg_publish(p, lds, 0, fresh_lane(), WS_F1 + ((tt >> 1) * 16 + (tt & 1) * 8 + grp) * FSTR, tag); }
    phase2(p, lds, fresh_lane(), w);
    wg_publish(p, lds, 1, fresh_lane(), WS_F2 + (tl * 16 + (r & 15)) * FSTR, tag);
    phase3(p, lds, fresh_lane(), w);
    wg_publish(p, lds, 2, fresh_lane(), WS_F3 + (t2d * 16 + c2d) * FSTR, tag);
    phase4(p, lds, fresh_lane(), w);
  }
}

extern "C" void kernel_launch(void* const* d_in, const int* in_sizes, int n_in, void* d_out, int out_size, void* d_ws, size_t ws_size, hipStream_t stream) {
  Params p;
  p.x = (const unsigned short*)d_in[0]; p.wqkv = (const unsigned short*)d_in[1]; p.wo = (const unsigned short*)d_in[2];
  p.w1 = (const unsigned short*)d_in[4]; p.b1 = (const unsigned short*)d_in[5]; p.w2 = (const unsigned short*)d_in[6]; p.b2 = (const unsigned short*)d_in[7];
  p.out = (unsigned short*)d_out; p.ws = (unsigned char*)d_ws;
  (void)hipFuncSetAttribute((const void*)fused_block, hipFuncAttributeMaxDynamicSharedMemorySize, LDS_BYTES);
  hipLaunchKernelGGL(fused_block, dim3(256), dim3(768), LDS_BYTES, stream, p);
}
```
